```python
import torch

def _softmax(x, axis=-1):
    return torch.softmax(x, dim=axis)
def _gelu(x):
    return torch.nn.functional.gelu(x, approximate="tanh")
def _silu(x):
    return torch.nn.functional.silu(x)
def _sigmoid(x):
    return torch.sigmoid(x)
def _layernorm(x, axis=-1, eps=1e-5):
    m = x.mean(dim=axis, keepdim=True)
    v = x.var(dim=axis, keepdim=True, unbiased=False)
    return (x - m) / torch.sqrt(v + eps)
def _scatter_add(tgt, idx, src, axis=0):
    return tgt.clone().index_add_(axis, idx, src)
def _segment_sum(src, seg, n):
    out = torch.zeros((n,) + src.shape[1:], dtype=src.dtype)
    return out.index_add_(0, seg, src)
def _pad(x, pw):
    flat = tuple(v for pair in reversed(pw) for v in pair)
    return torch.nn.functional.pad(x, flat)

kernel_name = 'recurrent_scan__gqa_block_cached__glu_gated_geglu__prompt'

def setup_inputs():

    _w = (lambda a: (lambda t: t.to(torch.bfloat16) if t.is_floating_point() else t)(torch.as_tensor(a)))
    def _A_setup():
        B, Sq, D = 1, 2048, 576
        Hq, Hkv, Sk, Dh = 32, 16, 2048, 128
        assert Hq % Hkv == 0, (Hq, Hkv)
        return {
            "x": _w((lambda *s: torch.randn(*s, dtype=torch.bfloat16) * 0.05)(B, Sq, D)),
            "k_cache": _w((lambda *s: torch.randn(*s, dtype=torch.bfloat16) * 3.2)(B, Hkv, Sk, Dh)),
            "v_cache": _w((lambda *s: torch.randn(*s, dtype=torch.bfloat16) * 3.2)(B, Hkv, Sk, Dh)),
            "wq": _w((lambda *s: torch.randn(*s, dtype=torch.bfloat16) * 3.2)(D, Hq * Dh) * (D ** -0.5)),
            "wo": _w((lambda *s: torch.randn(*s, dtype=torch.bfloat16) * 3.2)(Hq * Dh, D) * ((Hq * Dh) ** -0.5)),
        }
    _A_in = _A_setup()

    def _B_setup():
        B, S, Dm, Df = 1, 2048, 576, 4608
        return {
            "x": _w((lambda *s: torch.randn(*s, dtype=torch.bfloat16) * 0.05)(B, S, Dm)),
            "w_gate": _w((lambda *s: torch.randn(*s, dtype=torch.bfloat16) * 0.4)(Dm, Df) * (Dm ** -0.5)),
            "w_up": _w((lambda *s: torch.randn(*s, dtype=torch.bfloat16) * 0.4)(Dm, Df) * (Dm ** -0.5)),
            "w_down": _w((lambda *s: torch.randn(*s, dtype=torch.bfloat16) * 0.4)(Df, Dm) * (Df ** -0.5)),
        }
    _B_in = _B_setup()

    return {
        "A_x": _A_in["x"],
        "A_k_cache": _A_in["k_cache"],
        "A_v_cache": _A_in["v_cache"],
        "A_wq": _A_in["wq"],
        "A_wo": _A_in["wo"],
        "B_x": _B_in["x"],
        "B_w_gate": _B_in["w_gate"],
        "B_w_up": _B_in["w_up"],
        "B_w_down": _B_in["w_down"],
    }

def reference(A_x, A_k_cache, A_v_cache, A_wq, A_wo, B_x, B_w_gate, B_w_up, B_w_down):
    def _A_ref(x, k_cache, v_cache, wq, wo):
        B, Sq, D = x.shape
        Hq, Dh = 32, 128
        Hkv = k_cache.shape[1]
        rep = Hq // Hkv
        q = (lambda x, *d: x.permute(*d))((x @ wq).reshape(B, Sq, Hq, Dh), 0, 2, 1, 3)
        k = (lambda x, n, axis: torch.repeat_interleave(x, n, dim=axis))(k_cache, rep, axis=1)
        v = (lambda x, n, axis: torch.repeat_interleave(x, n, dim=axis))(v_cache, rep, axis=1)
        scores = torch.einsum("bhqd,bhkd->bhqk", q, k) * (Dh ** -0.5)
        p = _softmax(scores, axis=-1)
        o = torch.einsum("bhqk,bhkd->bhqd", p, v)
        return (lambda x, *d: x.permute(*d))(o, 0, 2, 1, 3).reshape(B, Sq, Hq * Dh) @ wo

    def _B_ref(x, w_gate, w_up, w_down):
        g = torch.einsum("bsd,df->bsf", x, w_gate)
        u = torch.einsum("bsd,df->bsf", x, w_up)
        h = _gelu(g) * u
        return torch.einsum("bsf,fd->bsd", h, w_down)

    x = A_x
    xn = _layernorm(x, axis=-1)
    s = _A_ref(xn, A_k_cache, A_v_cache, A_wq, A_wo)
    g = _silu(_B_ref(xn, B_w_gate, B_w_up, B_w_down))
    return x + s * g
```

```cpp
#include <hip/hip_runtime.h>
#include <hip/hip_bf16.h>
typedef unsigned short u16;
__device__ inline float b2f(u16 v){ return __uint_as_float(((unsigned)v)<<16); }
__device__ inline u16 f2b(float f){ __bf16 b=(__bf16)f; return __builtin_bit_cast(u16,b); }
constexpr int S=2048,D=576,HQ=32,HKV=16,DH=128,DF=4608;
__global__ void ln_k(const u16* x,u16* xn){
  int r=blockIdx.x; __shared__ float red[64];
  float v[9]; float s=0; for(int i=0;i<9;i++){v[i]=b2f(x[r*D+threadIdx.x+64*i]); s+=v[i];}
  for(int o=32;o;o>>=1) s+=__shfl_xor(s,o); float m=s/D; float q=0; for(int i=0;i<9;i++){float d=v[i]-m;q+=d*d;}
  for(int o=32;o;o>>=1) q+=__shfl_xor(q,o); float rs=rsqrtf(q/D+1e-5f);
  for(int i=0;i<9;i++) xn[r*D+threadIdx.x+64*i]=f2b((v[i]-m)*rs);
}
__global__ void gemm_k(const u16* A,const u16* B,float* C,int M,int N,int K){
  __shared__ float As[64][33]; __shared__ float Bs[32][65];
  int bm=blockIdx.y*64,bn=blockIdx.x*64; int tx=threadIdx.x&15,ty=threadIdx.x>>4;
  float acc[4][4]={};
  for(int k0=0;k0<K;k0+=32){
    for(int i=threadIdx.x;i<64*32;i+=256){int r=i>>5,c=i&31;As[r][c]=b2f(A[(long)(bm+r)*K+k0+c]);}
    for(int i=threadIdx.x;i<64*32;i+=256){int r=i>>6,c=i&63;Bs[r][c]=b2f(B[(long)(k0+r)*N+bn+c]);}
    __syncthreads();
    for(int k=0;k<32;k++){float a[4],b[4];for(int i=0;i<4;i++)a[i]=As[ty*4+i][k];for(int j=0;j<4;j++)b[j]=Bs[k][tx*4+j];
      for(int i=0;i<4;i++)for(int j=0;j<4;j++)acc[i][j]+=a[i]*b[j];}
    __syncthreads();
  }
  for(int i=0;i<4;i++)for(int j=0;j<4;j++)C[(long)(bm+ty*4+i)*N+bn+tx*4+j]=acc[i][j];
}
__global__ void attn_k(const float* Qf,const u16* Kc,const u16* Vc,u16* O){
  int h=blockIdx.y,q0=blockIdx.x*16; __shared__ float q[16][128]; __shared__ float sc[16][2048];
  for(int i=threadIdx.x;i<16*128;i+=256){int r=i>>7,c=i&127;q[r][c]=b2f(f2b(Qf[(long)(q0+r)*4096+h*128+c]))*0.08838834764f;}
  __syncthreads();
  const u16* K=Kc+(long)(h/2)*S*DH; const u16* V=Vc+(long)(h/2)*S*DH;
  for(int k=threadIdx.x;k<S;k+=256){float a[16]={};for(int d=0;d<128;d++){float kv=b2f(K[(long)k*128+d]);for(int r=0;r<16;r++)a[r]+=q[r][d]*kv;}for(int r=0;r<16;r++)sc[r][k]=a[r];}
  __syncthreads();
  int w=threadIdx.x>>6,l=threadIdx.x&63;
  for(int r=w;r<16;r+=4){float m=-1e30f;for(int k=l;k<S;k+=64)m=fmaxf(m,sc[r][k]);for(int o=32;o;o>>=1)m=fmaxf(m,__shfl_xor(m,o));
    float s=0;for(int k=l;k<S;k+=64){float e=__expf(sc[r][k]-m);sc[r][k]=e;s+=e;}for(int o=32;o;o>>=1)s+=__shfl_xor(s,o);float is=1.f/s;for(int k=l;k<S;k+=64)sc[r][k]*=is;}
  __syncthreads();
  int d=threadIdx.x&127,rh=threadIdx.x>>7; float a[8]={};
  for(int k=0;k<S;k++){float v=b2f(V[(long)k*128+d]);for(int r=0;r<8;r++)a[r]+=sc[rh*8+r][k]*v;}
  for(int r=0;r<8;r++)O[(long)(q0+rh*8+r)*4096+h*128+d]=f2b(a[r]);
}
__global__ void glu_k(const float* g,const float* u,u16* H,int n){int i=blockIdx.x*256+threadIdx.x;if(i<n){float x=g[i];float t=tanhf(0.7978845608f*(x+0.044715f*x*x*x));H[i]=f2b(0.5f*x*(1+t)*u[i]);}}
__global__ void fin_k(const u16* x,const float* s,const float* g,u16* out,int n){int i=blockIdx.x*256+threadIdx.x;if(i<n){float gg=g[i];out[i]=f2b(b2f(x[i])+s[i]*gg/(1+__expf(-gg)));}}
extern "C" void kernel_launch(void* const* d_in,const int* in_sizes,int n_in,void* d_out,int out_size,void* d_ws,size_t ws_size,hipStream_t stream){
  const u16* x=(const u16*)d_in[0];const u16* K=(const u16*)d_in[1];const u16* V=(const u16*)d_in[2];const u16* wq=(const u16*)d_in[3];const u16* wo=(const u16*)d_in[4];
  const u16* wg=(const u16*)d_in[6];const u16* wu=(const u16*)d_in[7];const u16* wd=(const u16*)d_in[8];
  char* ws=(char*)d_ws; u16* xn=(u16*)ws; float* Q=(float*)(ws+(4<<20)); float* G=(float*)(ws+(40<<20)); float* U=(float*)(ws+(80<<20));
  u16* O=(u16*)(ws+(120<<20)); u16* H=(u16*)(ws+(140<<20)); float* s=(float*)(ws+(160<<20)); float* g=(float*)(ws+(170<<20));
  ln_k<<<S,64,0,stream>>>(x,xn);
  gemm_k<<<dim3(4096/64,S/64),256,0,stream>>>(xn,wq,Q,S,4096,D);
  gemm_k<<<dim3(DF/64,S/64),256,0,stream>>>(xn,wg,G,S,DF,D);
  gemm_k<<<dim3(DF/64,S/64),256,0,stream>>>(xn,wu,U,S,DF,D);
  attn_k<<<dim3(S/16,HQ),256,0,stream>>>(Q,K,V,O);
  glu_k<<<S*DF/256,256,0,stream>>>(G,U,H,S*DF);
  gemm_k<<<dim3(D/64,S/64),256,0,stream>>>(O,wo,s,S,D,4096);
  gemm_k<<<dim3(D/64,S/64),256,0,stream>>>(H,wd,g,S,D,DF);
  fin_k<<<S*D/256,256,0,stream>>>(x,s,g,(u16*)d_out,S*D);
}
```

```cpp
#include <hip/hip_runtime.h>
extern "C" __global__ __launch_bounds__(256,1) void kf(void* ws,const void* K,const void* V,const void* wq,const void* wg,const void* wu,const void* wo,const void* wd,const void* x,void* out){
  __shared__ __attribute__((aligned(16))) char lds[163840];
  asm volatile(
"s_load_dwordx4 s[40:43], s[4:5], 64\n"
"s_load_dwordx16 s[64:79], s[4:5], 0\n"
"v_lshrrev_b32 v1, 6, v0\n"
"v_and_b32 v0, 63, v0\n"
"s_and_b32 s34, s6, 7\n"
"v_readfirstlane_b32 s52, v1\n"
"s_lshr_b32 s35, s6, 3\n"
"s_lshl_b32 s34, s34, 2\n"
"s_and_b32 s53, s35, 3\n"
"s_add_u32 s53, s53, s34\n"
"s_xor_b32 s53, s53, 4\n"
"s_mov_b32 s39, 0x20000\n"
"s_cmp_eq_u32 s52, 3\n"
"s_cbranch_scc0 L_w0\n"
"s_getpc_b64 s[36:37]\n"
"L_pc:\n"
"s_mov_b32 s38, L_end-L_pc\n"
"s_and_b32 s37, s37, 0xffff\n"
"s_bfe_u32 s34, s6, 0x50003\n"
"s_lshl_b32 s34, s34, 12\n"
"v_lshlrev_b32 v54, 6, v0\n"
"buffer_load_dword v53, v54, s[36:39], s34 offen\n"
"v_lshrrev_b32 v41, 4, v0\n"
"v_and_b32 v45, 15, v0\n"
"v_lshlrev_b32 v46, 2, v41\n"
"v_xor_b32 v46, v46, v45\n"
"v_lshlrev_b32 v46, 4, v46\n"
"v_lshl_add_u32 v1, v41, 13, v46\n"
"s_lshr_b32 s80, s6, 5\n"
"s_mul_i32 s80, s80, 9\n"
"s_lshr_b32 s80, s80, 1\n"
"s_lshl_b32 s60, s53, 8\n"
"s_and_b32 s90, s6, 1\n"
"s_lshl_b32 s90, s90, 4\n"
"s_waitcnt lgkmcnt(0)\n"
"s_load_dwordx2 s[100:101], s[64:65], 0\n"
"s_mov_b32 s12, s70\n"
"s_and_b32 s13, s71, 0xffff\n"
"s_mov_b32 s14, 0x480000\n"
"s_mov_b32 s15, 0x20000\n"
"s_mov_b32 s81, 5\n"
"L_ua:\n"
"s_add_u32 s34, s80, s90\n"
"s_sub_u32 m0, s34, 36\n"
"s_min_u32 s34, s34, m0\n"
"s_lshl_b32 m0, s34, 12\n"
"s_lshl_b32 s59, s80, 17\n"
"s_add_u32 s59, s59, s60\n"
"buffer_load_dwordx4 v1, s[12:15], s59 offen lds\n"
"s_add_u32 s59, s59, 31744\n"
"buffer_load_dwordx4 v1, s[12:15], s59 offen offset:1024 lds\n"
"s_add_u32 s59, s59, 31744\n"
"buffer_load_dwordx4 v1, s[12:15], s59 offen offset:2048 lds\n"
"s_add_u32 s59, s59, 31744\n"
"buffer_load_dwordx4 v1, s[12:15], s59 offen offset:3072 lds\n"
"s_add_u32 s80, s80, 1\n"
"s_cmp_ge_u32 s80, 36\n"
"s_cselect_b32 s34, 36, 0\n"
"s_sub_u32 s80, s80, s34\n"
"s_sub_u32 s81, s81, 1\n"
"s_cbranch_scc0 L_ua\n"
"s_branch L_sa\n"
"L_w0:\n"
"v_and_b32 v44, 7, v0\n"
"v_mul_u32_u24 v104, 1152, v44\n"
"v_lshrrev_b32 v46, 5, v0\n"
"v_lshl_add_u32 v48, v46, 6, v104\n"
"v_and_b32 v46, 24, v0\n"
"v_add_u32 v48, v46, v48\n"
"s_andn2_b32 s34, s6, 31\n"
"s_add_u32 s34, s34, s53\n"
"s_mul_i32 s34, s34, 9216\n"
"s_mul_i32 s35, s52, 384\n"
"s_add_u32 s35, s35, s34\n"
"s_mov_b32 s38, 0x240000\n"
"s_waitcnt lgkmcnt(0)\n"
"s_mov_b32 s36, s40\n"
"s_and_b32 s37, s41, 0xffff\n"
"buffer_load_ushort v104, v104, s[36:39], s34 offen\n"
"buffer_load_dwordx2 v[64:65], v48, s[36:39], s35 offen offset:0\n"
"buffer_load_dwordx2 v[66:67], v48, s[36:39], s35 offen offset:32\n"
"buffer_load_dwordx2 v[68:69], v48, s[36:39], s35 offen offset:128\n"
"buffer_load_dwordx2 v[70:71], v48, s[36:39], s35 offen offset:160\n"
"buffer_load_dwordx2 v[72:73], v48, s[36:39], s35 offen offset:256\n"
"buffer_load_dwordx2 v[74:75], v48, s[36:39], s35 offen offset:288\n"
"s_load_dwordx2 s[100:101], s[64:65], 0\n"
"s_mov_b32 s12, s70\n"
"s_and_b32 s13, s71, 0xffff\n"
"s_mov_b32 s14, 0x480000\n"
"s_mov_b32 s15, 0x20000\n"
"v_lshrrev_b32 v41, 4, v0\n"
"v_and_b32 v45, 15, v0\n"
"v_lshlrev_b32 v46, 2, v41\n"
"v_xor_b32 v46, v46, v45\n"
"v_lshlrev_b32 v46, 4, v46\n"
"v_lshl_add_u32 v1, v41, 13, v46\n"
"s_lshr_b32 s80, s6, 5\n"
"s_mul_i32 s80, s80, 9\n"
"s_lshr_b32 s80, s80, 1\n"
"s_lshl_b32 s60, s53, 8\n"
"s_and_b32 s90, s6, 1\n"
"s_lshl_b32 s90, s90, 4\n"
"s_add_u32 s80, s80, s52\n"
"s_add_u32 s80, s80, 16\n"
"s_cmp_ge_u32 s80, 36\n"
"s_cselect_b32 s34, 36, 0\n"
"s_sub_u32 s80, s80, s34\n"
"L_sa:\n"
"v_bfe_u32 v42, v0, 2, 2\n"
"v_and_b32 v43, 3, v0\n"
"v_lshlrev_b32 v47, 4, v0\n"
"s_lshr_b32 s35, s6, 5\n"
"s_lshl_b32 s35, s35, 4\n"
"s_lshl2_add_u32 s54, s52, s35\n"
"s_lshl_b32 s84, s52, 10\n"
"s_add_u32 s84, s84, 158208\n"
"s_mov_b32 s8, s64\n"
"s_and_b32 s9, s65, 0xffff\n"
"s_mov_b32 s10, 0x8000000\n"
"s_mov_b32 s11, 0x20000\n"
"s_mov_b32 s16, s72\n"
"s_and_b32 s17, s73, 0xffff\n"
"s_mov_b32 s18, 0x510000\n"
"s_mov_b32 s19, 0x20000\n"
"s_mov_b32 s20, s74\n"
"s_and_b32 s21, s75, 0xffff\n"
"s_mov_b32 s22, 0x510000\n"
"s_mov_b32 s23, 0x20000\n"
"s_mov_b32 s24, s66\n"
"s_and_b32 s25, s67, 0xffff\n"
"s_mov_b32 s26, 0x80000\n"
"s_mov_b32 s27, 0x20000\n"
"s_mov_b32 s28, s68\n"
"s_and_b32 s29, s69, 0xffff\n"
"s_mov_b32 s30, 0x80000\n"
"s_mov_b32 s31, 0x20000\n"
"s_lshr_b32 s34, s53, 1\n"
"s_lshl_b32 s35, s34, 19\n"
"s_add_u32 s24, s24, s35\n"
"s_addc_u32 s25, s25, 0\n"
"s_add_u32 s28, s28, s35\n"
"s_addc_u32 s29, s29, 0\n"
"s_mov_b32 s36, s64\n"
"s_and_b32 s37, s65, 0xffff\n"
"s_mov_b32 s38, 0x0\n"
"s_mov_b32 s39, 0x20000\n"
"s_add_u32 s36, s36, 0x1400000\n"
"s_addc_u32 s37, s37, 0\n"
"s_mov_b32 s87, 0x1200000\n"
"s_mov_b32 s63, 0xc2469614\n"
"s_mov_b32 s33, 0x3c853526\n"
"v_mov_b32 v3, 0xc1906ebb\n"
"s_and_b32 s34, s53, 3\n"
"s_mul_i32 s35, s34, 5\n"
"s_lshl2_add_u32 s34, s34, 20\n"
"s_cmp_eq_u32 s90, 0\n"
"s_cselect_b32 s97, s35, s34\n"
"s_cselect_b32 s88, 0, 73728\n"
"s_cselect_b32 s89, 0, -147456\n"
"s_cselect_b32 s62, 0, 1\n"
"s_add_u32 s61, s89, 73728\n"
"s_add_u32 s89, s89, 16384\n"
"s_bfe_u32 s34, s6, 0x20001\n"
"s_mul_i32 s34, s34, 36\n"
"s_add_u32 s97, s97, s34\n"
"s_lshl_b32 s86, s97, 6\n"
"s_sub_u32 s97, s97, 1\n"
"s_mul_i32 s35, s52, 1327104\n"
"s_add_u32 s86, s86, s35\n"
"s_mov_b32 s85, 0\n"
"s_mul_i32 s92, s52, 4224\n"
"v_lshl_add_u32 v46, v41, 2, v42\n"
"v_xor_b32 v44, 0, v42\n"
"v_lshlrev_b32 v44, 6, v44\n"
"v_lshl_add_u32 v44, v46, 8, v44\n"
"v_lshl_add_u32 v32, v43, 4, v44\n"
"v_lshl_add_u32 v32, s90, 12, v32\n"
"v_xor_b32 v44, 1, v42\n"
"v_lshlrev_b32 v44, 6, v44\n"
"v_lshl_add_u32 v44, v46, 8, v44\n"
"v_lshl_add_u32 v33, v43, 4, v44\n"
"v_lshl_add_u32 v33, s90, 12, v33\n"
"v_xor_b32 v44, 2, v42\n"
"v_lshlrev_b32 v44, 6, v44\n"
"v_lshl_add_u32 v44, v46, 8, v44\n"
"v_lshl_add_u32 v34, v43, 4, v44\n"
"v_lshl_add_u32 v34, s90, 12, v34\n"
"v_xor_b32 v44, 3, v42\n"
"v_lshlrev_b32 v44, 6, v44\n"
"v_lshl_add_u32 v44, v46, 8, v44\n"
"v_lshl_add_u32 v35, v43, 4, v44\n"
"v_lshl_add_u32 v35, s90, 12, v35\n"
"v_and_b32 v44, 1, v41\n"
"v_lshlrev_b32 v44, 5, v44\n"
"v_lshl_add_u32 v44, v46, 6, v44\n"
"v_lshl_add_u32 v36, v43, 3, v44\n"
"v_add_u32 v36, s88, v36\n"
"v_add_u32 v37, 36864, v36\n"
"v_xor_b32 v38, 32, v36\n"
"v_xor_b32 v39, 32, v37\n"
"v_and_b32 v44, 1, v41\n"
"v_lshlrev_b32 v44, 1, v44\n"
"v_xor_b32 v44, v44, v43\n"
"v_lshlrev_b32 v44, 4, v44\n"
"v_mul_u32_u24 v2, 9216, v46\n"
"v_add_u32 v2, v2, v44\n"
"v_mul_u32_u24 v44, 1056, v45\n"
"v_lshl_add_u32 v27, v41, 4, v44\n"
"v_add_u32 v26, 141312, v27\n"
"v_add_u32 v24, 73728, v27\n"
"v_mul_u32_u24 v44, 1056, v46\n"
"v_lshl_add_u32 v44, v43, 3, v44\n"
"v_add_u32 v25, 73728, v44\n"
"v_mul_u32_u24 v44, 1056, v42\n"
"v_lshl_add_u32 v44, v43, 8, v44\n"
"v_lshl_add_u32 v44, v41, 4, v44\n"
"v_add_u32 v40, 147456, v44\n"
"v_lshrrev_b32 v44, 1, v0\n"
"v_mul_u32_u24 v44, 1152, v44\n"
"v_and_b32 v28, 1, v0\n"
"v_lshl_add_u32 v28, v28, 4, v44\n"
"v_lshlrev_b32 v44, 3, v0\n"
"v_add_u32 v29, s84, v44\n"
"v_xor_b32 v44, v41, v43\n"
"v_and_b32 v44, 2, v44\n"
"v_cmp_eq_u32 vcc, 0, v44\n"
"v_mov_b32 v44, 0x3f803f80\n"
"s_nop 1\n"
"v_cndmask_b32 v4, 0, v44, vcc\n"
"v_cndmask_b32 v5, 0, v44, vcc\n"
"v_cndmask_b32 v6, 0, v44, vcc\n"
"v_cndmask_b32 v7, 0, v44, vcc\n"
"s_mul_i32 s34, s62, -12\n"
"s_add_u32 s34, s34, 59\n"
"s_mul_i32 s35, s62, 819\n"
"s_add_u32 s35, s35, 3277\n"
"s_lshl_b32 s98, s62, 8\n"
"s_add_u32 s98, s98, 7936\n"
"v_min_u32 v44, s34, v0\n"
"v_mul_u32_u24 v46, s35, v44\n"
"v_lshrrev_b32 v46, 16, v46\n"
"v_mul_u32_u24 v46, s98, v46\n"
"v_lshl_add_u32 v60, v44, 6, v46\n"
"v_add_u32 v61, 27648, v60\n"
"v_add_u32 v62, 55296, v60\n"
"v_lshlrev_b32 v43, 6, v0\n"
"v_and_b32 v44, 7, v0\n"
"v_lshrrev_b32 v46, 3, v0\n"
"v_lshlrev_b32 v46, 8, v46\n"
"v_lshl_add_u32 v49, v44, 4, v46\n"
"v_lshlrev_b32 v44, 3, v44\n"
"v_add_u32 v51, 162368, v44\n"
"s_lshl_b32 s34, s52, 6\n"
"v_add_u32 v52, s34, v51\n"
"v_and_b32 v44, 3, v0\n"
"v_min_u32 v44, 2, v44\n"
"v_and_b32 v46, 28, v0\n"
"v_lshlrev_b32 v46, 0, v46\n"
"v_add_lshl_u32 v50, v44, v46, 3\n"
"v_mov_b32 v0, v47\n"
"s_andn2_b32 s34, s6, 31\n"
"s_add_u32 s34, s34, s53\n"
"s_lshr_b32 s35, s34, 1\n"
"s_mul_i32 s35, s35, 18432\n"
"s_and_b32 s34, s34, 1\n"
"s_lshl_b32 s34, s34, 7\n"
"s_add_u32 s35, s35, s34\n"
"s_mul_i32 s34, s52, 6144\n"
"s_add_u32 s35, s35, s34\n"
"s_add_u32 s98, s35, 0x10000\n"
"s_add_u32 s99, s98, 4096\n"
"s_cmp_eq_u32 s52, 3\n"
"s_cbranch_scc0 L_ln0\n"
"s_barrier\n"
"s_mov_b32 s81, 9\n"
"L_ub:\n"
"s_add_u32 s34, s80, s90\n"
"s_sub_u32 m0, s34, 36\n"
"s_min_u32 s34, s34, m0\n"
"s_lshl_b32 m0, s34, 12\n"
"s_lshl_b32 s59, s80, 17\n"
"s_add_u32 s59, s59, s60\n"
"buffer_load_dwordx4 v1, s[12:15], s59 offen lds\n"
"s_add_u32 s59, s59, 31744\n"
"buffer_load_dwordx4 v1, s[12:15], s59 offen offset:1024 lds\n"
"s_add_u32 s59, s59, 31744\n"
"buffer_load_dwordx4 v1, s[12:15], s59 offen offset:2048 lds\n"
"s_add_u32 s59, s59, 31744\n"
"buffer_load_dwordx4 v1, s[12:15], s59 offen offset:3072 lds\n"
"s_add_u32 s80, s80, 1\n"
"s_cmp_ge_u32 s80, 36\n"
"s_cselect_b32 s34, 36, 0\n"
"s_sub_u32 s80, s80, s34\n"
"s_sub_u32 s81, s81, 1\n"
"s_cbranch_scc0 L_ub\n"
"s_add_u32 s80, s80, 3\n"
"s_cmp_ge_u32 s80, 36\n"
"s_cselect_b32 s34, 36, 0\n"
"s_sub_u32 s80, s80, s34\n"
"s_branch L_ln4\n"
"L_ln0:\n"
"s_waitcnt vmcnt(0)\n"
"v_lshlrev_b32 v104, 16, v104\n"
"v_xor_b32 v104, 0x80000000, v104\n"
"v_mov_b32 v105, v104\n"
"v_lshlrev_b32 v80, 16, v64\n"
"v_and_b32 v81, 0xffff0000, v64\n"
"v_lshlrev_b32 v82, 16, v65\n"
"v_and_b32 v83, 0xffff0000, v65\n"
"v_lshlrev_b32 v84, 16, v66\n"
"v_and_b32 v85, 0xffff0000, v66\n"
"v_lshlrev_b32 v86, 16, v67\n"
"v_and_b32 v87, 0xffff0000, v67\n"
"v_lshlrev_b32 v88, 16, v68\n"
"v_and_b32 v89, 0xffff0000, v68\n"
"v_lshlrev_b32 v90, 16, v69\n"
"v_and_b32 v91, 0xffff0000, v69\n"
"v_lshlrev_b32 v92, 16, v70\n"
"v_and_b32 v93, 0xffff0000, v70\n"
"v_lshlrev_b32 v94, 16, v71\n"
"v_and_b32 v95, 0xffff0000, v71\n"
"v_lshlrev_b32 v96, 16, v72\n"
"v_and_b32 v97, 0xffff0000, v72\n"
"v_lshlrev_b32 v98, 16, v73\n"
"v_and_b32 v99, 0xffff0000, v73\n"
"v_lshlrev_b32 v100, 16, v74\n"
"v_and_b32 v101, 0xffff0000, v74\n"
"v_lshlrev_b32 v102, 16, v75\n"
"v_and_b32 v103, 0xffff0000, v75\n"
"v_pk_add_f32 v[80:81], v[80:81], v[104:105]\n"
"v_pk_add_f32 v[82:83], v[82:83], v[104:105]\n"
"v_pk_add_f32 v[84:85], v[84:85], v[104:105]\n"
"v_pk_add_f32 v[86:87], v[86:87], v[104:105]\n"
"v_pk_add_f32 v[88:89], v[88:89], v[104:105]\n"
"v_pk_add_f32 v[90:91], v[90:91], v[104:105]\n"
"v_pk_add_f32 v[92:93], v[92:93], v[104:105]\n"
"v_pk_add_f32 v[94:95], v[94:95], v[104:105]\n"
"v_pk_add_f32 v[96:97], v[96:97], v[104:105]\n"
"v_pk_add_f32 v[98:99], v[98:99], v[104:105]\n"
"v_pk_add_f32 v[100:101], v[100:101], v[104:105]\n"
"v_pk_add_f32 v[102:103], v[102:103], v[104:105]\n"
"v_pk_add_f32 v[54:55], v[80:81], v[82:83]\n"
"v_pk_add_f32 v[58:59], v[84:85], v[86:87]\n"
"v_pk_mul_f32 v[56:57], v[80:81], v[80:81]\n"
"v_pk_mul_f32 v[106:107], v[82:83], v[82:83]\n"
"v_pk_fma_f32 v[56:57], v[84:85], v[84:85], v[56:57]\n"
"v_pk_fma_f32 v[106:107], v[86:87], v[86:87], v[106:107]\n"
"v_pk_add_f32 v[54:55], v[54:55], v[88:89]\n"
"v_pk_fma_f32 v[56:57], v[88:89], v[88:89], v[56:57]\n"
"v_pk_add_f32 v[58:59], v[58:59], v[90:91]\n"
"v_pk_fma_f32 v[106:107], v[90:91], v[90:91], v[106:107]\n"
"v_pk_add_f32 v[54:55], v[54:55], v[92:93]\n"
"v_pk_fma_f32 v[56:57], v[92:93], v[92:93], v[56:57]\n"
"v_pk_add_f32 v[58:59], v[58:59], v[94:95]\n"
"v_pk_fma_f32 v[106:107], v[94:95], v[94:95], v[106:107]\n"
"v_pk_add_f32 v[54:55], v[54:55], v[96:97]\n"
"v_pk_fma_f32 v[56:57], v[96:97], v[96:97], v[56:57]\n"
"v_pk_add_f32 v[58:59], v[58:59], v[98:99]\n"
"v_pk_fma_f32 v[106:107], v[98:99], v[98:99], v[106:107]\n"
"v_pk_add_f32 v[54:55], v[54:55], v[100:101]\n"
"v_pk_fma_f32 v[56:57], v[100:101], v[100:101], v[56:57]\n"
"v_pk_add_f32 v[58:59], v[58:59], v[102:103]\n"
"v_pk_fma_f32 v[106:107], v[102:103], v[102:103], v[106:107]\n"
"v_pk_add_f32 v[54:55], v[54:55], v[58:59]\n"
"v_pk_add_f32 v[56:57], v[56:57], v[106:107]\n"
"v_add_f32 v54, v54, v55\n"
"v_add_f32 v55, v56, v57\n"
"s_nop 0\n"
"v_add_f32_dpp v54, v54, v54 row_ror:8 row_mask:0xf bank_mask:0xf\n"
"v_add_f32_dpp v55, v55, v55 row_ror:8 row_mask:0xf bank_mask:0xf\n"
"v_mov_b32 v56, v54\n"
"v_mov_b32 v57, v55\n"
"s_nop 0\n"
"v_permlane32_swap_b32 v54, v56\n"
"v_permlane32_swap_b32 v55, v57\n"
"v_pk_add_f32 v[54:55], v[54:55], v[56:57]\n"
"v_mov_b32 v56, v54\n"
"v_mov_b32 v57, v55\n"
"s_nop 0\n"
"v_permlane16_swap_b32 v54, v56\n"
"v_permlane16_swap_b32 v55, v57\n"
"v_pk_add_f32 v[54:55], v[54:55], v[56:57]\n"
"ds_write_b64 v52, v[54:55]\n"
"s_waitcnt lgkmcnt(0)\n"
"s_barrier\n"
"ds_read_b64 v[54:55], v51 offset:0\n"
"ds_read_b64 v[56:57], v51 offset:64\n"
"ds_read_b64 v[58:59], v51 offset:128\n"
"s_waitcnt lgkmcnt(0)\n"
"v_pk_add_f32 v[54:55], v[54:55], v[56:57]\n"
"v_pk_add_f32 v[54:55], v[54:55], v[58:59]\n"
"v_mul_f32 v54, 0x3ae38e39, v54\n"
"v_mul_f32 v55, 0x3ae38e39, v55\n"
"v_fma_f32 v55, -v54, v54, v55\n"
"v_max_f32 v55, 0, v55\n"
"v_add_f32 v55, 0x3727c5ac, v55\n"
"v_rsq_f32 v55, v55\n"
"s_nop 0\n"
"v_mul_f32 v56, 0x3e0293ee, v55\n"
"v_mul_f32 v57, 0x3e0293ee, v55\n"
"v_mul_f32 v58, -v54, v56\n"
"v_mul_f32 v59, -v54, v56\n"
"v_pk_fma_f32 v[80:81], v[80:81], v[56:57], v[58:59]\n"
"v_pk_fma_f32 v[82:83], v[82:83], v[56:57], v[58:59]\n"
"v_pk_fma_f32 v[84:85], v[84:85], v[56:57], v[58:59]\n"
"v_pk_fma_f32 v[86:87], v[86:87], v[56:57], v[58:59]\n"
"v_cvt_pk_bf16_f32 v64, v80, v81\n"
"v_cvt_pk_bf16_f32 v65, v82, v83\n"
"v_cvt_pk_bf16_f32 v66, v84, v85\n"
"v_cvt_pk_bf16_f32 v67, v86, v87\n"
"buffer_store_dwordx4 v[64:67], v49, s[8:11], s98 offen offset:0 sc1\n"
"v_pk_fma_f32 v[88:89], v[88:89], v[56:57], v[58:59]\n"
"v_pk_fma_f32 v[90:91], v[90:91], v[56:57], v[58:59]\n"
"v_pk_fma_f32 v[92:93], v[92:93], v[56:57], v[58:59]\n"
"v_pk_fma_f32 v[94:95], v[94:95], v[56:57], v[58:59]\n"
"v_cvt_pk_bf16_f32 v68, v88, v89\n"
"v_cvt_pk_bf16_f32 v69, v90, v91\n"
"v_cvt_pk_bf16_f32 v70, v92, v93\n"
"v_cvt_pk_bf16_f32 v71, v94, v95\n"
"buffer_store_dwordx4 v[68:71], v49, s[8:11], s98 offen offset:2048 sc1\n"
"v_pk_fma_f32 v[96:97], v[96:97], v[56:57], v[58:59]\n"
"v_pk_fma_f32 v[98:99], v[98:99], v[56:57], v[58:59]\n"
"v_pk_fma_f32 v[100:101], v[100:101], v[56:57], v[58:59]\n"
"v_pk_fma_f32 v[102:103], v[102:103], v[56:57], v[58:59]\n"
"v_cvt_pk_bf16_f32 v72, v96, v97\n"
"v_cvt_pk_bf16_f32 v73, v98, v99\n"
"v_cvt_pk_bf16_f32 v74, v100, v101\n"
"v_cvt_pk_bf16_f32 v75, v102, v103\n"
"buffer_store_dwordx4 v[72:75], v49, s[8:11], s99 offen offset:0 sc1\n"
"L_ln4:\n"
"s_xor_b32 s34, s54, 0\n"
"s_mul_i32 s55, s34, 18432\n"
"s_add_u32 s55, s55, 0x10000\n"
"s_xor_b32 s34, s54, 1\n"
"s_mul_i32 s56, s34, 18432\n"
"s_add_u32 s56, s56, 0x10000\n"
"s_xor_b32 s34, s54, 2\n"
"s_mul_i32 s57, s34, 18432\n"
"s_add_u32 s57, s57, 0x10000\n"
"s_xor_b32 s34, s54, 3\n"
"s_mul_i32 s58, s34, 18432\n"
"s_add_u32 s58, s58, 0x10000\n"
"s_xor_b32 s34, s54, 0\n"
"s_lshl_b32 s34, s34, 5\n"
"s_add_u32 s34, s34, s53\n"
"s_lshl_b32 s34, s34, 12\n"
"s_add_u32 s93, s34, 0x400000\n"
"s_xor_b32 s34, s54, 1\n"
"s_lshl_b32 s34, s34, 5\n"
"s_add_u32 s34, s34, s53\n"
"s_lshl_b32 s34, s34, 12\n"
"s_add_u32 s94, s34, 0x400000\n"
"s_xor_b32 s34, s54, 2\n"
"s_lshl_b32 s34, s34, 5\n"
"s_add_u32 s34, s34, s53\n"
"s_lshl_b32 s34, s34, 12\n"
"s_add_u32 s95, s34, 0x400000\n"
"s_xor_b32 s34, s54, 3\n"
"s_lshl_b32 s34, s34, 5\n"
"s_add_u32 s34, s34, s53\n"
"s_lshl_b32 s34, s34, 12\n"
"s_add_u32 s96, s34, 0x400000\n"
"s_mul_i32 s34, s6, 7282\n"
"s_lshr_b32 s34, s34, 16\n"
"s_mul_i32 s35, s34, 9\n"
"s_sub_u32 s35, s6, s35\n"
"s_lshl_b32 s35, s35, 2\n"
"s_add_u32 s35, s35, s52\n"
"s_mul_i32 s98, s34, 9363\n"
"s_lshr_b32 s98, s98, 16\n"
"s_mul_i32 s99, s98, 7\n"
"s_sub_u32 s99, s34, s99\n"
"s_mul_i32 s34, s99, 9\n"
"s_mul_i32 s7, s99, 11\n"
"s_sub_u32 s7, s7, 8\n"
"s_mul_i32 s82, s98, 36\n"
"s_lshl_b32 s83, s98, 5\n"
"s_sub_u32 s83, s83, 36\n"
"s_cmp_lt_u32 s99, 4\n"
"s_cselect_b32 s44, s78, s76\n"
"s_cselect_b32 s45, s79, s77\n"
"s_cselect_b32 s76, s34, s7\n"
"s_cselect_b32 s82, s82, s83\n"
"s_cselect_b32 s77, 9, 11\n"
"s_add_u32 s77, s77, s76\n"
"s_min_u32 s77, s77, 68\n"
"s_cmp_lt_u32 s98, 4\n"
"s_cselect_b32 s77, s77, 0\n"
"s_cselect_b32 s82, s82, 0\n"
"s_and_b32 s45, s45, 0xffff\n"
"s_mov_b32 s47, 0x20000\n"
"s_add_u32 s83, s82, s77\n"
"s_mul_i32 s46, s83, 36864\n"
"s_add_u32 s82, s82, s76\n"
"s_mul_i32 s82, s82, 36864\n"
"s_lshl_b32 s83, s35, 5\n"
"s_add_u32 s82, s82, s83\n"
"s_mul_i32 s7, s35, 7282\n"
"s_lshr_b32 s7, s7, 16\n"
"s_mul_i32 s34, s7, 9\n"
"s_sub_u32 s35, s35, s34\n"
"s_lshl_b32 s83, s7, 20\n"
"s_lshl_b32 s7, s7, 2\n"
"s_add_u32 s7, s7, s98\n"
"s_mul_i32 s7, s7, 626688\n"
"s_lshr_b32 s34, s35, 1\n"
"s_sub_u32 s34, s34, s98\n"
"s_sub_u32 s34, s34, 1\n"
"s_and_b32 s34, s34, 3\n"
"s_lshl_b32 s34, s34, 1\n"
"s_and_b32 s99, s35, 1\n"
"s_add_u32 s34, s34, s99\n"
"s_add_u32 s34, s34, 1\n"
"s_cmp_eq_u32 s35, 8\n"
"s_cselect_b32 s78, 0, s34\n"
"s_mul_i32 s79, s78, 3\n"
"s_add_u32 s79, s79, 108\n"
"s_mul_i32 s34, s78, 5\n"
"s_add_u32 s34, s34, 96\n"
"s_cmp_lt_u32 s78, 7\n"
"s_cselect_b32 s79, s79, s34\n"
"s_mov_b32 s48, s64\n"
"s_and_b32 s49, s65, 0xffff\n"
"s_mov_b32 s50, 0x0\n"
"s_mov_b32 s51, 0x20000\n"
"s_add_u32 s48, s48, 0x2800000\n"
"s_addc_u32 s49, s49, 0\n"
"s_add_u32 s48, s48, s7\n"
"s_addc_u32 s49, s49, 0\n"
"s_mov_b32 s7, 0\n"
"s_lshl_b32 s77, s77, 8\n"
"s_lshl_b32 s78, s78, 16\n"
"s_lshl_b32 s79, s79, 24\n"
"s_or_b32 s76, s76, s77\n"
"s_or_b32 s76, s76, s83\n"
"s_or_b32 s78, s78, s79\n"
"s_or_b32 s83, s76, s78\n"
"s_cmp_lt_u32 s6, 128\n"
"s_cselect_b32 s4, s16, s20\n"
"s_cselect_b32 s5, s17, s21\n"
"s_bfe_u32 s34, s6, 0x40003\n"
"s_lshl2_add_u32 s34, s34, s52\n"
"s_mul_i32 s34, s34, 82944\n"
"s_and_b32 s35, s6, 6\n"
"s_mul_i32 s35, s35, 1152\n"
"s_add_u32 s34, s34, s35\n"
"s_mul_i32 s35, s62, 1280\n"
"s_add_u32 s34, s34, s35\n"
"s_add_u32 s4, s4, s34\n"
"s_addc_u32 s5, s5, 0\n"
"s_waitcnt lgkmcnt(0)\n"
"s_add_u32 s100, s100, 1\n"
"s_not_b32 s101, s101\n"
"s_andn2_b32 s98, s6, 31\n"
"s_add_u32 s34, s98, s53\n"
"s_lshl_b32 s34, s34, 5\n"
"s_lshl3_add_u32 s34, s52, s34\n"
"s_add_u32 s34, s34, 12288\n"
"s_lshl3_add_u32 s98, s52, s98\n"
"s_lshl_b32 s98, s98, 5\n"
"s_add_u32 s98, s98, 12288\n"
"s_mov_b32 s35, 1048576\n"
"s_cmp_eq_u32 s52, 3\n"
"s_cbranch_scc1 L_b2\n"
"v_mov_b32 v30, s100\n"
"v_mov_b32 v31, s101\n"
"s_mov_b64 exec, 1\n"
"s_waitcnt vmcnt(0)\n"
"buffer_store_dwordx2 v[30:31], v0, s[8:11], s34 offen sc1\n"
"s_mov_b64 exec, -1\n"
"L_b2:\n"
"s_mov_b32 s81, 4\n"
"L_uc:\n"
"s_add_u32 s34, s80, s90\n"
"s_sub_u32 m0, s34, 36\n"
"s_min_u32 s34, s34, m0\n"
"s_lshl_b32 m0, s34, 12\n"
"s_lshl_b32 s59, s80, 17\n"
"s_add_u32 s59, s59, s60\n"
"buffer_load_dwordx4 v1, s[12:15], s59 offen lds\n"
"s_add_u32 s59, s59, 31744\n"
"buffer_load_dwordx4 v1, s[12:15], s59 offen offset:1024 lds\n"
"s_add_u32 s59, s59, 31744\n"
"buffer_load_dwordx4 v1, s[12:15], s59 offen offset:2048 lds\n"
"s_add_u32 s59, s59, 31744\n"
"buffer_load_dwordx4 v1, s[12:15], s59 offen offset:3072 lds\n"
"s_add_u32 s80, s80, 4\n"
"s_cmp_ge_u32 s80, 36\n"
"s_cselect_b32 s34, 36, 0\n"
"s_sub_u32 s80, s80, s34\n"
"s_sub_u32 s81, s81, 1\n"
"s_cbranch_scc0 L_uc\n"
"L_lp:\n"
"buffer_load_dwordx2 v[56:57], v50, s[8:11], s98 offen sc1\n"
"s_sub_u32 s35, s35, 1\n"
"s_waitcnt vmcnt(0)\n"
"s_cbranch_scc1 L_b3\n"
"v_cmp_ne_u64 vcc, s[100:101], v[56:57]\n"
"s_nop 1\n"
"s_cbranch_vccnz L_lp\n"
"L_b3:\n"
"buffer_load_dwordx4 a[0:3], v0, s[8:11], s55 offen sc1\n"
"buffer_load_dwordx4 a[16:19], v0, s[8:11], s55 offen offset:1024 sc1\n"
"buffer_load_dwordx4 a[4:7], v0, s[8:11], s56 offen sc1\n"
"buffer_load_dwordx4 a[20:23], v0, s[8:11], s56 offen offset:1024 sc1\n"
"buffer_load_dwordx4 a[8:11], v0, s[8:11], s57 offen sc1\n"
"buffer_load_dwordx4 a[24:27], v0, s[8:11], s57 offen offset:1024 sc1\n"
"buffer_load_dwordx4 a[12:15], v0, s[8:11], s58 offen sc1\n"
"buffer_load_dwordx4 a[28:31], v0, s[8:11], s58 offen offset:1024 sc1\n"
"s_add_u32 s55, s55, 2048\n"
"buffer_load_dwordx4 a[32:35], v0, s[8:11], s55 offen sc1\n"
"buffer_load_dwordx4 a[48:51], v0, s[8:11], s55 offen offset:1024 sc1\n"
"s_add_u32 s56, s56, 2048\n"
"buffer_load_dwordx4 a[36:39], v0, s[8:11], s56 offen sc1\n"
"buffer_load_dwordx4 a[52:55], v0, s[8:11], s56 offen offset:1024 sc1\n"
"s_add_u32 s57, s57, 2048\n"
"buffer_load_dwordx4 a[40:43], v0, s[8:11], s57 offen sc1\n"
"buffer_load_dwordx4 a[56:59], v0, s[8:11], s57 offen offset:1024 sc1\n"
"s_add_u32 s58, s58, 2048\n"
"buffer_load_dwordx4 a[44:47], v0, s[8:11], s58 offen sc1\n"
"buffer_load_dwordx4 a[60:63], v0, s[8:11], s58 offen offset:1024 sc1\n"
"s_barrier\n"
"ds_read_b64_tr_b16 v[64:65], v32 offset:0\n"
"ds_read_b64_tr_b16 v[66:67], v32 offset:4096\n"
"ds_read_b64_tr_b16 v[68:69], v32 offset:8192\n"
"ds_read_b64_tr_b16 v[70:71], v32 offset:12288\n"
"ds_read_b64_tr_b16 v[72:73], v32 offset:8\n"
"ds_read_b64_tr_b16 v[74:75], v32 offset:4104\n"
"ds_read_b64_tr_b16 v[76:77], v32 offset:8200\n"
"ds_read_b64_tr_b16 v[78:79], v32 offset:12296\n"
"ds_read_b64_tr_b16 v[80:81], v33 offset:0\n"
"ds_read_b64_tr_b16 v[82:83], v33 offset:4096\n"
"ds_read_b64_tr_b16 v[84:85], v33 offset:8192\n"
"ds_read_b64_tr_b16 v[86:87], v33 offset:12288\n"
"s_waitcnt vmcnt(8) lgkmcnt(8)\n"
"v_mfma_f32_16x16x32_bf16 v[160:163], v[64:67], a[0:3], 0\n"
"ds_read_b64_tr_b16 v[88:89], v33 offset:8\n"
"ds_read_b64_tr_b16 v[90:91], v33 offset:4104\n"
"v_mfma_f32_16x16x32_bf16 v[160:163], v[68:71], a[16:19], v[160:163]\n"
"ds_read_b64_tr_b16 v[92:93], v33 offset:8200\n"
"ds_read_b64_tr_b16 v[94:95], v33 offset:12296\n"
"v_mfma_f32_16x16x32_bf16 v[164:167], v[68:71], a[20:23], 0\n"
"s_add_u32 s55, s55, 2048\n"
"v_mfma_f32_16x16x32_bf16 v[164:167], v[64:67], a[4:7], v[164:167]\n"
"buffer_load_dwordx4 a[64:67], v0, s[8:11], s55 offen sc1\n"
"v_mfma_f32_16x16x32_bf16 v[168:171], v[64:67], a[8:11], 0\n"
"v_mfma_f32_16x16x32_bf16 v[168:171], v[68:71], a[24:27], v[168:171]\n"
"v_mfma_f32_16x16x32_bf16 v[172:175], v[68:71], a[28:31], 0\n"
"v_mfma_f32_16x16x32_bf16 v[172:175], v[64:67], a[12:15], v[172:175]\n"
"s_waitcnt lgkmcnt(8)\n"
"buffer_load_dwordx4 a[80:83], v0, s[8:11], s55 offen offset:1024 sc1\n"
"v_mfma_f32_16x16x32_bf16 v[96:99], v[72:75], a[0:3], 0\n"
"s_add_u32 s56, s56, 2048\n"
"ds_read_b64_tr_b16 v[64:65], v34 offset:0\n"
"ds_read_b64_tr_b16 v[66:67], v34 offset:4096\n"
"v_mfma_f32_16x16x32_bf16 v[96:99], v[76:79], a[16:19], v[96:99]\n"
"ds_read_b64_tr_b16 v[68:69], v34 offset:8192\n"
"ds_read_b64_tr_b16 v[70:71], v34 offset:12288\n"
"v_mfma_f32_16x16x32_bf16 v[100:103], v[76:79], a[20:23], 0\n"
"v_mfma_f32_16x16x32_bf16 v[100:103], v[72:75], a[4:7], v[100:103]\n"
"v_mfma_f32_16x16x32_bf16 v[104:107], v[72:75], a[8:11], 0\n"
"buffer_load_dwordx4 a[68:71], v0, s[8:11], s56 offen sc1\n"
"v_mfma_f32_16x16x32_bf16 v[104:107], v[76:79], a[24:27], v[104:107]\n"
"v_mfma_f32_16x16x32_bf16 v[108:111], v[76:79], a[28:31], 0\n"
"v_mfma_f32_16x16x32_bf16 v[108:111], v[72:75], a[12:15], v[108:111]\n"
"s_waitcnt lgkmcnt(8)\n"
"v_mfma_f32_16x16x32_bf16 v[176:179], v[80:83], a[0:3], 0\n"
"ds_read_b64_tr_b16 v[72:73], v34 offset:8\n"
"ds_read_b64_tr_b16 v[74:75], v34 offset:4104\n"
"v_mfma_f32_16x16x32_bf16 v[176:179], v[84:87], a[16:19], v[176:179]\n"
"ds_read_b64_tr_b16 v[76:77], v34 offset:8200\n"
"ds_read_b64_tr_b16 v[78:79], v34 offset:12296\n"
"v_mfma_f32_16x16x32_bf16 v[180:183], v[84:87], a[20:23], 0\n"
"buffer_load_dwordx4 a[84:87], v0, s[8:11], s56 offen offset:1024 sc1\n"
"v_mfma_f32_16x16x32_bf16 v[180:183], v[80:83], a[4:7], v[180:183]\n"
"s_add_u32 s57, s57, 2048\n"
"v_mfma_f32_16x16x32_bf16 v[184:187], v[80:83], a[8:11], 0\n"
"v_mfma_f32_16x16x32_bf16 v[184:187], v[84:87], a[24:27], v[184:187]\n"
"v_mfma_f32_16x16x32_bf16 v[188:191], v[84:87], a[28:31], 0\n"
"v_mfma_f32_16x16x32_bf16 v[188:191], v[80:83], a[12:15], v[188:191]\n"
"s_waitcnt lgkmcnt(8)\n"
"buffer_load_dwordx4 a[72:75], v0, s[8:11], s57 offen sc1\n"
"v_mfma_f32_16x16x32_bf16 v[112:115], v[88:91], a[0:3], 0\n"
"ds_read_b64_tr_b16 v[80:81], v35 offset:0\n"
"ds_read_b64_tr_b16 v[82:83], v35 offset:4096\n"
"v_mfma_f32_16x16x32_bf16 v[112:115], v[92:95], a[16:19], v[112:115]\n"
"ds_read_b64_tr_b16 v[84:85], v35 offset:8192\n"
"ds_read_b64_tr_b16 v[86:87], v35 offset:12288\n"
"v_mfma_f32_16x16x32_bf16 v[116:119], v[92:95], a[20:23], 0\n"
"v_mfma_f32_16x16x32_bf16 v[116:119], v[88:91], a[4:7], v[116:119]\n"
"v_mfma_f32_16x16x32_bf16 v[120:123], v[88:91], a[8:11], 0\n"
"buffer_load_dwordx4 a[88:91], v0, s[8:11], s57 offen offset:1024 sc1\n"
"v_mfma_f32_16x16x32_bf16 v[120:123], v[92:95], a[24:27], v[120:123]\n"
"s_add_u32 s58, s58, 2048\n"
"v_mfma_f32_16x16x32_bf16 v[124:127], v[92:95], a[28:31], 0\n"
"v_mfma_f32_16x16x32_bf16 v[124:127], v[88:91], a[12:15], v[124:127]\n"
"s_waitcnt lgkmcnt(8)\n"
"v_mfma_f32_16x16x32_bf16 v[192:195], v[64:67], a[0:3], 0\n"
"ds_read_b64_tr_b16 v[88:89], v35 offset:8\n"
"ds_read_b64_tr_b16 v[90:91], v35 offset:4104\n"
"v_mfma_f32_16x16x32_bf16 v[192:195], v[68:71], a[16:19], v[192:195]\n"
"ds_read_b64_tr_b16 v[92:93], v35 offset:8200\n"
"ds_read_b64_tr_b16 v[94:95], v35 offset:12296\n"
"v_mfma_f32_16x16x32_bf16 v[196:199], v[68:71], a[20:23], 0\n"
"buffer_load_dwordx4 a[76:79], v0, s[8:11], s58 offen sc1\n"
"v_mfma_f32_16x16x32_bf16 v[196:199], v[64:67], a[4:7], v[196:199]\n"
"v_mfma_f32_16x16x32_bf16 v[200:203], v[64:67], a[8:11], 0\n"
"v_mfma_f32_16x16x32_bf16 v[200:203], v[68:71], a[24:27], v[200:203]\n"
"v_mfma_f32_16x16x32_bf16 v[204:207], v[68:71], a[28:31], 0\n"
"v_mfma_f32_16x16x32_bf16 v[204:207], v[64:67], a[12:15], v[204:207]\n"
"s_waitcnt lgkmcnt(8)\n"
"buffer_load_dwordx4 a[92:95], v0, s[8:11], s58 offen offset:1024 sc1\n"
"v_mfma_f32_16x16x32_bf16 v[128:131], v[72:75], a[0:3], 0\n"
"s_add_u32 s55, s55, 2048\n"
"ds_read_b64_tr_b16 v[64:65], v32 offset:16384\n"
"ds_read_b64_tr_b16 v[66:67], v32 offset:20480\n"
"v_mfma_f32_16x16x32_bf16 v[128:131], v[76:79], a[16:19], v[128:131]\n"
"ds_read_b64_tr_b16 v[68:69], v32 offset:24576\n"
"ds_read_b64_tr_b16 v[70:71], v32 offset:28672\n"
"v_mfma_f32_16x16x32_bf16 v[132:135], v[76:79], a[20:23], 0\n"
"v_mfma_f32_16x16x32_bf16 v[132:135], v[72:75], a[4:7], v[132:135]\n"
"v_mfma_f32_16x16x32_bf16 v[136:139], v[72:75], a[8:11], 0\n"
"buffer_load_dwordx4 a[96:99], v0, s[8:11], s55 offen sc1\n"
"v_mfma_f32_16x16x32_bf16 v[136:139], v[76:79], a[24:27], v[136:139]\n"
"v_mfma_f32_16x16x32_bf16 v[140:143], v[76:79], a[28:31], 0\n"
"v_mfma_f32_16x16x32_bf16 v[140:143], v[72:75], a[12:15], v[140:143]\n"
"s_waitcnt lgkmcnt(8)\n"
"v_mfma_f32_16x16x32_bf16 v[208:211], v[80:83], a[0:3], 0\n"
"ds_read_b64_tr_b16 v[72:73], v32 offset:16392\n"
"ds_read_b64_tr_b16 v[74:75], v32 offset:20488\n"
"v_mfma_f32_16x16x32_bf16 v[208:211], v[84:87], a[16:19], v[208:211]\n"
"ds_read_b64_tr_b16 v[76:77], v32 offset:24584\n"
"ds_read_b64_tr_b16 v[78:79], v32 offset:28680\n"
"v_mfma_f32_16x16x32_bf16 v[212:215], v[84:87], a[20:23], 0\n"
"buffer_load_dwordx4 a[112:115], v0, s[8:11], s55 offen offset:1024 sc1\n"
"v_mfma_f32_16x16x32_bf16 v[212:215], v[80:83], a[4:7], v[212:215]\n"
"s_add_u32 s56, s56, 2048\n"
"v_mfma_f32_16x16x32_bf16 v[216:219], v[80:83], a[8:11], 0\n"
"v_mfma_f32_16x16x32_bf16 v[216:219], v[84:87], a[24:27], v[216:219]\n"
"v_mfma_f32_16x16x32_bf16 v[220:223], v[84:87], a[28:31], 0\n"
"buffer_load_dwordx4 a[100:103], v0, s[8:11], s56 offen sc1\n"
"v_mfma_f32_16x16x32_bf16 v[220:223], v[80:83], a[12:15], v[220:223]\n"
"s_waitcnt lgkmcnt(8)\n"
"v_mfma_f32_16x16x32_bf16 v[144:147], v[88:91], a[0:3], 0\n"
"ds_read_b64_tr_b16 v[80:81], v33 offset:16384\n"
"ds_read_b64_tr_b16 v[82:83], v33 offset:20480\n"
"v_mfma_f32_16x16x32_bf16 v[144:147], v[92:95], a[16:19], v[144:147]\n"
"ds_read_b64_tr_b16 v[84:85], v33 offset:24576\n"
"ds_read_b64_tr_b16 v[86:87], v33 offset:28672\n"
"v_mfma_f32_16x16x32_bf16 v[148:151], v[92:95], a[20:23], 0\n"
"v_mfma_f32_16x16x32_bf16 v[148:151], v[88:91], a[4:7], v[148:151]\n"
"v_mfma_f32_16x16x32_bf16 v[152:155], v[88:91], a[8:11], 0\n"
"buffer_load_dwordx4 a[116:119], v0, s[8:11], s56 offen offset:1024 sc1\n"
"v_mfma_f32_16x16x32_bf16 v[152:155], v[92:95], a[24:27], v[152:155]\n"
"s_add_u32 s57, s57, 2048\n"
"v_mfma_f32_16x16x32_bf16 v[156:159], v[92:95], a[28:31], 0\n"
"v_mfma_f32_16x16x32_bf16 v[156:159], v[88:91], a[12:15], v[156:159]\n"
"s_waitcnt vmcnt(12) lgkmcnt(8)\n"
"v_mfma_f32_16x16x32_bf16 v[160:163], v[64:67], a[32:35], v[160:163]\n"
"ds_read_b64_tr_b16 v[88:89], v33 offset:16392\n"
"ds_read_b64_tr_b16 v[90:91], v33 offset:20488\n"
"v_mfma_f32_16x16x32_bf16 v[160:163], v[68:71], a[48:51], v[160:163]\n"
"ds_read_b64_tr_b16 v[92:93], v33 offset:24584\n"
"ds_read_b64_tr_b16 v[94:95], v33 offset:28680\n"
"v_mfma_f32_16x16x32_bf16 v[164:167], v[68:71], a[52:55], v[164:167]\n"
"buffer_load_dwordx4 a[104:107], v0, s[8:11], s57 offen sc1\n"
"v_mfma_f32_16x16x32_bf16 v[164:167], v[64:67], a[36:39], v[164:167]\n"
"v_mfma_f32_16x16x32_bf16 v[168:171], v[64:67], a[40:43], v[168:171]\n"
"v_mfma_f32_16x16x32_bf16 v[168:171], v[68:71], a[56:59], v[168:171]\n"
"v_mfma_f32_16x16x32_bf16 v[172:175], v[68:71], a[60:63], v[172:175]\n"
"buffer_load_dwordx4 a[120:123], v0, s[8:11], s57 offen offset:1024 sc1\n"
"v_mfma_f32_16x16x32_bf16 v[172:175], v[64:67], a[44:47], v[172:175]\n"
"s_waitcnt lgkmcnt(8)\n"
"v_mfma_f32_16x16x32_bf16 v[96:99], v[72:75], a[32:35], v[96:99]\n"
"s_add_u32 s58, s58, 2048\n"
"ds_read_b64_tr_b16 v[64:65], v34 offset:16384\n"
"ds_read_b64_tr_b16 v[66:67], v34 offset:20480\n"
"v_mfma_f32_16x16x32_bf16 v[96:99], v[76:79], a[48:51], v[96:99]\n"
"ds_read_b64_tr_b16 v[68:69], v34 offset:24576\n"
"ds_read_b64_tr_b16 v[70:71], v34 offset:28672\n"
"v_mfma_f32_16x16x32_bf16 v[100:103], v[76:79], a[52:55], v[100:103]\n"
"v_mfma_f32_16x16x32_bf16 v[100:103], v[72:75], a[36:39], v[100:103]\n"
"v_mfma_f32_16x16x32_bf16 v[104:107], v[72:75], a[40:43], v[104:107]\n"
"buffer_load_dwordx4 a[108:111], v0, s[8:11], s58 offen sc1\n"
"v_mfma_f32_16x16x32_bf16 v[104:107], v[76:79], a[56:59], v[104:107]\n"
"v_mfma_f32_16x16x32_bf16 v[108:111], v[76:79], a[60:63], v[108:111]\n"
"v_mfma_f32_16x16x32_bf16 v[108:111], v[72:75], a[44:47], v[108:111]\n"
"s_waitcnt lgkmcnt(8)\n"
"v_mfma_f32_16x16x32_bf16 v[176:179], v[80:83], a[32:35], v[176:179]\n"
"ds_read_b64_tr_b16 v[72:73], v34 offset:16392\n"
"ds_read_b64_tr_b16 v[74:75], v34 offset:20488\n"
"v_mfma_f32_16x16x32_bf16 v[176:179], v[84:87], a[48:51], v[176:179]\n"
"ds_read_b64_tr_b16 v[76:77], v34 offset:24584\n"
"ds_read_b64_tr_b16 v[78:79], v34 offset:28680\n"
"v_mfma_f32_16x16x32_bf16 v[180:183], v[84:87], a[52:55], v[180:183]\n"
"buffer_load_dwordx4 a[124:127], v0, s[8:11], s58 offen offset:1024 sc1\n"
"v_mfma_f32_16x16x32_bf16 v[180:183], v[80:83], a[36:39], v[180:183]\n"
"s_add_u32 s55, s55, 2048\n"
"v_mfma_f32_16x16x32_bf16 v[184:187], v[80:83], a[40:43], v[184:187]\n"
"v_mfma_f32_16x16x32_bf16 v[184:187], v[84:87], a[56:59], v[184:187]\n"
"v_mfma_f32_16x16x32_bf16 v[188:191], v[84:87], a[60:63], v[188:191]\n"
"buffer_load_dwordx4 a[128:131], v0, s[8:11], s55 offen sc1\n"
"v_mfma_f32_16x16x32_bf16 v[188:191], v[80:83], a[44:47], v[188:191]\n"
"s_waitcnt lgkmcnt(8)\n"
"v_mfma_f32_16x16x32_bf16 v[112:115], v[88:91], a[32:35], v[112:115]\n"
"ds_read_b64_tr_b16 v[80:81], v35 offset:16384\n"
"ds_read_b64_tr_b16 v[82:83], v35 offset:20480\n"
"v_mfma_f32_16x16x32_bf16 v[112:115], v[92:95], a[48:51], v[112:115]\n"
"ds_read_b64_tr_b16 v[84:85], v35 offset:24576\n"
"ds_read_b64_tr_b16 v[86:87], v35 offset:28672\n"
"v_mfma_f32_16x16x32_bf16 v[116:119], v[92:95], a[52:55], v[116:119]\n"
"v_mfma_f32_16x16x32_bf16 v[116:119], v[88:91], a[36:39], v[116:119]\n"
"v_mfma_f32_16x16x32_bf16 v[120:123], v[88:91], a[40:43], v[120:123]\n"
"buffer_load_dwordx4 a[144:147], v0, s[8:11], s55 offen offset:1024 sc1\n"
"v_mfma_f32_16x16x32_bf16 v[120:123], v[92:95], a[56:59], v[120:123]\n"
"s_add_u32 s56, s56, 2048\n"
"v_mfma_f32_16x16x32_bf16 v[124:127], v[92:95], a[60:63], v[124:127]\n"
"v_mfma_f32_16x16x32_bf16 v[124:127], v[88:91], a[44:47], v[124:127]\n"
"s_waitcnt lgkmcnt(8)\n"
"v_mfma_f32_16x16x32_bf16 v[192:195], v[64:67], a[32:35], v[192:195]\n"
"ds_read_b64_tr_b16 v[88:89], v35 offset:16392\n"
"ds_read_b64_tr_b16 v[90:91], v35 offset:20488\n"
"v_mfma_f32_16x16x32_bf16 v[192:195], v[68:71], a[48:51], v[192:195]\n"
"ds_read_b64_tr_b16 v[92:93], v35 offset:24584\n"
"ds_read_b64_tr_b16 v[94:95], v35 offset:28680\n"
"v_mfma_f32_16x16x32_bf16 v[196:199], v[68:71], a[52:55], v[196:199]\n"
"buffer_load_dwordx4 a[132:135], v0, s[8:11], s56 offen sc1\n"
"v_mfma_f32_16x16x32_bf16 v[196:199], v[64:67], a[36:39], v[196:199]\n"
"v_mfma_f32_16x16x32_bf16 v[200:203], v[64:67], a[40:43], v[200:203]\n"
"v_mfma_f32_16x16x32_bf16 v[200:203], v[68:71], a[56:59], v[200:203]\n"
"v_mfma_f32_16x16x32_bf16 v[204:207], v[68:71], a[60:63], v[204:207]\n"
"buffer_load_dwordx4 a[148:151], v0, s[8:11], s56 offen offset:1024 sc1\n"
"v_mfma_f32_16x16x32_bf16 v[204:207], v[64:67], a[44:47], v[204:207]\n"
"s_waitcnt lgkmcnt(8)\n"
"v_mfma_f32_16x16x32_bf16 v[128:131], v[72:75], a[32:35], v[128:131]\n"
"s_add_u32 s57, s57, 2048\n"
"ds_read_b64_tr_b16 v[64:65], v32 offset:32768\n"
"ds_read_b64_tr_b16 v[66:67], v32 offset:36864\n"
"v_mfma_f32_16x16x32_bf16 v[128:131], v[76:79], a[48:51], v[128:131]\n"
"ds_read_b64_tr_b16 v[68:69], v32 offset:40960\n"
"ds_read_b64_tr_b16 v[70:71], v32 offset:45056\n"
"v_mfma_f32_16x16x32_bf16 v[132:135], v[76:79], a[52:55], v[132:135]\n"
"v_mfma_f32_16x16x32_bf16 v[132:135], v[72:75], a[36:39], v[132:135]\n"
"buffer_load_dwordx4 a[136:139], v0, s[8:11], s57 offen sc1\n"
"v_mfma_f32_16x16x32_bf16 v[136:139], v[72:75], a[40:43], v[136:139]\n"
"v_mfma_f32_16x16x32_bf16 v[136:139], v[76:79], a[56:59], v[136:139]\n"
"v_mfma_f32_16x16x32_bf16 v[140:143], v[76:79], a[60:63], v[140:143]\n"
"v_mfma_f32_16x16x32_bf16 v[140:143], v[72:75], a[44:47], v[140:143]\n"
"s_waitcnt lgkmcnt(8)\n"
"v_mfma_f32_16x16x32_bf16 v[208:211], v[80:83], a[32:35], v[208:211]\n"
"ds_read_b64_tr_b16 v[72:73], v32 offset:32776\n"
"ds_read_b64_tr_b16 v[74:75], v32 offset:36872\n"
"v_mfma_f32_16x16x32_bf16 v[208:211], v[84:87], a[48:51], v[208:211]\n"
"ds_read_b64_tr_b16 v[76:77], v32 offset:40968\n"
"ds_read_b64_tr_b16 v[78:79], v32 offset:45064\n"
"v_mfma_f32_16x16x32_bf16 v[212:215], v[84:87], a[52:55], v[212:215]\n"
"buffer_load_dwordx4 a[152:155], v0, s[8:11], s57 offen offset:1024 sc1\n"
"v_mfma_f32_16x16x32_bf16 v[212:215], v[80:83], a[36:39], v[212:215]\n"
"s_add_u32 s58, s58, 2048\n"
"v_mfma_f32_16x16x32_bf16 v[216:219], v[80:83], a[40:43], v[216:219]\n"
"v_mfma_f32_16x16x32_bf16 v[216:219], v[84:87], a[56:59], v[216:219]\n"
"v_mfma_f32_16x16x32_bf16 v[220:223], v[84:87], a[60:63], v[220:223]\n"
"buffer_load_dwordx4 a[140:143], v0, s[8:11], s58 offen sc1\n"
"v_mfma_f32_16x16x32_bf16 v[220:223], v[80:83], a[44:47], v[220:223]\n"
"s_waitcnt lgkmcnt(8)\n"
"v_mfma_f32_16x16x32_bf16 v[144:147], v[88:91], a[32:35], v[144:147]\n"
"ds_read_b64_tr_b16 v[80:81], v33 offset:32768\n"
"ds_read_b64_tr_b16 v[82:83], v33 offset:36864\n"
"v_mfma_f32_16x16x32_bf16 v[144:147], v[92:95], a[48:51], v[144:147]\n"
"ds_read_b64_tr_b16 v[84:85], v33 offset:40960\n"
"ds_read_b64_tr_b16 v[86:87], v33 offset:45056\n"
"v_mfma_f32_16x16x32_bf16 v[148:151], v[92:95], a[52:55], v[148:151]\n"
"v_mfma_f32_16x16x32_bf16 v[148:151], v[88:91], a[36:39], v[148:151]\n"
"buffer_load_dwordx4 a[156:159], v0, s[8:11], s58 offen offset:1024 sc1\n"
"v_mfma_f32_16x16x32_bf16 v[152:155], v[88:91], a[40:43], v[152:155]\n"
"s_add_u32 s55, s55, 2048\n"
"v_mfma_f32_16x16x32_bf16 v[152:155], v[92:95], a[56:59], v[152:155]\n"
"v_mfma_f32_16x16x32_bf16 v[156:159], v[92:95], a[60:63], v[156:159]\n"
"v_mfma_f32_16x16x32_bf16 v[156:159], v[88:91], a[44:47], v[156:159]\n"
"s_waitcnt vmcnt(16) lgkmcnt(8)\n"
"v_mfma_f32_16x16x32_bf16 v[160:163], v[64:67], a[64:67], v[160:163]\n"
"ds_read_b64_tr_b16 v[88:89], v33 offset:32776\n"
"ds_read_b64_tr_b16 v[90:91], v33 offset:36872\n"
"v_mfma_f32_16x16x32_bf16 v[160:163], v[68:71], a[80:83], v[160:163]\n"
"ds_read_b64_tr_b16 v[92:93], v33 offset:40968\n"
"ds_read_b64_tr_b16 v[94:95], v33 offset:45064\n"
"v_mfma_f32_16x16x32_bf16 v[164:167], v[68:71], a[84:87], v[164:167]\n"
"buffer_load_dwordx4 a[160:163], v0, s[8:11], s55 offen sc1\n"
"v_mfma_f32_16x16x32_bf16 v[164:167], v[64:67], a[68:71], v[164:167]\n"
"v_mfma_f32_16x16x32_bf16 v[168:171], v[64:67], a[72:75], v[168:171]\n"
"v_mfma_f32_16x16x32_bf16 v[168:171], v[68:71], a[88:91], v[168:171]\n"
"v_mfma_f32_16x16x32_bf16 v[172:175], v[68:71], a[92:95], v[172:175]\n"
"buffer_load_dwordx4 a[176:179], v0, s[8:11], s55 offen offset:1024 sc1\n"
"v_mfma_f32_16x16x32_bf16 v[172:175], v[64:67], a[76:79], v[172:175]\n"
"s_waitcnt lgkmcnt(8)\n"
"v_mfma_f32_16x16x32_bf16 v[96:99], v[72:75], a[64:67], v[96:99]\n"
"s_add_u32 s56, s56, 2048\n"
"ds_read_b64_tr_b16 v[64:65], v34 offset:32768\n"
"ds_read_b64_tr_b16 v[66:67], v34 offset:36864\n"
"v_mfma_f32_16x16x32_bf16 v[96:99], v[76:79], a[80:83], v[96:99]\n"
"ds_read_b64_tr_b16 v[68:69], v34 offset:40960\n"
"ds_read_b64_tr_b16 v[70:71], v34 offset:45056\n"
"v_mfma_f32_16x16x32_bf16 v[100:103], v[76:79], a[84:87], v[100:103]\n"
"v_mfma_f32_16x16x32_bf16 v[100:103], v[72:75], a[68:71], v[100:103]\n"
"buffer_load_dwordx4 a[164:167], v0, s[8:11], s56 offen sc1\n"
"v_mfma_f32_16x16x32_bf16 v[104:107], v[72:75], a[72:75], v[104:107]\n"
"v_mfma_f32_16x16x32_bf16 v[104:107], v[76:79], a[88:91], v[104:107]\n"
"v_mfma_f32_16x16x32_bf16 v[108:111], v[76:79], a[92:95], v[108:111]\n"
"v_mfma_f32_16x16x32_bf16 v[108:111], v[72:75], a[76:79], v[108:111]\n"
"s_waitcnt lgkmcnt(8)\n"
"v_mfma_f32_16x16x32_bf16 v[176:179], v[80:83], a[64:67], v[176:179]\n"
"ds_read_b64_tr_b16 v[72:73], v34 offset:32776\n"
"ds_read_b64_tr_b16 v[74:75], v34 offset:36872\n"
"v_mfma_f32_16x16x32_bf16 v[176:179], v[84:87], a[80:83], v[176:179]\n"
"ds_read_b64_tr_b16 v[76:77], v34 offset:40968\n"
"ds_read_b64_tr_b16 v[78:79], v34 offset:45064\n"
"v_mfma_f32_16x16x32_bf16 v[180:183], v[84:87], a[84:87], v[180:183]\n"
"buffer_load_dwordx4 a[180:183], v0, s[8:11], s56 offen offset:1024 sc1\n"
"v_mfma_f32_16x16x32_bf16 v[180:183], v[80:83], a[68:71], v[180:183]\n"
"s_add_u32 s57, s57, 2048\n"
"v_mfma_f32_16x16x32_bf16 v[184:187], v[80:83], a[72:75], v[184:187]\n"
"v_mfma_f32_16x16x32_bf16 v[184:187], v[84:87], a[88:91], v[184:187]\n"
"v_mfma_f32_16x16x32_bf16 v[188:191], v[84:87], a[92:95], v[188:191]\n"
"buffer_load_dwordx4 a[168:171], v0, s[8:11], s57 offen sc1\n"
"v_mfma_f32_16x16x32_bf16 v[188:191], v[80:83], a[76:79], v[188:191]\n"
"s_waitcnt lgkmcnt(8)\n"
"v_mfma_f32_16x16x32_bf16 v[112:115], v[88:91], a[64:67], v[112:115]\n"
"ds_read_b64_tr_b16 v[80:81], v35 offset:32768\n"
"ds_read_b64_tr_b16 v[82:83], v35 offset:36864\n"
"v_mfma_f32_16x16x32_bf16 v[112:115], v[92:95], a[80:83], v[112:115]\n"
"ds_read_b64_tr_b16 v[84:85], v35 offset:40960\n"
"ds_read_b64_tr_b16 v[86:87], v35 offset:45056\n"
"v_mfma_f32_16x16x32_bf16 v[116:119], v[92:95], a[84:87], v[116:119]\n"
"v_mfma_f32_16x16x32_bf16 v[116:119], v[88:91], a[68:71], v[116:119]\n"
"buffer_load_dwordx4 a[184:187], v0, s[8:11], s57 offen offset:1024 sc1\n"
"v_mfma_f32_16x16x32_bf16 v[120:123], v[88:91], a[72:75], v[120:123]\n"
"s_add_u32 s58, s58, 2048\n"
"v_mfma_f32_16x16x32_bf16 v[120:123], v[92:95], a[88:91], v[120:123]\n"
"v_mfma_f32_16x16x32_bf16 v[124:127], v[92:95], a[92:95], v[124:127]\n"
"v_mfma_f32_16x16x32_bf16 v[124:127], v[88:91], a[76:79], v[124:127]\n"
"s_waitcnt lgkmcnt(8)\n"
"v_mfma_f32_16x16x32_bf16 v[192:195], v[64:67], a[64:67], v[192:195]\n"
"ds_read_b64_tr_b16 v[88:89], v35 offset:32776\n"
"ds_read_b64_tr_b16 v[90:91], v35 offset:36872\n"
"v_mfma_f32_16x16x32_bf16 v[192:195], v[68:71], a[80:83], v[192:195]\n"
"ds_read_b64_tr_b16 v[92:93], v35 offset:40968\n"
"ds_read_b64_tr_b16 v[94:95], v35 offset:45064\n"
"v_mfma_f32_16x16x32_bf16 v[196:199], v[68:71], a[84:87], v[196:199]\n"
"buffer_load_dwordx4 a[172:175], v0, s[8:11], s58 offen sc1\n"
"v_mfma_f32_16x16x32_bf16 v[196:199], v[64:67], a[68:71], v[196:199]\n"
"v_mfma_f32_16x16x32_bf16 v[200:203], v[64:67], a[72:75], v[200:203]\n"
"v_mfma_f32_16x16x32_bf16 v[200:203], v[68:71], a[88:91], v[200:203]\n"
"v_mfma_f32_16x16x32_bf16 v[204:207], v[68:71], a[92:95], v[204:207]\n"
"buffer_load_dwordx4 a[188:191], v0, s[8:11], s58 offen offset:1024 sc1\n"
"v_mfma_f32_16x16x32_bf16 v[204:207], v[64:67], a[76:79], v[204:207]\n"
"s_waitcnt lgkmcnt(8)\n"
"v_mfma_f32_16x16x32_bf16 v[128:131], v[72:75], a[64:67], v[128:131]\n"
"s_add_u32 s55, s55, 2048\n"
"ds_read_b64_tr_b16 v[64:65], v32 offset:49152\n"
"ds_read_b64_tr_b16 v[66:67], v32 offset:53248\n"
"v_mfma_f32_16x16x32_bf16 v[128:131], v[76:79], a[80:83], v[128:131]\n"
"ds_read_b64_tr_b16 v[68:69], v32 offset:57344\n"
"ds_read_b64_tr_b16 v[70:71], v32 offset:61440\n"
"v_mfma_f32_16x16x32_bf16 v[132:135], v[76:79], a[84:87], v[132:135]\n"
"v_mfma_f32_16x16x32_bf16 v[132:135], v[72:75], a[68:71], v[132:135]\n"
"buffer_load_dwordx4 a[192:195], v0, s[8:11], s55 offen sc1\n"
"v_mfma_f32_16x16x32_bf16 v[136:139], v[72:75], a[72:75], v[136:139]\n"
"v_mfma_f32_16x16x32_bf16 v[136:139], v[76:79], a[88:91], v[136:139]\n"
"v_mfma_f32_16x16x32_bf16 v[140:143], v[76:79], a[92:95], v[140:143]\n"
"v_mfma_f32_16x16x32_bf16 v[140:143], v[72:75], a[76:79], v[140:143]\n"
"s_waitcnt lgkmcnt(8)\n"
"v_mfma_f32_16x16x32_bf16 v[208:211], v[80:83], a[64:67], v[208:211]\n"
"ds_read_b64_tr_b16 v[72:73], v32 offset:49160\n"
"ds_read_b64_tr_b16 v[74:75], v32 offset:53256\n"
"v_mfma_f32_16x16x32_bf16 v[208:211], v[84:87], a[80:83], v[208:211]\n"
"ds_read_b64_tr_b16 v[76:77], v32 offset:57352\n"
"ds_read_b64_tr_b16 v[78:79], v32 offset:61448\n"
"v_mfma_f32_16x16x32_bf16 v[212:215], v[84:87], a[84:87], v[212:215]\n"
"buffer_load_dwordx4 a[208:211], v0, s[8:11], s55 offen offset:1024 sc1\n"
"v_add_u32 v32, 0x10000, v32\n"
"v_mfma_f32_16x16x32_bf16 v[212:215], v[80:83], a[68:71], v[212:215]\n"
"s_add_u32 s56, s56, 2048\n"
"v_mfma_f32_16x16x32_bf16 v[216:219], v[80:83], a[72:75], v[216:219]\n"
"v_mfma_f32_16x16x32_bf16 v[216:219], v[84:87], a[88:91], v[216:219]\n"
"v_mfma_f32_16x16x32_bf16 v[220:223], v[84:87], a[92:95], v[220:223]\n"
"buffer_load_dwordx4 a[196:199], v0, s[8:11], s56 offen sc1\n"
"v_mfma_f32_16x16x32_bf16 v[220:223], v[80:83], a[76:79], v[220:223]\n"
"s_waitcnt lgkmcnt(8)\n"
"v_mfma_f32_16x16x32_bf16 v[144:147], v[88:91], a[64:67], v[144:147]\n"
"ds_read_b64_tr_b16 v[80:81], v33 offset:49152\n"
"ds_read_b64_tr_b16 v[82:83], v33 offset:53248\n"
"v_mfma_f32_16x16x32_bf16 v[144:147], v[92:95], a[80:83], v[144:147]\n"
"ds_read_b64_tr_b16 v[84:85], v33 offset:57344\n"
"ds_read_b64_tr_b16 v[86:87], v33 offset:61440\n"
"v_mfma_f32_16x16x32_bf16 v[148:151], v[92:95], a[84:87], v[148:151]\n"
"v_mfma_f32_16x16x32_bf16 v[148:151], v[88:91], a[68:71], v[148:151]\n"
"buffer_load_dwordx4 a[212:215], v0, s[8:11], s56 offen offset:1024 sc1\n"
"v_mfma_f32_16x16x32_bf16 v[152:155], v[88:91], a[72:75], v[152:155]\n"
"s_add_u32 s57, s57, 2048\n"
"v_mfma_f32_16x16x32_bf16 v[152:155], v[92:95], a[88:91], v[152:155]\n"
"v_mfma_f32_16x16x32_bf16 v[156:159], v[92:95], a[92:95], v[156:159]\n"
"v_mfma_f32_16x16x32_bf16 v[156:159], v[88:91], a[76:79], v[156:159]\n"
"s_waitcnt vmcnt(20) lgkmcnt(8)\n"
"v_mfma_f32_16x16x32_bf16 v[160:163], v[64:67], a[96:99], v[160:163]\n"
"ds_read_b64_tr_b16 v[88:89], v33 offset:49160\n"
"ds_read_b64_tr_b16 v[90:91], v33 offset:53256\n"
"v_mfma_f32_16x16x32_bf16 v[160:163], v[68:71], a[112:115], v[160:163]\n"
"ds_read_b64_tr_b16 v[92:93], v33 offset:57352\n"
"ds_read_b64_tr_b16 v[94:95], v33 offset:61448\n"
"v_mfma_f32_16x16x32_bf16 v[164:167], v[68:71], a[116:119], v[164:167]\n"
"buffer_load_dwordx4 a[200:203], v0, s[8:11], s57 offen sc1\n"
"v_add_u32 v33, 0x10000, v33\n"
"v_mfma_f32_16x16x32_bf16 v[164:167], v[64:67], a[100:103], v[164:167]\n"
"v_mfma_f32_16x16x32_bf16 v[168:171], v[64:67], a[104:107], v[168:171]\n"
"v_mfma_f32_16x16x32_bf16 v[168:171], v[68:71], a[120:123], v[168:171]\n"
"v_mfma_f32_16x16x32_bf16 v[172:175], v[68:71], a[124:127], v[172:175]\n"
"buffer_load_dwordx4 a[216:219], v0, s[8:11], s57 offen offset:1024 sc1\n"
"v_mfma_f32_16x16x32_bf16 v[172:175], v[64:67], a[108:111], v[172:175]\n"
"s_waitcnt lgkmcnt(8)\n"
"v_mfma_f32_16x16x32_bf16 v[96:99], v[72:75], a[96:99], v[96:99]\n"
"s_add_u32 s58, s58, 2048\n"
"ds_read_b64_tr_b16 v[64:65], v34 offset:49152\n"
"ds_read_b64_tr_b16 v[66:67], v34 offset:53248\n"
"v_mfma_f32_16x16x32_bf16 v[96:99], v[76:79], a[112:115], v[96:99]\n"
"ds_read_b64_tr_b16 v[68:69], v34 offset:57344\n"
"ds_read_b64_tr_b16 v[70:71], v34 offset:61440\n"
"v_mfma_f32_16x16x32_bf16 v[100:103], v[76:79], a[116:119], v[100:103]\n"
"v_mfma_f32_16x16x32_bf16 v[100:103], v[72:75], a[100:103], v[100:103]\n"
"buffer_load_dwordx4 a[204:207], v0, s[8:11], s58 offen sc1\n"
"v_mfma_f32_16x16x32_bf16 v[104:107], v[72:75], a[104:107], v[104:107]\n"
"v_mfma_f32_16x16x32_bf16 v[104:107], v[76:79], a[120:123], v[104:107]\n"
"v_mfma_f32_16x16x32_bf16 v[108:111], v[76:79], a[124:127], v[108:111]\n"
"v_mfma_f32_16x16x32_bf16 v[108:111], v[72:75], a[108:111], v[108:111]\n"
"s_waitcnt lgkmcnt(8)\n"
"v_mfma_f32_16x16x32_bf16 v[176:179], v[80:83], a[96:99], v[176:179]\n"
"ds_read_b64_tr_b16 v[72:73], v34 offset:49160\n"
"ds_read_b64_tr_b16 v[74:75], v34 offset:53256\n"
"v_mfma_f32_16x16x32_bf16 v[176:179], v[84:87], a[112:115], v[176:179]\n"
"ds_read_b64_tr_b16 v[76:77], v34 offset:57352\n"
"ds_read_b64_tr_b16 v[78:79], v34 offset:61448\n"
"v_mfma_f32_16x16x32_bf16 v[180:183], v[84:87], a[116:119], v[180:183]\n"
"buffer_load_dwordx4 a[220:223], v0, s[8:11], s58 offen offset:1024 sc1\n"
"v_add_u32 v34, 0x10000, v34\n"
"v_mfma_f32_16x16x32_bf16 v[180:183], v[80:83], a[100:103], v[180:183]\n"
"s_add_u32 s55, s55, 2048\n"
"v_mfma_f32_16x16x32_bf16 v[184:187], v[80:83], a[104:107], v[184:187]\n"
"v_mfma_f32_16x16x32_bf16 v[184:187], v[84:87], a[120:123], v[184:187]\n"
"v_mfma_f32_16x16x32_bf16 v[188:191], v[84:87], a[124:127], v[188:191]\n"
"buffer_load_dwordx4 a[224:227], v0, s[8:11], s55 offen sc1\n"
"v_mfma_f32_16x16x32_bf16 v[188:191], v[80:83], a[108:111], v[188:191]\n"
"s_waitcnt lgkmcnt(8)\n"
"v_mfma_f32_16x16x32_bf16 v[112:115], v[88:91], a[96:99], v[112:115]\n"
"ds_read_b64_tr_b16 v[80:81], v35 offset:49152\n"
"ds_read_b64_tr_b16 v[82:83], v35 offset:53248\n"
"v_mfma_f32_16x16x32_bf16 v[112:115], v[92:95], a[112:115], v[112:115]\n"
"ds_read_b64_tr_b16 v[84:85], v35 offset:57344\n"
"ds_read_b64_tr_b16 v[86:87], v35 offset:61440\n"
"v_mfma_f32_16x16x32_bf16 v[116:119], v[92:95], a[116:119], v[116:119]\n"
"v_mfma_f32_16x16x32_bf16 v[116:119], v[88:91], a[100:103], v[116:119]\n"
"buffer_load_dwordx4 a[240:243], v0, s[8:11], s55 offen offset:1024 sc1\n"
"v_mfma_f32_16x16x32_bf16 v[120:123], v[88:91], a[104:107], v[120:123]\n"
"s_add_u32 s56, s56, 2048\n"
"v_mfma_f32_16x16x32_bf16 v[120:123], v[92:95], a[120:123], v[120:123]\n"
"v_mfma_f32_16x16x32_bf16 v[124:127], v[92:95], a[124:127], v[124:127]\n"
"v_mfma_f32_16x16x32_bf16 v[124:127], v[88:91], a[108:111], v[124:127]\n"
"s_waitcnt lgkmcnt(8)\n"
"v_mfma_f32_16x16x32_bf16 v[192:195], v[64:67], a[96:99], v[192:195]\n"
"ds_read_b64_tr_b16 v[88:89], v35 offset:49160\n"
"ds_read_b64_tr_b16 v[90:91], v35 offset:53256\n"
"v_mfma_f32_16x16x32_bf16 v[192:195], v[68:71], a[112:115], v[192:195]\n"
"ds_read_b64_tr_b16 v[92:93], v35 offset:57352\n"
"ds_read_b64_tr_b16 v[94:95], v35 offset:61448\n"
"v_mfma_f32_16x16x32_bf16 v[196:199], v[68:71], a[116:119], v[196:199]\n"
"buffer_load_dwordx4 a[228:231], v0, s[8:11], s56 offen sc1\n"
"v_add_u32 v35, 0x10000, v35\n"
"v_mfma_f32_16x16x32_bf16 v[196:199], v[64:67], a[100:103], v[196:199]\n"
"v_mfma_f32_16x16x32_bf16 v[200:203], v[64:67], a[104:107], v[200:203]\n"
"v_mfma_f32_16x16x32_bf16 v[200:203], v[68:71], a[120:123], v[200:203]\n"
"buffer_load_dwordx4 a[244:247], v0, s[8:11], s56 offen offset:1024 sc1\n"
"v_mfma_f32_16x16x32_bf16 v[204:207], v[68:71], a[124:127], v[204:207]\n"
"s_add_u32 s57, s57, 2048\n"
"v_mfma_f32_16x16x32_bf16 v[204:207], v[64:67], a[108:111], v[204:207]\n"
"s_waitcnt lgkmcnt(8)\n"
"v_mfma_f32_16x16x32_bf16 v[128:131], v[72:75], a[96:99], v[128:131]\n"
"ds_read_b64_tr_b16 v[64:65], v32 offset:0\n"
"ds_read_b64_tr_b16 v[66:67], v32 offset:4096\n"
"v_mfma_f32_16x16x32_bf16 v[128:131], v[76:79], a[112:115], v[128:131]\n"
"ds_read_b64_tr_b16 v[68:69], v32 offset:8192\n"
"ds_read_b64_tr_b16 v[70:71], v32 offset:12288\n"
"v_mfma_f32_16x16x32_bf16 v[132:135], v[76:79], a[116:119], v[132:135]\n"
"v_mfma_f32_16x16x32_bf16 v[132:135], v[72:75], a[100:103], v[132:135]\n"
"buffer_load_dwordx4 a[232:235], v0, s[8:11], s57 offen sc1\n"
"v_mfma_f32_16x16x32_bf16 v[136:139], v[72:75], a[104:107], v[136:139]\n"
"v_mfma_f32_16x16x32_bf16 v[136:139], v[76:79], a[120:123], v[136:139]\n"
"v_mfma_f32_16x16x32_bf16 v[140:143], v[76:79], a[124:127], v[140:143]\n"
"v_mfma_f32_16x16x32_bf16 v[140:143], v[72:75], a[108:111], v[140:143]\n"
"s_waitcnt lgkmcnt(8)\n"
"v_mfma_f32_16x16x32_bf16 v[208:211], v[80:83], a[96:99], v[208:211]\n"
"ds_read_b64_tr_b16 v[72:73], v32 offset:8\n"
"ds_read_b64_tr_b16 v[74:75], v32 offset:4104\n"
"v_mfma_f32_16x16x32_bf16 v[208:211], v[84:87], a[112:115], v[208:211]\n"
"ds_read_b64_tr_b16 v[76:77], v32 offset:8200\n"
"ds_read_b64_tr_b16 v[78:79], v32 offset:12296\n"
"v_mfma_f32_16x16x32_bf16 v[212:215], v[84:87], a[116:119], v[212:215]\n"
"buffer_load_dwordx4 a[248:251], v0, s[8:11], s57 offen offset:1024 sc1\n"
"v_add_u32 v32, s89, v32\n"
"v_mfma_f32_16x16x32_bf16 v[212:215], v[80:83], a[100:103], v[212:215]\n"
"s_add_u32 s58, s58, 2048\n"
"v_mfma_f32_16x16x32_bf16 v[216:219], v[80:83], a[104:107], v[216:219]\n"
"v_mfma_f32_16x16x32_bf16 v[216:219], v[84:87], a[120:123], v[216:219]\n"
"buffer_load_dwordx4 a[236:239], v0, s[8:11], s58 offen sc1\n"
"v_mfma_f32_16x16x32_bf16 v[220:223], v[84:87], a[124:127], v[220:223]\n"
"v_mfma_f32_16x16x32_bf16 v[220:223], v[80:83], a[108:111], v[220:223]\n"
"s_waitcnt lgkmcnt(8)\n"
"v_mfma_f32_16x16x32_bf16 v[144:147], v[88:91], a[96:99], v[144:147]\n"
"ds_read_b64_tr_b16 v[80:81], v33 offset:0\n"
"ds_read_b64_tr_b16 v[82:83], v33 offset:4096\n"
"v_mfma_f32_16x16x32_bf16 v[144:147], v[92:95], a[112:115], v[144:147]\n"
"ds_read_b64_tr_b16 v[84:85], v33 offset:8192\n"
"ds_read_b64_tr_b16 v[86:87], v33 offset:12288\n"
"v_mfma_f32_16x16x32_bf16 v[148:151], v[92:95], a[116:119], v[148:151]\n"
"v_mfma_f32_16x16x32_bf16 v[148:151], v[88:91], a[100:103], v[148:151]\n"
"buffer_load_dwordx4 a[252:255], v0, s[8:11], s58 offen offset:1024 sc1\n"
"v_mfma_f32_16x16x32_bf16 v[152:155], v[88:91], a[104:107], v[152:155]\n"
"s_add_u32 s55, s55, 2048\n"
"v_mfma_f32_16x16x32_bf16 v[152:155], v[92:95], a[120:123], v[152:155]\n"
"v_mfma_f32_16x16x32_bf16 v[156:159], v[92:95], a[124:127], v[156:159]\n"
"v_mfma_f32_16x16x32_bf16 v[156:159], v[88:91], a[108:111], v[156:159]\n"
"s_waitcnt vmcnt(24) lgkmcnt(8)\n"
"v_mfma_f32_16x16x32_bf16 v[160:163], v[64:67], a[128:131], v[160:163]\n"
"ds_read_b64_tr_b16 v[88:89], v33 offset:8\n"
"ds_read_b64_tr_b16 v[90:91], v33 offset:4104\n"
"v_mfma_f32_16x16x32_bf16 v[160:163], v[68:71], a[144:147], v[160:163]\n"
"ds_read_b64_tr_b16 v[92:93], v33 offset:8200\n"
"ds_read_b64_tr_b16 v[94:95], v33 offset:12296\n"
"v_mfma_f32_16x16x32_bf16 v[164:167], v[68:71], a[148:151], v[164:167]\n"
"buffer_load_dwordx4 v[224:227], v0, s[8:11], s55 offen sc1\n"
"v_add_u32 v33, s89, v33\n"
"v_mfma_f32_16x16x32_bf16 v[164:167], v[64:67], a[132:135], v[164:167]\n"
"v_mfma_f32_16x16x32_bf16 v[168:171], v[64:67], a[136:139], v[168:171]\n"
"v_mfma_f32_16x16x32_bf16 v[168:171], v[68:71], a[152:155], v[168:171]\n"
"buffer_load_dwordx4 v[240:243], v0, s[8:11], s55 offen offset:1024 sc1\n"
"v_mfma_f32_16x16x32_bf16 v[172:175], v[68:71], a[156:159], v[172:175]\n"
"s_add_u32 s56, s56, 2048\n"
"v_mfma_f32_16x16x32_bf16 v[172:175], v[64:67], a[140:143], v[172:175]\n"
"s_waitcnt lgkmcnt(8)\n"
"v_mfma_f32_16x16x32_bf16 v[96:99], v[72:75], a[128:131], v[96:99]\n"
"ds_read_b64_tr_b16 v[64:65], v34 offset:0\n"
"ds_read_b64_tr_b16 v[66:67], v34 offset:4096\n"
"v_mfma_f32_16x16x32_bf16 v[96:99], v[76:79], a[144:147], v[96:99]\n"
"ds_read_b64_tr_b16 v[68:69], v34 offset:8192\n"
"ds_read_b64_tr_b16 v[70:71], v34 offset:12288\n"
"v_mfma_f32_16x16x32_bf16 v[100:103], v[76:79], a[148:151], v[100:103]\n"
"v_mfma_f32_16x16x32_bf16 v[100:103], v[72:75], a[132:135], v[100:103]\n"
"buffer_load_dwordx4 v[228:231], v0, s[8:11], s56 offen sc1\n"
"v_mfma_f32_16x16x32_bf16 v[104:107], v[72:75], a[136:139], v[104:107]\n"
"v_mfma_f32_16x16x32_bf16 v[104:107], v[76:79], a[152:155], v[104:107]\n"
"v_mfma_f32_16x16x32_bf16 v[108:111], v[76:79], a[156:159], v[108:111]\n"
"v_mfma_f32_16x16x32_bf16 v[108:111], v[72:75], a[140:143], v[108:111]\n"
"s_waitcnt lgkmcnt(8)\n"
"v_mfma_f32_16x16x32_bf16 v[176:179], v[80:83], a[128:131], v[176:179]\n"
"ds_read_b64_tr_b16 v[72:73], v34 offset:8\n"
"ds_read_b64_tr_b16 v[74:75], v34 offset:4104\n"
"v_mfma_f32_16x16x32_bf16 v[176:179], v[84:87], a[144:147], v[176:179]\n"
"ds_read_b64_tr_b16 v[76:77], v34 offset:8200\n"
"ds_read_b64_tr_b16 v[78:79], v34 offset:12296\n"
"v_mfma_f32_16x16x32_bf16 v[180:183], v[84:87], a[148:151], v[180:183]\n"
"buffer_load_dwordx4 v[244:247], v0, s[8:11], s56 offen offset:1024 sc1\n"
"v_add_u32 v34, s89, v34\n"
"v_mfma_f32_16x16x32_bf16 v[180:183], v[80:83], a[132:135], v[180:183]\n"
"s_add_u32 s57, s57, 2048\n"
"v_mfma_f32_16x16x32_bf16 v[184:187], v[80:83], a[136:139], v[184:187]\n"
"v_mfma_f32_16x16x32_bf16 v[184:187], v[84:87], a[152:155], v[184:187]\n"
"buffer_load_dwordx4 v[232:235], v0, s[8:11], s57 offen sc1\n"
"v_mfma_f32_16x16x32_bf16 v[188:191], v[84:87], a[156:159], v[188:191]\n"
"v_mfma_f32_16x16x32_bf16 v[188:191], v[80:83], a[140:143], v[188:191]\n"
"s_waitcnt lgkmcnt(8)\n"
"v_mfma_f32_16x16x32_bf16 v[112:115], v[88:91], a[128:131], v[112:115]\n"
"ds_read_b64_tr_b16 v[80:81], v35 offset:0\n"
"ds_read_b64_tr_b16 v[82:83], v35 offset:4096\n"
"v_mfma_f32_16x16x32_bf16 v[112:115], v[92:95], a[144:147], v[112:115]\n"
"ds_read_b64_tr_b16 v[84:85], v35 offset:8192\n"
"ds_read_b64_tr_b16 v[86:87], v35 offset:12288\n"
"v_mfma_f32_16x16x32_bf16 v[116:119], v[92:95], a[148:151], v[116:119]\n"
"buffer_load_dwordx4 v[248:251], v0, s[8:11], s57 offen offset:1024 sc1\n"
"v_mfma_f32_16x16x32_bf16 v[116:119], v[88:91], a[132:135], v[116:119]\n"
"s_add_u32 s58, s58, 2048\n"
"v_mfma_f32_16x16x32_bf16 v[120:123], v[88:91], a[136:139], v[120:123]\n"
"v_mfma_f32_16x16x32_bf16 v[120:123], v[92:95], a[152:155], v[120:123]\n"
"v_mfma_f32_16x16x32_bf16 v[124:127], v[92:95], a[156:159], v[124:127]\n"
"v_mfma_f32_16x16x32_bf16 v[124:127], v[88:91], a[140:143], v[124:127]\n"
"s_waitcnt lgkmcnt(8)\n"
"v_mfma_f32_16x16x32_bf16 v[192:195], v[64:67], a[128:131], v[192:195]\n"
"ds_read_b64_tr_b16 v[88:89], v35 offset:8\n"
"ds_read_b64_tr_b16 v[90:91], v35 offset:4104\n"
"v_mfma_f32_16x16x32_bf16 v[192:195], v[68:71], a[144:147], v[192:195]\n"
"ds_read_b64_tr_b16 v[92:93], v35 offset:8200\n"
"ds_read_b64_tr_b16 v[94:95], v35 offset:12296\n"
"v_mfma_f32_16x16x32_bf16 v[196:199], v[68:71], a[148:151], v[196:199]\n"
"buffer_load_dwordx4 v[236:239], v0, s[8:11], s58 offen sc1\n"
"v_add_u32 v35, s89, v35\n"
"v_mfma_f32_16x16x32_bf16 v[196:199], v[64:67], a[132:135], v[196:199]\n"
"v_mfma_f32_16x16x32_bf16 v[200:203], v[64:67], a[136:139], v[200:203]\n"
"v_mfma_f32_16x16x32_bf16 v[200:203], v[68:71], a[152:155], v[200:203]\n"
"buffer_load_dwordx4 v[252:255], v0, s[8:11], s58 offen offset:1024 sc1\n"
"v_mfma_f32_16x16x32_bf16 v[204:207], v[68:71], a[156:159], v[204:207]\n"
"s_mul_i32 s34, s52, 1056\n"
"v_mfma_f32_16x16x32_bf16 v[204:207], v[64:67], a[140:143], v[204:207]\n"
"s_waitcnt lgkmcnt(0)\n"
"v_mfma_f32_16x16x32_bf16 v[128:131], v[72:75], a[128:131], v[128:131]\n"
"s_barrier\n"
"ds_read_b64_tr_b16 v[64:65], v32 offset:0\n"
"ds_read_b64_tr_b16 v[66:67], v32 offset:4096\n"
"v_mfma_f32_16x16x32_bf16 v[128:131], v[76:79], a[144:147], v[128:131]\n"
"s_add_u32 m0, s34, 147456\n"
"ds_read_b64_tr_b16 v[68:69], v32 offset:8192\n"
"ds_read_b64_tr_b16 v[70:71], v32 offset:12288\n"
"v_mfma_f32_16x16x32_bf16 v[132:135], v[76:79], a[148:151], v[132:135]\n"
"s_lshl_b32 s34, s52, 10\n"
"v_mfma_f32_16x16x32_bf16 v[132:135], v[72:75], a[132:135], v[132:135]\n"
"buffer_load_dwordx4 v0, s[24:27], s34 offen lds\n"
"v_mfma_f32_16x16x32_bf16 v[136:139], v[72:75], a[136:139], v[136:139]\n"
"s_mul_i32 s60, s52, 9216\n"
"v_mfma_f32_16x16x32_bf16 v[136:139], v[76:79], a[152:155], v[136:139]\n"
"s_add_u32 s60, s60, s88\n"
"v_mfma_f32_16x16x32_bf16 v[140:143], v[76:79], a[156:159], v[140:143]\n"
"s_mov_b32 m0, s60\n"
"v_mfma_f32_16x16x32_bf16 v[140:143], v[72:75], a[140:143], v[140:143]\n"
"s_waitcnt lgkmcnt(8)\n"
"v_mfma_f32_16x16x32_bf16 v[208:211], v[80:83], a[128:131], v[208:211]\n"
"s_mov_b32 s59, s86\n"
"ds_read_b64_tr_b16 v[72:73], v32 offset:8\n"
"ds_read_b64_tr_b16 v[74:75], v32 offset:4104\n"
"v_mfma_f32_16x16x32_bf16 v[208:211], v[84:87], a[144:147], v[208:211]\n"
"ds_read_b64_tr_b16 v[76:77], v32 offset:8200\n"
"ds_read_b64_tr_b16 v[78:79], v32 offset:12296\n"
"v_mfma_f32_16x16x32_bf16 v[212:215], v[84:87], a[148:151], v[212:215]\n"
"buffer_load_dwordx4 v2, s[16:19], s59 offen lds\n"
"v_mfma_f32_16x16x32_bf16 v[212:215], v[80:83], a[132:135], v[212:215]\n"
"s_add_u32 m0, m0, 1024\n"
"v_mfma_f32_16x16x32_bf16 v[216:219], v[80:83], a[136:139], v[216:219]\n"
"s_add_u32 s59, s59, 147456\n"
"v_mfma_f32_16x16x32_bf16 v[216:219], v[84:87], a[152:155], v[216:219]\n"
"v_mfma_f32_16x16x32_bf16 v[220:223], v[84:87], a[156:159], v[220:223]\n"
"buffer_load_dwordx4 v2, s[16:19], s59 offen lds\n"
"v_mfma_f32_16x16x32_bf16 v[220:223], v[80:83], a[140:143], v[220:223]\n"
"s_waitcnt lgkmcnt(8)\n"
"v_mfma_f32_16x16x32_bf16 v[144:147], v[88:91], a[128:131], v[144:147]\n"
"s_add_u32 m0, m0, 1024\n"
"ds_read_b64_tr_b16 v[80:81], v33 offset:0\n"
"ds_read_b64_tr_b16 v[82:83], v33 offset:4096\n"
"v_mfma_f32_16x16x32_bf16 v[144:147], v[92:95], a[144:147], v[144:147]\n"
"s_add_u32 s59, s59, 147456\n"
"ds_read_b64_tr_b16 v[84:85], v33 offset:8192\n"
"ds_read_b64_tr_b16 v[86:87], v33 offset:12288\n"
"v_mfma_f32_16x16x32_bf16 v[148:151], v[92:95], a[148:151], v[148:151]\n"
"v_mfma_f32_16x16x32_bf16 v[148:151], v[88:91], a[132:135], v[148:151]\n"
"v_mfma_f32_16x16x32_bf16 v[152:155], v[88:91], a[136:139], v[152:155]\n"
"v_mfma_f32_16x16x32_bf16 v[152:155], v[92:95], a[152:155], v[152:155]\n"
"v_mfma_f32_16x16x32_bf16 v[156:159], v[92:95], a[156:159], v[156:159]\n"
"v_mfma_f32_16x16x32_bf16 v[156:159], v[88:91], a[140:143], v[156:159]\n"
"s_waitcnt vmcnt(27) lgkmcnt(8)\n"
"v_mfma_f32_16x16x32_bf16 v[160:163], v[64:67], a[160:163], v[160:163]\n"
"ds_read_b64_tr_b16 v[88:89], v33 offset:8\n"
"ds_read_b64_tr_b16 v[90:91], v33 offset:4104\n"
"v_mfma_f32_16x16x32_bf16 v[160:163], v[68:71], a[176:179], v[160:163]\n"
"ds_read_b64_tr_b16 v[92:93], v33 offset:8200\n"
"ds_read_b64_tr_b16 v[94:95], v33 offset:12296\n"
"v_mfma_f32_16x16x32_bf16 v[164:167], v[68:71], a[180:183], v[164:167]\n"
"buffer_load_dwordx4 v2, s[16:19], s59 offen lds\n"
"v_mfma_f32_16x16x32_bf16 v[164:167], v[64:67], a[164:167], v[164:167]\n"
"s_add_u32 m0, m0, 1024\n"
"v_mfma_f32_16x16x32_bf16 v[168:171], v[64:67], a[168:171], v[168:171]\n"
"s_add_u32 s59, s59, 147456\n"
"v_mfma_f32_16x16x32_bf16 v[168:171], v[68:71], a[184:187], v[168:171]\n"
"v_mfma_f32_16x16x32_bf16 v[172:175], v[68:71], a[188:191], v[172:175]\n"
"v_mfma_f32_16x16x32_bf16 v[172:175], v[64:67], a[172:175], v[172:175]\n"
"s_waitcnt lgkmcnt(8)\n"
"v_mfma_f32_16x16x32_bf16 v[96:99], v[72:75], a[160:163], v[96:99]\n"
"ds_read_b64_tr_b16 v[64:65], v34 offset:0\n"
"ds_read_b64_tr_b16 v[66:67], v34 offset:4096\n"
"v_mfma_f32_16x16x32_bf16 v[96:99], v[76:79], a[176:179], v[96:99]\n"
"ds_read_b64_tr_b16 v[68:69], v34 offset:8192\n"
"ds_read_b64_tr_b16 v[70:71], v34 offset:12288\n"
"v_mfma_f32_16x16x32_bf16 v[100:103], v[76:79], a[180:183], v[100:103]\n"
"buffer_load_dwordx4 v2, s[16:19], s59 offen lds\n"
"v_mfma_f32_16x16x32_bf16 v[100:103], v[72:75], a[164:167], v[100:103]\n"
"s_add_u32 m0, m0, 1024\n"
"v_mfma_f32_16x16x32_bf16 v[104:107], v[72:75], a[168:171], v[104:107]\n"
"s_add_u32 s59, s59, 147456\n"
"v_mfma_f32_16x16x32_bf16 v[104:107], v[76:79], a[184:187], v[104:107]\n"
"v_mfma_f32_16x16x32_bf16 v[108:111], v[76:79], a[188:191], v[108:111]\n"
"v_mfma_f32_16x16x32_bf16 v[108:111], v[72:75], a[172:175], v[108:111]\n"
"s_waitcnt lgkmcnt(8)\n"
"v_mfma_f32_16x16x32_bf16 v[176:179], v[80:83], a[160:163], v[176:179]\n"
"ds_read_b64_tr_b16 v[72:73], v34 offset:8\n"
"ds_read_b64_tr_b16 v[74:75], v34 offset:4104\n"
"v_mfma_f32_16x16x32_bf16 v[176:179], v[84:87], a[176:179], v[176:179]\n"
"ds_read_b64_tr_b16 v[76:77], v34 offset:8200\n"
"ds_read_b64_tr_b16 v[78:79], v34 offset:12296\n"
"v_mfma_f32_16x16x32_bf16 v[180:183], v[84:87], a[180:183], v[180:183]\n"
"v_mfma_f32_16x16x32_bf16 v[180:183], v[80:83], a[164:167], v[180:183]\n"
"v_mfma_f32_16x16x32_bf16 v[184:187], v[80:83], a[168:171], v[184:187]\n"
"buffer_load_dwordx4 v2, s[16:19], s59 offen lds\n"
"v_mfma_f32_16x16x32_bf16 v[184:187], v[84:87], a[184:187], v[184:187]\n"
"s_add_u32 m0, m0, 1024\n"
"v_mfma_f32_16x16x32_bf16 v[188:191], v[84:87], a[188:191], v[188:191]\n"
"s_add_u32 s59, s59, 147456\n"
"v_mfma_f32_16x16x32_bf16 v[188:191], v[80:83], a[172:175], v[188:191]\n"
"s_waitcnt lgkmcnt(8)\n"
"v_mfma_f32_16x16x32_bf16 v[112:115], v[88:91], a[160:163], v[112:115]\n"
"ds_read_b64_tr_b16 v[80:81], v35 offset:0\n"
"ds_read_b64_tr_b16 v[82:83], v35 offset:4096\n"
"v_mfma_f32_16x16x32_bf16 v[112:115], v[92:95], a[176:179], v[112:115]\n"
"ds_read_b64_tr_b16 v[84:85], v35 offset:8192\n"
"ds_read_b64_tr_b16 v[86:87], v35 offset:12288\n"
"v_mfma_f32_16x16x32_bf16 v[116:119], v[92:95], a[180:183], v[116:119]\n"
"v_mfma_f32_16x16x32_bf16 v[116:119], v[88:91], a[164:167], v[116:119]\n"
"v_mfma_f32_16x16x32_bf16 v[120:123], v[88:91], a[168:171], v[120:123]\n"
"v_mfma_f32_16x16x32_bf16 v[120:123], v[92:95], a[184:187], v[120:123]\n"
"v_mfma_f32_16x16x32_bf16 v[124:127], v[92:95], a[188:191], v[124:127]\n"
"buffer_load_dwordx4 v2, s[16:19], s59 offen lds\n"
"v_mfma_f32_16x16x32_bf16 v[124:127], v[88:91], a[172:175], v[124:127]\n"
"s_waitcnt lgkmcnt(8)\n"
"v_mfma_f32_16x16x32_bf16 v[192:195], v[64:67], a[160:163], v[192:195]\n"
"s_add_u32 m0, m0, 1024\n"
"ds_read_b64_tr_b16 v[88:89], v35 offset:8\n"
"ds_read_b64_tr_b16 v[90:91], v35 offset:4104\n"
"v_mfma_f32_16x16x32_bf16 v[192:195], v[68:71], a[176:179], v[192:195]\n"
"s_add_u32 s59, s59, 147456\n"
"ds_read_b64_tr_b16 v[92:93], v35 offset:8200\n"
"ds_read_b64_tr_b16 v[94:95], v35 offset:12296\n"
"v_mfma_f32_16x16x32_bf16 v[196:199], v[68:71], a[180:183], v[196:199]\n"
"v_mfma_f32_16x16x32_bf16 v[196:199], v[64:67], a[164:167], v[196:199]\n"
"v_mfma_f32_16x16x32_bf16 v[200:203], v[64:67], a[168:171], v[200:203]\n"
"v_mfma_f32_16x16x32_bf16 v[200:203], v[68:71], a[184:187], v[200:203]\n"
"v_mfma_f32_16x16x32_bf16 v[204:207], v[68:71], a[188:191], v[204:207]\n"
"v_mfma_f32_16x16x32_bf16 v[204:207], v[64:67], a[172:175], v[204:207]\n"
"s_waitcnt lgkmcnt(8)\n"
"v_mfma_f32_16x16x32_bf16 v[128:131], v[72:75], a[160:163], v[128:131]\n"
"ds_read_b64_tr_b16 v[64:65], v32 offset:16384\n"
"ds_read_b64_tr_b16 v[66:67], v32 offset:20480\n"
"v_mfma_f32_16x16x32_bf16 v[128:131], v[76:79], a[176:179], v[128:131]\n"
"ds_read_b64_tr_b16 v[68:69], v32 offset:24576\n"
"ds_read_b64_tr_b16 v[70:71], v32 offset:28672\n"
"v_mfma_f32_16x16x32_bf16 v[132:135], v[76:79], a[180:183], v[132:135]\n"
"buffer_load_dwordx4 v2, s[16:19], s59 offen lds\n"
"v_mfma_f32_16x16x32_bf16 v[132:135], v[72:75], a[164:167], v[132:135]\n"
"s_add_u32 m0, m0, 1024\n"
"v_mfma_f32_16x16x32_bf16 v[136:139], v[72:75], a[168:171], v[136:139]\n"
"s_add_u32 s59, s59, 147456\n"
"v_mfma_f32_16x16x32_bf16 v[136:139], v[76:79], a[184:187], v[136:139]\n"
"v_mfma_f32_16x16x32_bf16 v[140:143], v[76:79], a[188:191], v[140:143]\n"
"v_mfma_f32_16x16x32_bf16 v[140:143], v[72:75], a[172:175], v[140:143]\n"
"s_waitcnt lgkmcnt(8)\n"
"v_mfma_f32_16x16x32_bf16 v[208:211], v[80:83], a[160:163], v[208:211]\n"
"ds_read_b64_tr_b16 v[72:73], v32 offset:16392\n"
"ds_read_b64_tr_b16 v[74:75], v32 offset:20488\n"
"v_mfma_f32_16x16x32_bf16 v[208:211], v[84:87], a[176:179], v[208:211]\n"
"ds_read_b64_tr_b16 v[76:77], v32 offset:24584\n"
"ds_read_b64_tr_b16 v[78:79], v32 offset:28680\n"
"v_mfma_f32_16x16x32_bf16 v[212:215], v[84:87], a[180:183], v[212:215]\n"
"buffer_load_dwordx4 v2, s[16:19], s59 offen lds\n"
"v_mfma_f32_16x16x32_bf16 v[212:215], v[80:83], a[164:167], v[212:215]\n"
"s_add_u32 m0, m0, 1024\n"
"v_mfma_f32_16x16x32_bf16 v[216:219], v[80:83], a[168:171], v[216:219]\n"
"s_add_u32 s59, s59, 147456\n"
"v_mfma_f32_16x16x32_bf16 v[216:219], v[84:87], a[184:187], v[216:219]\n"
"v_mfma_f32_16x16x32_bf16 v[220:223], v[84:87], a[188:191], v[220:223]\n"
"v_mfma_f32_16x16x32_bf16 v[220:223], v[80:83], a[172:175], v[220:223]\n"
"s_waitcnt lgkmcnt(8)\n"
"v_mfma_f32_16x16x32_bf16 v[144:147], v[88:91], a[160:163], v[144:147]\n"
"ds_read_b64_tr_b16 v[80:81], v33 offset:16384\n"
"ds_read_b64_tr_b16 v[82:83], v33 offset:20480\n"
"v_mfma_f32_16x16x32_bf16 v[144:147], v[92:95], a[176:179], v[144:147]\n"
"ds_read_b64_tr_b16 v[84:85], v33 offset:24576\n"
"ds_read_b64_tr_b16 v[86:87], v33 offset:28672\n"
"v_mfma_f32_16x16x32_bf16 v[148:151], v[92:95], a[180:183], v[148:151]\n"
"v_mfma_f32_16x16x32_bf16 v[148:151], v[88:91], a[164:167], v[148:151]\n"
"v_mfma_f32_16x16x32_bf16 v[152:155], v[88:91], a[168:171], v[152:155]\n"
"buffer_load_dwordx4 v2, s[16:19], s59 offen lds\n"
"v_mfma_f32_16x16x32_bf16 v[152:155], v[92:95], a[184:187], v[152:155]\n"
"s_add_u32 m0, m0, 28672\n"
"v_mfma_f32_16x16x32_bf16 v[156:159], v[92:95], a[188:191], v[156:159]\n"
"s_sub_u32 s59, s59, 1179648\n"
"v_mfma_f32_16x16x32_bf16 v[156:159], v[88:91], a[172:175], v[156:159]\n"
"s_waitcnt vmcnt(26) lgkmcnt(8)\n"
"v_mfma_f32_16x16x32_bf16 v[160:163], v[64:67], a[192:195], v[160:163]\n"
"ds_read_b64_tr_b16 v[88:89], v33 offset:16392\n"
"ds_read_b64_tr_b16 v[90:91], v33 offset:20488\n"
"v_mfma_f32_16x16x32_bf16 v[160:163], v[68:71], a[208:211], v[160:163]\n"
"ds_read_b64_tr_b16 v[92:93], v33 offset:24584\n"
"ds_read_b64_tr_b16 v[94:95], v33 offset:28680\n"
"v_mfma_f32_16x16x32_bf16 v[164:167], v[68:71], a[212:215], v[164:167]\n"
"v_mfma_f32_16x16x32_bf16 v[164:167], v[64:67], a[196:199], v[164:167]\n"
"v_mfma_f32_16x16x32_bf16 v[168:171], v[64:67], a[200:203], v[168:171]\n"
"v_mfma_f32_16x16x32_bf16 v[168:171], v[68:71], a[216:219], v[168:171]\n"
"v_mfma_f32_16x16x32_bf16 v[172:175], v[68:71], a[220:223], v[172:175]\n"
"buffer_load_dwordx4 v2, s[20:23], s59 offen lds\n"
"v_mfma_f32_16x16x32_bf16 v[172:175], v[64:67], a[204:207], v[172:175]\n"
"s_waitcnt lgkmcnt(8)\n"
"v_mfma_f32_16x16x32_bf16 v[96:99], v[72:75], a[192:195], v[96:99]\n"
"s_add_u32 m0, m0, 1024\n"
"ds_read_b64_tr_b16 v[64:65], v34 offset:16384\n"
"ds_read_b64_tr_b16 v[66:67], v34 offset:20480\n"
"v_mfma_f32_16x16x32_bf16 v[96:99], v[76:79], a[208:211], v[96:99]\n"
"s_add_u32 s59, s59, 147456\n"
"ds_read_b64_tr_b16 v[68:69], v34 offset:24576\n"
"ds_read_b64_tr_b16 v[70:71], v34 offset:28672\n"
"v_mfma_f32_16x16x32_bf16 v[100:103], v[76:79], a[212:215], v[100:103]\n"
"v_mfma_f32_16x16x32_bf16 v[100:103], v[72:75], a[196:199], v[100:103]\n"
"v_mfma_f32_16x16x32_bf16 v[104:107], v[72:75], a[200:203], v[104:107]\n"
"v_mfma_f32_16x16x32_bf16 v[104:107], v[76:79], a[216:219], v[104:107]\n"
"v_mfma_f32_16x16x32_bf16 v[108:111], v[76:79], a[220:223], v[108:111]\n"
"v_mfma_f32_16x16x32_bf16 v[108:111], v[72:75], a[204:207], v[108:111]\n"
"s_waitcnt lgkmcnt(8)\n"
"v_mfma_f32_16x16x32_bf16 v[176:179], v[80:83], a[192:195], v[176:179]\n"
"ds_read_b64_tr_b16 v[72:73], v34 offset:16392\n"
"ds_read_b64_tr_b16 v[74:75], v34 offset:20488\n"
"v_mfma_f32_16x16x32_bf16 v[176:179], v[84:87], a[208:211], v[176:179]\n"
"ds_read_b64_tr_b16 v[76:77], v34 offset:24584\n"
"ds_read_b64_tr_b16 v[78:79], v34 offset:28680\n"
"v_mfma_f32_16x16x32_bf16 v[180:183], v[84:87], a[212:215], v[180:183]\n"
"buffer_load_dwordx4 v2, s[20:23], s59 offen lds\n"
"v_mfma_f32_16x16x32_bf16 v[180:183], v[80:83], a[196:199], v[180:183]\n"
"s_add_u32 m0, m0, 1024\n"
"v_mfma_f32_16x16x32_bf16 v[184:187], v[80:83], a[200:203], v[184:187]\n"
"s_add_u32 s59, s59, 147456\n"
"v_mfma_f32_16x16x32_bf16 v[184:187], v[84:87], a[216:219], v[184:187]\n"
"v_mfma_f32_16x16x32_bf16 v[188:191], v[84:87], a[220:223], v[188:191]\n"
"v_mfma_f32_16x16x32_bf16 v[188:191], v[80:83], a[204:207], v[188:191]\n"
"s_waitcnt lgkmcnt(8)\n"
"v_mfma_f32_16x16x32_bf16 v[112:115], v[88:91], a[192:195], v[112:115]\n"
"ds_read_b64_tr_b16 v[80:81], v35 offset:16384\n"
"ds_read_b64_tr_b16 v[82:83], v35 offset:20480\n"
"v_mfma_f32_16x16x32_bf16 v[112:115], v[92:95], a[208:211], v[112:115]\n"
"ds_read_b64_tr_b16 v[84:85], v35 offset:24576\n"
"ds_read_b64_tr_b16 v[86:87], v35 offset:28672\n"
"v_mfma_f32_16x16x32_bf16 v[116:119], v[92:95], a[212:215], v[116:119]\n"
"buffer_load_dwordx4 v2, s[20:23], s59 offen lds\n"
"v_mfma_f32_16x16x32_bf16 v[116:119], v[88:91], a[196:199], v[116:119]\n"
"s_add_u32 m0, m0, 1024\n"
"v_mfma_f32_16x16x32_bf16 v[120:123], v[88:91], a[200:203], v[120:123]\n"
"s_add_u32 s59, s59, 147456\n"
"v_mfma_f32_16x16x32_bf16 v[120:123], v[92:95], a[216:219], v[120:123]\n"
"v_mfma_f32_16x16x32_bf16 v[124:127], v[92:95], a[220:223], v[124:127]\n"
"v_mfma_f32_16x16x32_bf16 v[124:127], v[88:91], a[204:207], v[124:127]\n"
"s_waitcnt lgkmcnt(8)\n"
"v_mfma_f32_16x16x32_bf16 v[192:195], v[64:67], a[192:195], v[192:195]\n"
"ds_read_b64_tr_b16 v[88:89], v35 offset:16392\n"
"ds_read_b64_tr_b16 v[90:91], v35 offset:20488\n"
"v_mfma_f32_16x16x32_bf16 v[192:195], v[68:71], a[208:211], v[192:195]\n"
"ds_read_b64_tr_b16 v[92:93], v35 offset:24584\n"
"ds_read_b64_tr_b16 v[94:95], v35 offset:28680\n"
"v_mfma_f32_16x16x32_bf16 v[196:199], v[68:71], a[212:215], v[196:199]\n"
"v_mfma_f32_16x16x32_bf16 v[196:199], v[64:67], a[196:199], v[196:199]\n"
"v_mfma_f32_16x16x32_bf16 v[200:203], v[64:67], a[200:203], v[200:203]\n"
"buffer_load_dwordx4 v2, s[20:23], s59 offen lds\n"
"v_mfma_f32_16x16x32_bf16 v[200:203], v[68:71], a[216:219], v[200:203]\n"
"s_add_u32 m0, m0, 1024\n"
"v_mfma_f32_16x16x32_bf16 v[204:207], v[68:71], a[220:223], v[204:207]\n"
"s_add_u32 s59, s59, 147456\n"
"v_mfma_f32_16x16x32_bf16 v[204:207], v[64:67], a[204:207], v[204:207]\n"
"s_waitcnt lgkmcnt(8)\n"
"v_mfma_f32_16x16x32_bf16 v[128:131], v[72:75], a[192:195], v[128:131]\n"
"ds_read_b64_tr_b16 v[64:65], v32 offset:32768\n"
"ds_read_b64_tr_b16 v[66:67], v32 offset:36864\n"
"v_mfma_f32_16x16x32_bf16 v[128:131], v[76:79], a[208:211], v[128:131]\n"
"ds_read_b64_tr_b16 v[68:69], v32 offset:40960\n"
"ds_read_b64_tr_b16 v[70:71], v32 offset:45056\n"
"v_mfma_f32_16x16x32_bf16 v[132:135], v[76:79], a[212:215], v[132:135]\n"
"v_mfma_f32_16x16x32_bf16 v[132:135], v[72:75], a[196:199], v[132:135]\n"
"v_mfma_f32_16x16x32_bf16 v[136:139], v[72:75], a[200:203], v[136:139]\n"
"v_mfma_f32_16x16x32_bf16 v[136:139], v[76:79], a[216:219], v[136:139]\n"
"v_mfma_f32_16x16x32_bf16 v[140:143], v[76:79], a[220:223], v[140:143]\n"
"buffer_load_dwordx4 v2, s[20:23], s59 offen lds\n"
"v_mfma_f32_16x16x32_bf16 v[140:143], v[72:75], a[204:207], v[140:143]\n"
"s_waitcnt lgkmcnt(8)\n"
"v_mfma_f32_16x16x32_bf16 v[208:211], v[80:83], a[192:195], v[208:211]\n"
"s_add_u32 m0, m0, 1024\n"
"ds_read_b64_tr_b16 v[72:73], v32 offset:32776\n"
"ds_read_b64_tr_b16 v[74:75], v32 offset:36872\n"
"v_mfma_f32_16x16x32_bf16 v[208:211], v[84:87], a[208:211], v[208:211]\n"
"s_add_u32 s59, s59, 147456\n"
"ds_read_b64_tr_b16 v[76:77], v32 offset:40968\n"
"ds_read_b64_tr_b16 v[78:79], v32 offset:45064\n"
"v_mfma_f32_16x16x32_bf16 v[212:215], v[84:87], a[212:215], v[212:215]\n"
"v_mfma_f32_16x16x32_bf16 v[212:215], v[80:83], a[196:199], v[212:215]\n"
"v_mfma_f32_16x16x32_bf16 v[216:219], v[80:83], a[200:203], v[216:219]\n"
"v_mfma_f32_16x16x32_bf16 v[216:219], v[84:87], a[216:219], v[216:219]\n"
"v_mfma_f32_16x16x32_bf16 v[220:223], v[84:87], a[220:223], v[220:223]\n"
"v_mfma_f32_16x16x32_bf16 v[220:223], v[80:83], a[204:207], v[220:223]\n"
"s_waitcnt lgkmcnt(8)\n"
"v_mfma_f32_16x16x32_bf16 v[144:147], v[88:91], a[192:195], v[144:147]\n"
"ds_read_b64_tr_b16 v[80:81], v33 offset:32768\n"
"ds_read_b64_tr_b16 v[82:83], v33 offset:36864\n"
"v_mfma_f32_16x16x32_bf16 v[144:147], v[92:95], a[208:211], v[144:147]\n"
"ds_read_b64_tr_b16 v[84:85], v33 offset:40960\n"
"ds_read_b64_tr_b16 v[86:87], v33 offset:45056\n"
"v_mfma_f32_16x16x32_bf16 v[148:151], v[92:95], a[212:215], v[148:151]\n"
"buffer_load_dwordx4 v2, s[20:23], s59 offen lds\n"
"v_mfma_f32_16x16x32_bf16 v[148:151], v[88:91], a[196:199], v[148:151]\n"
"s_add_u32 m0, m0, 1024\n"
"v_mfma_f32_16x16x32_bf16 v[152:155], v[88:91], a[200:203], v[152:155]\n"
"s_add_u32 s59, s59, 147456\n"
"v_mfma_f32_16x16x32_bf16 v[152:155], v[92:95], a[216:219], v[152:155]\n"
"v_mfma_f32_16x16x32_bf16 v[156:159], v[92:95], a[220:223], v[156:159]\n"
"v_mfma_f32_16x16x32_bf16 v[156:159], v[88:91], a[204:207], v[156:159]\n"
"s_waitcnt vmcnt(24) lgkmcnt(8)\n"
"v_mfma_f32_16x16x32_bf16 v[160:163], v[64:67], a[224:227], v[160:163]\n"
"ds_read_b64_tr_b16 v[88:89], v33 offset:32776\n"
"ds_read_b64_tr_b16 v[90:91], v33 offset:36872\n"
"v_mfma_f32_16x16x32_bf16 v[160:163], v[68:71], a[240:243], v[160:163]\n"
"ds_read_b64_tr_b16 v[92:93], v33 offset:40968\n"
"ds_read_b64_tr_b16 v[94:95], v33 offset:45064\n"
"v_mfma_f32_16x16x32_bf16 v[164:167], v[68:71], a[244:247], v[164:167]\n"
"buffer_load_dwordx4 v2, s[20:23], s59 offen lds\n"
"v_mfma_f32_16x16x32_bf16 v[164:167], v[64:67], a[228:231], v[164:167]\n"
"s_add_u32 m0, m0, 1024\n"
"v_mfma_f32_16x16x32_bf16 v[168:171], v[64:67], a[232:235], v[168:171]\n"
"s_add_u32 s59, s59, 147456\n"
"v_mfma_f32_16x16x32_bf16 v[168:171], v[68:71], a[248:251], v[168:171]\n"
"v_mfma_f32_16x16x32_bf16 v[172:175], v[68:71], a[252:255], v[172:175]\n"
"v_mfma_f32_16x16x32_bf16 v[172:175], v[64:67], a[236:239], v[172:175]\n"
"s_waitcnt lgkmcnt(8)\n"
"v_mfma_f32_16x16x32_bf16 v[96:99], v[72:75], a[224:227], v[96:99]\n"
"ds_read_b64_tr_b16 v[64:65], v34 offset:32768\n"
"ds_read_b64_tr_b16 v[66:67], v34 offset:36864\n"
"v_mfma_f32_16x16x32_bf16 v[96:99], v[76:79], a[240:243], v[96:99]\n"
"ds_read_b64_tr_b16 v[68:69], v34 offset:40960\n"
"ds_read_b64_tr_b16 v[70:71], v34 offset:45056\n"
"v_mfma_f32_16x16x32_bf16 v[100:103], v[76:79], a[244:247], v[100:103]\n"
"v_mfma_f32_16x16x32_bf16 v[100:103], v[72:75], a[228:231], v[100:103]\n"
"v_mfma_f32_16x16x32_bf16 v[104:107], v[72:75], a[232:235], v[104:107]\n"
"buffer_load_dwordx4 v2, s[20:23], s59 offen lds\n"
"v_mfma_f32_16x16x32_bf16 v[104:107], v[76:79], a[248:251], v[104:107]\n"
"s_add_u32 m0, m0, 1024\n"
"v_mfma_f32_16x16x32_bf16 v[108:111], v[76:79], a[252:255], v[108:111]\n"
"s_add_u32 s59, s59, 147456\n"
"v_mfma_f32_16x16x32_bf16 v[108:111], v[72:75], a[236:239], v[108:111]\n"
"s_waitcnt lgkmcnt(8)\n"
"v_mfma_f32_16x16x32_bf16 v[176:179], v[80:83], a[224:227], v[176:179]\n"
"ds_read_b64_tr_b16 v[72:73], v34 offset:32776\n"
"ds_read_b64_tr_b16 v[74:75], v34 offset:36872\n"
"v_mfma_f32_16x16x32_bf16 v[176:179], v[84:87], a[240:243], v[176:179]\n"
"ds_read_b64_tr_b16 v[76:77], v34 offset:40968\n"
"ds_read_b64_tr_b16 v[78:79], v34 offset:45064\n"
"v_mfma_f32_16x16x32_bf16 v[180:183], v[84:87], a[244:247], v[180:183]\n"
"v_mfma_f32_16x16x32_bf16 v[180:183], v[80:83], a[228:231], v[180:183]\n"
"v_mfma_f32_16x16x32_bf16 v[184:187], v[80:83], a[232:235], v[184:187]\n"
"v_mfma_f32_16x16x32_bf16 v[184:187], v[84:87], a[248:251], v[184:187]\n"
"v_mfma_f32_16x16x32_bf16 v[188:191], v[84:87], a[252:255], v[188:191]\n"
"buffer_load_dwordx4 v2, s[20:23], s59 offen lds\n"
"v_mfma_f32_16x16x32_bf16 v[188:191], v[80:83], a[236:239], v[188:191]\n"
"s_waitcnt lgkmcnt(8)\n"
"v_mfma_f32_16x16x32_bf16 v[112:115], v[88:91], a[224:227], v[112:115]\n"
"s_add_u32 s60, s60, s61\n"
"ds_read_b64_tr_b16 v[80:81], v35 offset:32768\n"
"ds_read_b64_tr_b16 v[82:83], v35 offset:36864\n"
"v_mfma_f32_16x16x32_bf16 v[112:115], v[92:95], a[240:243], v[112:115]\n"
"s_add_u32 s86, s86, 64\n"
"ds_read_b64_tr_b16 v[84:85], v35 offset:40960\n"
"ds_read_b64_tr_b16 v[86:87], v35 offset:45056\n"
"v_mfma_f32_16x16x32_bf16 v[116:119], v[92:95], a[244:247], v[116:119]\n"
"s_xor_b32 s34, s54, 0\n"
"v_mfma_f32_16x16x32_bf16 v[116:119], v[88:91], a[228:231], v[116:119]\n"
"s_mul_i32 s34, s34, 144\n"
"v_mfma_f32_16x16x32_bf16 v[120:123], v[88:91], a[232:235], v[120:123]\n"
"s_add_u32 s34, s34, s97\n"
"v_mfma_f32_16x16x32_bf16 v[120:123], v[92:95], a[248:251], v[120:123]\n"
"s_lshl_b32 s55, s34, 10\n"
"v_mfma_f32_16x16x32_bf16 v[124:127], v[92:95], a[252:255], v[124:127]\n"
"s_xor_b32 s34, s54, 1\n"
"v_mfma_f32_16x16x32_bf16 v[124:127], v[88:91], a[236:239], v[124:127]\n"
"s_waitcnt lgkmcnt(8)\n"
"v_mfma_f32_16x16x32_bf16 v[192:195], v[64:67], a[224:227], v[192:195]\n"
"s_mul_i32 s34, s34, 144\n"
"ds_read_b64_tr_b16 v[88:89], v35 offset:32776\n"
"ds_read_b64_tr_b16 v[90:91], v35 offset:36872\n"
"v_mfma_f32_16x16x32_bf16 v[192:195], v[68:71], a[240:243], v[192:195]\n"
"s_add_u32 s34, s34, s97\n"
"ds_read_b64_tr_b16 v[92:93], v35 offset:40968\n"
"ds_read_b64_tr_b16 v[94:95], v35 offset:45064\n"
"v_mfma_f32_16x16x32_bf16 v[196:199], v[68:71], a[244:247], v[196:199]\n"
"s_lshl_b32 s56, s34, 10\n"
"v_mfma_f32_16x16x32_bf16 v[196:199], v[64:67], a[228:231], v[196:199]\n"
"s_xor_b32 s34, s54, 2\n"
"v_mfma_f32_16x16x32_bf16 v[200:203], v[64:67], a[232:235], v[200:203]\n"
"s_mul_i32 s34, s34, 144\n"
"v_mfma_f32_16x16x32_bf16 v[200:203], v[68:71], a[248:251], v[200:203]\n"
"s_add_u32 s34, s34, s97\n"
"v_mfma_f32_16x16x32_bf16 v[204:207], v[68:71], a[252:255], v[204:207]\n"
"s_lshl_b32 s57, s34, 10\n"
"v_mfma_f32_16x16x32_bf16 v[204:207], v[64:67], a[236:239], v[204:207]\n"
"s_waitcnt lgkmcnt(8)\n"
"v_mfma_f32_16x16x32_bf16 v[128:131], v[72:75], a[224:227], v[128:131]\n"
"s_xor_b32 s34, s54, 3\n"
"ds_read_b64_tr_b16 v[64:65], v32 offset:49152\n"
"ds_read_b64_tr_b16 v[66:67], v32 offset:53248\n"
"v_mfma_f32_16x16x32_bf16 v[128:131], v[76:79], a[240:243], v[128:131]\n"
"s_mul_i32 s34, s34, 144\n"
"ds_read_b64_tr_b16 v[68:69], v32 offset:57344\n"
"ds_read_b64_tr_b16 v[70:71], v32 offset:61440\n"
"v_mfma_f32_16x16x32_bf16 v[132:135], v[76:79], a[244:247], v[132:135]\n"
"s_add_u32 s34, s34, s97\n"
"v_mfma_f32_16x16x32_bf16 v[132:135], v[72:75], a[228:231], v[132:135]\n"
"s_lshl_b32 s58, s34, 10\n"
"v_mfma_f32_16x16x32_bf16 v[136:139], v[72:75], a[232:235], v[136:139]\n"
"s_bfe_u32 s12, s83, 0x80000\n"
"v_mfma_f32_16x16x32_bf16 v[136:139], v[76:79], a[248:251], v[136:139]\n"
"s_bfe_u32 s13, s83, 0x80008\n"
"v_mfma_f32_16x16x32_bf16 v[140:143], v[76:79], a[252:255], v[140:143]\n"
"s_bfe_u32 s14, s83, 0x40010\n"
"v_mfma_f32_16x16x32_bf16 v[140:143], v[72:75], a[236:239], v[140:143]\n"
"s_waitcnt lgkmcnt(8)\n"
"v_mfma_f32_16x16x32_bf16 v[208:211], v[80:83], a[224:227], v[208:211]\n"
"s_bfe_u32 s15, s83, 0x80018\n"
"ds_read_b64_tr_b16 v[72:73], v32 offset:49160\n"
"ds_read_b64_tr_b16 v[74:75], v32 offset:53256\n"
"v_mfma_f32_16x16x32_bf16 v[208:211], v[84:87], a[240:243], v[208:211]\n"
"s_bfe_u32 s98, s83, 0x20014\n"
"ds_read_b64_tr_b16 v[76:77], v32 offset:57352\n"
"ds_read_b64_tr_b16 v[78:79], v32 offset:61448\n"
"v_mfma_f32_16x16x32_bf16 v[212:215], v[84:87], a[244:247], v[212:215]\n"
"s_lshl_b32 s98, s98, 8\n"
"v_mfma_f32_16x16x32_bf16 v[212:215], v[80:83], a[228:231], v[212:215]\n"
"s_add_u32 s12, s12, s98\n"
"v_mfma_f32_16x16x32_bf16 v[216:219], v[80:83], a[232:235], v[216:219]\n"
"s_add_u32 s13, s13, s98\n"
"global_load_dword v41, v60, s[4:5]\n"
"v_mfma_f32_16x16x32_bf16 v[216:219], v[84:87], a[248:251], v[216:219]\n"
"s_bfe_u32 s98, s12, 0x50002\n"
"v_mfma_f32_16x16x32_bf16 v[220:223], v[84:87], a[252:255], v[220:223]\n"
"s_mul_i32 s99, s98, 9\n"
"v_mfma_f32_16x16x32_bf16 v[220:223], v[80:83], a[236:239], v[220:223]\n"
"s_waitcnt lgkmcnt(8)\n"
"v_mfma_f32_16x16x32_bf16 v[144:147], v[88:91], a[224:227], v[144:147]\n"
"s_mul_i32 s83, s98, 7\n"
"ds_read_b64_tr_b16 v[80:81], v33 offset:49152\n"
"ds_read_b64_tr_b16 v[82:83], v33 offset:53248\n"
"v_mfma_f32_16x16x32_bf16 v[144:147], v[92:95], a[240:243], v[144:147]\n"
"s_add_u32 s83, s83, 24\n"
"ds_read_b64_tr_b16 v[84:85], v33 offset:57344\n"
"ds_read_b64_tr_b16 v[86:87], v33 offset:61440\n"
"v_mfma_f32_16x16x32_bf16 v[148:151], v[92:95], a[244:247], v[148:151]\n"
"s_min_u32 s99, s99, s83\n"
"v_mfma_f32_16x16x32_bf16 v[148:151], v[88:91], a[228:231], v[148:151]\n"
"s_add_u32 s99, s99, s14\n"
"v_mfma_f32_16x16x32_bf16 v[152:155], v[88:91], a[232:235], v[152:155]\n"
"s_cmp_lt_u32 s14, 7\n"
"v_mfma_f32_16x16x32_bf16 v[152:155], v[92:95], a[248:251], v[152:155]\n"
"s_cselect_b32 s50, 14, 12\n"
"v_mfma_f32_16x16x32_bf16 v[156:159], v[92:95], a[252:255], v[156:159]\n"
"s_add_u32 s83, s98, s15\n"
"v_mfma_f32_16x16x32_bf16 v[156:159], v[88:91], a[236:239], v[156:159]\n"
"s_waitcnt vmcnt(20) lgkmcnt(8)\n"
"v_mfma_f32_16x16x32_bf16 v[160:163], v[64:67], v[224:227], v[160:163]\n"
"s_cmp_lt_u32 s98, s50\n"
"ds_read_b64_tr_b16 v[88:89], v33 offset:49160\n"
"ds_read_b64_tr_b16 v[90:91], v33 offset:53256\n"
"v_mfma_f32_16x16x32_bf16 v[160:163], v[68:71], v[240:243], v[160:163]\n"
"s_cselect_b32 s99, s99, s83\n"
"ds_read_b64_tr_b16 v[92:93], v33 offset:57352\n"
"ds_read_b64_tr_b16 v[94:95], v33 offset:61448\n"
"v_mfma_f32_16x16x32_bf16 v[164:167], v[68:71], v[244:247], v[164:167]\n"
"s_and_b32 s98, s12, 3\n"
"v_mfma_f32_16x16x32_bf16 v[164:167], v[64:67], v[228:231], v[164:167]\n"
"s_lshl2_add_u32 s99, s99, s98\n"
"v_mfma_f32_16x16x32_bf16 v[168:171], v[64:67], v[232:235], v[168:171]\n"
"s_lshl_b32 s83, s99, 10\n"
"v_cvt_pk_bf16_f32 v160, v160, v161\n"
"v_cvt_pk_bf16_f32 v161, v162, v163\n"
"v_mfma_f32_16x16x32_bf16 v[168:171], v[68:71], v[248:251], v[168:171]\n"
"s_cmp_lt_u32 s12, s13\n"
"v_mfma_f32_16x16x32_bf16 v[172:175], v[68:71], v[252:255], v[172:175]\n"
"s_cselect_b32 s50, 626688, 0\n"
"v_cvt_pk_bf16_f32 v164, v164, v165\n"
"v_cvt_pk_bf16_f32 v165, v166, v167\n"
"v_mfma_f32_16x16x32_bf16 v[172:175], v[64:67], v[236:239], v[172:175]\n"
"s_waitcnt lgkmcnt(8)\n"
"v_mfma_f32_16x16x32_bf16 v[96:99], v[72:75], v[224:227], v[96:99]\n"
"s_add_u32 s12, s12, 1\n"
"ds_read_b64_tr_b16 v[64:65], v34 offset:49152\n"
"ds_read_b64_tr_b16 v[66:67], v34 offset:53248\n"
"v_mfma_f32_16x16x32_bf16 v[96:99], v[76:79], v[240:243], v[96:99]\n"
"ds_read_b64_tr_b16 v[68:69], v34 offset:57344\n"
"ds_read_b64_tr_b16 v[70:71], v34 offset:61440\n"
"v_mfma_f32_16x16x32_bf16 v[100:103], v[76:79], v[244:247], v[100:103]\n"
"global_load_dword v41, v61, s[4:5]\n"
"v_cvt_pk_bf16_f32 v168, v168, v169\n"
"v_mfma_f32_16x16x32_bf16 v[100:103], v[72:75], v[228:231], v[100:103]\n"
"v_cvt_pk_bf16_f32 v169, v170, v171\n"
"v_cvt_pk_bf16_f32 v172, v172, v173\n"
"v_mfma_f32_16x16x32_bf16 v[104:107], v[72:75], v[232:235], v[104:107]\n"
"v_cvt_pk_bf16_f32 v173, v174, v175\n"
"v_cvt_pk_bf16_f32 v162, v96, v97\n"
"v_mfma_f32_16x16x32_bf16 v[104:107], v[76:79], v[248:251], v[104:107]\n"
"v_cvt_pk_bf16_f32 v163, v98, v99\n"
"v_mfma_f32_16x16x32_bf16 v[108:111], v[76:79], v[252:255], v[108:111]\n"
"v_cvt_pk_bf16_f32 v166, v100, v101\n"
"v_cvt_pk_bf16_f32 v167, v102, v103\n"
"v_mfma_f32_16x16x32_bf16 v[108:111], v[72:75], v[236:239], v[108:111]\n"
"s_waitcnt lgkmcnt(8)\n"
"v_mfma_f32_16x16x32_bf16 v[176:179], v[80:83], v[224:227], v[176:179]\n"
"ds_read_b64_tr_b16 v[72:73], v34 offset:49160\n"
"ds_read_b64_tr_b16 v[74:75], v34 offset:53256\n"
"v_mfma_f32_16x16x32_bf16 v[176:179], v[84:87], v[240:243], v[176:179]\n"
"ds_read_b64_tr_b16 v[76:77], v34 offset:57352\n"
"ds_read_b64_tr_b16 v[78:79], v34 offset:61448\n"
"v_mfma_f32_16x16x32_bf16 v[180:183], v[84:87], v[244:247], v[180:183]\n"
"v_cvt_pk_bf16_f32 v170, v104, v105\n"
"v_cvt_pk_bf16_f32 v171, v106, v107\n"
"v_mfma_f32_16x16x32_bf16 v[180:183], v[80:83], v[228:231], v[180:183]\n"
"v_cvt_pk_bf16_f32 v174, v108, v109\n"
"v_cvt_pk_bf16_f32 v175, v110, v111\n"
"v_mfma_f32_16x16x32_bf16 v[184:187], v[80:83], v[232:235], v[184:187]\n"
"v_cvt_pk_bf16_f32 v176, v176, v177\n"
"v_cvt_pk_bf16_f32 v177, v178, v179\n"
"v_mfma_f32_16x16x32_bf16 v[184:187], v[84:87], v[248:251], v[184:187]\n"
"v_mfma_f32_16x16x32_bf16 v[188:191], v[84:87], v[252:255], v[188:191]\n"
"v_cvt_pk_bf16_f32 v180, v180, v181\n"
"v_cvt_pk_bf16_f32 v181, v182, v183\n"
"v_mfma_f32_16x16x32_bf16 v[188:191], v[80:83], v[236:239], v[188:191]\n"
"s_waitcnt lgkmcnt(8)\n"
"v_mfma_f32_16x16x32_bf16 v[112:115], v[88:91], v[224:227], v[112:115]\n"
"ds_read_b64_tr_b16 v[80:81], v35 offset:49152\n"
"ds_read_b64_tr_b16 v[82:83], v35 offset:53248\n"
"v_mfma_f32_16x16x32_bf16 v[112:115], v[92:95], v[240:243], v[112:115]\n"
"ds_read_b64_tr_b16 v[84:85], v35 offset:57344\n"
"ds_read_b64_tr_b16 v[86:87], v35 offset:61440\n"
"v_mfma_f32_16x16x32_bf16 v[116:119], v[92:95], v[244:247], v[116:119]\n"
"v_cvt_pk_bf16_f32 v184, v184, v185\n"
"v_cvt_pk_bf16_f32 v185, v186, v187\n"
"v_mfma_f32_16x16x32_bf16 v[116:119], v[88:91], v[228:231], v[116:119]\n"
"v_cvt_pk_bf16_f32 v188, v188, v189\n"
"v_cvt_pk_bf16_f32 v189, v190, v191\n"
"v_mfma_f32_16x16x32_bf16 v[120:123], v[88:91], v[232:235], v[120:123]\n"
"v_cvt_pk_bf16_f32 v178, v112, v113\n"
"v_cvt_pk_bf16_f32 v179, v114, v115\n"
"v_mfma_f32_16x16x32_bf16 v[120:123], v[92:95], v[248:251], v[120:123]\n"
"v_mfma_f32_16x16x32_bf16 v[124:127], v[92:95], v[252:255], v[124:127]\n"
"global_load_dword v41, v62, s[4:5]\n"
"v_cvt_pk_bf16_f32 v182, v116, v117\n"
"v_mfma_f32_16x16x32_bf16 v[124:127], v[88:91], v[236:239], v[124:127]\n"
"s_waitcnt lgkmcnt(8)\n"
"v_cvt_pk_bf16_f32 v183, v118, v119\n"
"v_mfma_f32_16x16x32_bf16 v[192:195], v[64:67], v[224:227], v[192:195]\n"
"ds_read_b64_tr_b16 v[88:89], v35 offset:49160\n"
"ds_read_b64_tr_b16 v[90:91], v35 offset:53256\n"
"v_mfma_f32_16x16x32_bf16 v[192:195], v[68:71], v[240:243], v[192:195]\n"
"ds_read_b64_tr_b16 v[92:93], v35 offset:57352\n"
"ds_read_b64_tr_b16 v[94:95], v35 offset:61448\n"
"v_mfma_f32_16x16x32_bf16 v[196:199], v[68:71], v[244:247], v[196:199]\n"
"v_cvt_pk_bf16_f32 v186, v120, v121\n"
"v_cvt_pk_bf16_f32 v187, v122, v123\n"
"v_mfma_f32_16x16x32_bf16 v[196:199], v[64:67], v[228:231], v[196:199]\n"
"v_cvt_pk_bf16_f32 v190, v124, v125\n"
"v_cvt_pk_bf16_f32 v191, v126, v127\n"
"v_mfma_f32_16x16x32_bf16 v[200:203], v[64:67], v[232:235], v[200:203]\n"
"v_cvt_pk_bf16_f32 v192, v192, v193\n"
"v_cvt_pk_bf16_f32 v193, v194, v195\n"
"v_mfma_f32_16x16x32_bf16 v[200:203], v[68:71], v[248:251], v[200:203]\n"
"v_mfma_f32_16x16x32_bf16 v[204:207], v[68:71], v[252:255], v[204:207]\n"
"v_cvt_pk_bf16_f32 v196, v196, v197\n"
"v_cvt_pk_bf16_f32 v197, v198, v199\n"
"v_mfma_f32_16x16x32_bf16 v[204:207], v[64:67], v[236:239], v[204:207]\n"
"s_waitcnt vmcnt(21) lgkmcnt(8)\n"
"v_mfma_f32_16x16x32_bf16 v[128:131], v[72:75], v[224:227], v[128:131]\n"
"s_barrier\n"
"ds_read_b128 v[48:51], v40 offset:0\n"
"v_cvt_pk_bf16_f32 v200, v200, v201\n"
"v_mfma_f32_16x16x32_bf16 v[128:131], v[76:79], v[240:243], v[128:131]\n"
"ds_read_b128 v[52:55], v40 offset:64\n"
"v_cvt_pk_bf16_f32 v201, v202, v203\n"
"v_mfma_f32_16x16x32_bf16 v[132:135], v[76:79], v[244:247], v[132:135]\n"
"ds_read_b128 v[56:59], v40 offset:128\n"
"v_cvt_pk_bf16_f32 v204, v204, v205\n"
"v_mfma_f32_16x16x32_bf16 v[132:135], v[72:75], v[228:231], v[132:135]\n"
"ds_read_b128 v[60:63], v40 offset:192\n"
"v_cvt_pk_bf16_f32 v205, v206, v207\n"
"v_mfma_f32_16x16x32_bf16 v[136:139], v[72:75], v[232:235], v[136:139]\n"
"v_cvt_pk_bf16_f32 v194, v128, v129\n"
"v_cvt_pk_bf16_f32 v195, v130, v131\n"
"v_mfma_f32_16x16x32_bf16 v[136:139], v[76:79], v[248:251], v[136:139]\n"
"v_mfma_f32_16x16x32_bf16 v[140:143], v[76:79], v[252:255], v[140:143]\n"
"v_cvt_pk_bf16_f32 v198, v132, v133\n"
"v_cvt_pk_bf16_f32 v199, v134, v135\n"
"v_mfma_f32_16x16x32_bf16 v[140:143], v[72:75], v[236:239], v[140:143]\n"
"s_waitcnt lgkmcnt(8)\n"
"v_mfma_f32_16x16x32_bf16 v[208:211], v[80:83], v[224:227], v[208:211]\n"
"v_cvt_pk_bf16_f32 v202, v136, v137\n"
"v_cvt_pk_bf16_f32 v203, v138, v139\n"
"v_mfma_f32_16x16x32_bf16 v[208:211], v[84:87], v[240:243], v[208:211]\n"
"v_mfma_f32_16x16x32_bf16 v[212:215], v[84:87], v[244:247], v[212:215]\n"
"s_bitcmp1_b32 s53, 0\n"
"s_cselect_b32 s4, s28, s24\n"
"s_cselect_b32 s5, s29, s25\n"
"v_mfma_f32_16x16x32_bf16 v[212:215], v[80:83], v[228:231], v[212:215]\n"
"s_lshr_b32 s35, s6, 5\n"
"v_cvt_pk_bf16_f32 v206, v140, v141\n"
"v_cvt_pk_bf16_f32 v207, v142, v143\n"
"v_mfma_f32_16x16x32_bf16 v[216:219], v[80:83], v[232:235], v[216:219]\n"
"s_lshl2_add_u32 s35, s35, s52\n"
"v_cvt_pk_bf16_f32 v208, v208, v209\n"
"v_cvt_pk_bf16_f32 v209, v210, v211\n"
"v_mfma_f32_16x16x32_bf16 v[216:219], v[84:87], v[248:251], v[216:219]\n"
"s_lshl_b32 s35, s35, 12\n"
"v_mfma_f32_16x16x32_bf16 v[220:223], v[84:87], v[252:255], v[220:223]\n"
"s_add_u32 s4, s4, s35\n"
"s_addc_u32 s5, s5, 0\n"
"v_cvt_pk_bf16_f32 v212, v212, v213\n"
"v_mfma_f32_16x16x32_bf16 v[220:223], v[80:83], v[236:239], v[220:223]\n"
"s_waitcnt lgkmcnt(4)\n"
"v_cvt_pk_bf16_f32 v213, v214, v215\n"
"v_mfma_f32_16x16x32_bf16 v[144:147], v[88:91], v[224:227], v[144:147]\n"
"v_cvt_pk_bf16_f32 v216, v216, v217\n"
"v_cvt_pk_bf16_f32 v217, v218, v219\n"
"v_mfma_f32_16x16x32_bf16 v[144:147], v[92:95], v[240:243], v[144:147]\n"
"v_mfma_f32_16x16x32_bf16 v[148:151], v[92:95], v[244:247], v[148:151]\n"
"v_cvt_pk_bf16_f32 v220, v220, v221\n"
"v_cvt_pk_bf16_f32 v221, v222, v223\n"
"v_mfma_f32_16x16x32_bf16 v[148:151], v[88:91], v[228:231], v[148:151]\n"
"v_mfma_f32_16x16x32_bf16 v[152:155], v[88:91], v[232:235], v[152:155]\n"
"v_cvt_pk_bf16_f32 v210, v144, v145\n"
"v_cvt_pk_bf16_f32 v211, v146, v147\n"
"v_mfma_f32_16x16x32_bf16 v[152:155], v[92:95], v[248:251], v[152:155]\n"
"v_mfma_f32_16x16x32_bf16 v[156:159], v[92:95], v[252:255], v[156:159]\n"
"v_cvt_pk_bf16_f32 v214, v148, v149\n"
"v_cvt_pk_bf16_f32 v215, v150, v151\n"
"v_mfma_f32_16x16x32_bf16 v[156:159], v[88:91], v[236:239], v[156:159]\n"
"s_waitcnt vmcnt(3) lgkmcnt(0)\n"
"v_mfma_f32_16x16x32_bf16 v[8:11], v[48:51], v[160:163], 0\n"
"s_barrier\n"
"ds_read_b64_tr_b16 v[64:65], v36 offset:0\n"
"v_cvt_pk_bf16_f32 v218, v152, v153\n"
"v_mfma_f32_16x16x32_bf16 v[8:11], v[52:55], v[176:179], v[8:11]\n"
"ds_read_b64_tr_b16 v[66:67], v36 offset:1024\n"
"v_cvt_pk_bf16_f32 v219, v154, v155\n"
"v_mfma_f32_16x16x32_bf16 v[8:11], v[56:59], v[192:195], v[8:11]\n"
"ds_read_b64_tr_b16 v[68:69], v36 offset:2048\n"
"v_cvt_pk_bf16_f32 v222, v156, v157\n"
"v_mfma_f32_16x16x32_bf16 v[8:11], v[60:63], v[208:211], v[8:11]\n"
"ds_read_b64_tr_b16 v[70:71], v36 offset:3072\n"
"v_cvt_pk_bf16_f32 v223, v158, v159\n"
"v_mfma_f32_16x16x32_bf16 v[12:15], v[48:51], v[164:167], 0\n"
"ds_read_b64_tr_b16 v[72:73], v36 offset:4096\n"
"v_mfma_f32_16x16x32_bf16 v[12:15], v[52:55], v[180:183], v[12:15]\n"
"ds_read_b64_tr_b16 v[74:75], v36 offset:5120\n"
"v_mfma_f32_16x16x32_bf16 v[12:15], v[56:59], v[196:199], v[12:15]\n"
"ds_read_b64_tr_b16 v[76:77], v36 offset:6144\n"
"v_mfma_f32_16x16x32_bf16 v[12:15], v[60:63], v[212:215], v[12:15]\n"
"ds_read_b64_tr_b16 v[78:79], v36 offset:7168\n"
"v_mfma_f32_16x16x32_bf16 v[16:19], v[48:51], v[168:171], 0\n"
"ds_read_b64_tr_b16 v[80:81], v36 offset:8192\n"
"v_mfma_f32_16x16x32_bf16 v[16:19], v[52:55], v[184:187], v[16:19]\n"
"ds_read_b64_tr_b16 v[82:83], v36 offset:9216\n"
"v_mfma_f32_16x16x32_bf16 v[16:19], v[56:59], v[200:203], v[16:19]\n"
"ds_read_b64_tr_b16 v[84:85], v36 offset:10240\n"
"v_max3_f32 v8, v8, v9, v10\n"
"v_mfma_f32_16x16x32_bf16 v[16:19], v[60:63], v[216:219], v[16:19]\n"
"ds_read_b64_tr_b16 v[86:87], v36 offset:11264\n"
"v_max3_f32 v12, v12, v13, v14\n"
"v_mfma_f32_16x16x32_bf16 v[20:23], v[48:51], v[172:175], 0\n"
"v_max_f32 v8, v8, v11\n"
"v_max_f32 v12, v12, v15\n"
"v_mfma_f32_16x16x32_bf16 v[20:23], v[52:55], v[188:191], v[20:23]\n"
"v_mfma_f32_16x16x32_bf16 v[20:23], v[56:59], v[204:207], v[20:23]\n"
"v_mfma_f32_16x16x32_bf16 v[20:23], v[60:63], v[220:223], v[20:23]\n"
"v_mov_b32 v30, v8\n"
"v_mov_b32 v31, v12\n"
"s_nop 0\n"
"v_permlane32_swap_b32 v8, v30\n"
"v_permlane32_swap_b32 v12, v31\n"
"v_max_f32 v8, v8, v30\n"
"v_max_f32 v12, v12, v31\n"
"v_mov_b32 v30, v8\n"
"v_mov_b32 v31, v12\n"
"s_nop 0\n"
"v_permlane16_swap_b32 v8, v30\n"
"v_permlane16_swap_b32 v12, v31\n"
"v_min_f32 v9, -v8, -v30\n"
"v_min_f32 v13, -v12, -v31\n"
"v_min_f32 v10, -v8, -v30\n"
"v_min_f32 v14, -v12, -v31\n"
"v_min_f32 v11, -v8, -v30\n"
"v_min_f32 v15, -v12, -v31\n"
"v_min_f32 v8, -v8, -v30\n"
"v_min_f32 v12, -v12, -v31\n"
"v_max3_f32 v16, v16, v17, v18\n"
"v_max3_f32 v20, v20, v21, v22\n"
"v_max_f32 v16, v16, v19\n"
"v_max_f32 v20, v20, v23\n"
"v_mov_b32 v30, v16\n"
"v_mov_b32 v31, v20\n"
"s_nop 0\n"
"v_permlane32_swap_b32 v16, v30\n"
"v_permlane32_swap_b32 v20, v31\n"
"v_max_f32 v16, v16, v30\n"
"v_max_f32 v20, v20, v31\n"
"v_mov_b32 v30, v16\n"
"v_mov_b32 v31, v20\n"
"s_nop 0\n"
"v_permlane16_swap_b32 v16, v30\n"
"v_permlane16_swap_b32 v20, v31\n"
"v_min_f32 v17, -v16, -v30\n"
"v_min_f32 v21, -v20, -v31\n"
"v_min_f32 v18, -v16, -v30\n"
"v_min_f32 v22, -v20, -v31\n"
"v_min_f32 v19, -v16, -v30\n"
"v_min_f32 v23, -v20, -v31\n"
"v_min_f32 v16, -v16, -v30\n"
"v_min_f32 v20, -v20, -v31\n"
"s_waitcnt lgkmcnt(0)\n"
"L_gu:\n"
"v_mfma_f32_16x16x32_bf16 v[96:99], v[64:67], a[0:3], 0\n"
"ds_read_b64_tr_b16 v[88:89], v36 offset:12288\n"
"ds_read_b64_tr_b16 v[90:91], v36 offset:13312\n"
"v_mfma_f32_16x16x32_bf16 v[96:99], v[68:71], a[16:19], v[96:99]\n"
"ds_read_b64_tr_b16 v[92:93], v36 offset:14336\n"
"ds_read_b64_tr_b16 v[94:95], v36 offset:15360\n"
"v_mfma_f32_16x16x32_bf16 v[100:103], v[68:71], a[20:23], 0\n"
"s_mov_b32 m0, s60\n"
"v_mfma_f32_16x16x32_bf16 v[100:103], v[64:67], a[4:7], v[100:103]\n"
"s_mov_b32 s59, s86\n"
"v_mul_f32 v144, v144, v128\n"
"v_mul_f32 v145, v145, v129\n"
"v_mfma_f32_16x16x32_bf16 v[104:107], v[64:67], a[8:11], 0\n"
"buffer_load_dwordx4 v2, s[16:19], s59 offen lds\n"
"v_mul_f32 v146, v146, v130\n"
"v_mfma_f32_16x16x32_bf16 v[104:107], v[68:71], a[24:27], v[104:107]\n"
"s_add_u32 m0, m0, 1024\n"
"v_mul_f32 v147, v147, v131\n"
"v_cvt_pk_bf16_f32 v50, v144, v145\n"
"v_mfma_f32_16x16x32_bf16 v[108:111], v[68:71], a[28:31], 0\n"
"s_add_u32 s59, s59, 147456\n"
"v_cvt_pk_bf16_f32 v51, v146, v147\n"
"v_mfma_f32_16x16x32_bf16 v[108:111], v[64:67], a[12:15], v[108:111]\n"
"s_waitcnt lgkmcnt(8)\n"
"buffer_store_dwordx4 v[48:51], v0, s[36:39], s55 offen sc1\n"
"v_mul_f32 v148, v148, v132\n"
"v_mfma_f32_16x16x32_bf16 v[96:99], v[72:75], a[32:35], v[96:99]\n"
"s_add_u32 s55, s55, 1024\n"
"ds_read_b64_tr_b16 v[64:65], v36 offset:16384\n"
"ds_read_b64_tr_b16 v[66:67], v36 offset:17408\n"
"v_mfma_f32_16x16x32_bf16 v[96:99], v[76:79], a[48:51], v[96:99]\n"
"ds_read_b64_tr_b16 v[68:69], v36 offset:18432\n"
"ds_read_b64_tr_b16 v[70:71], v36 offset:19456\n"
"v_mfma_f32_16x16x32_bf16 v[100:103], v[76:79], a[52:55], v[100:103]\n"
"buffer_load_dwordx4 v2, s[16:19], s59 offen lds\n"
"v_mul_f32 v149, v149, v133\n"
"v_mfma_f32_16x16x32_bf16 v[100:103], v[72:75], a[36:39], v[100:103]\n"
"s_add_u32 m0, m0, 1024\n"
"v_mul_f32 v150, v150, v134\n"
"v_mul_f32 v151, v151, v135\n"
"v_mfma_f32_16x16x32_bf16 v[104:107], v[72:75], a[40:43], v[104:107]\n"
"s_add_u32 s59, s59, 147456\n"
"v_cvt_pk_bf16_f32 v54, v148, v149\n"
"v_cvt_pk_bf16_f32 v55, v150, v151\n"
"v_mfma_f32_16x16x32_bf16 v[104:107], v[76:79], a[56:59], v[104:107]\n"
"buffer_store_dwordx4 v[52:55], v0, s[36:39], s56 offen sc1\n"
"v_mfma_f32_16x16x32_bf16 v[108:111], v[76:79], a[60:63], v[108:111]\n"
"s_add_u32 s56, s56, 1024\n"
"v_exp_f32 v44, v44\n"
"v_mfma_f32_16x16x32_bf16 v[108:111], v[72:75], a[44:47], v[108:111]\n"
"s_waitcnt lgkmcnt(8)\n"
"v_exp_f32 v45, v45\n"
"v_mfma_f32_16x16x32_bf16 v[96:99], v[80:83], a[64:67], v[96:99]\n"
"ds_read_b64_tr_b16 v[72:73], v36 offset:20480\n"
"ds_read_b64_tr_b16 v[74:75], v36 offset:21504\n"
"v_mfma_f32_16x16x32_bf16 v[96:99], v[84:87], a[80:83], v[96:99]\n"
"ds_read_b64_tr_b16 v[76:77], v36 offset:22528\n"
"ds_read_b64_tr_b16 v[78:79], v36 offset:23552\n"
"v_mfma_f32_16x16x32_bf16 v[100:103], v[84:87], a[84:87], v[100:103]\n"
"buffer_load_dwordx4 v2, s[16:19], s59 offen lds\n"
"v_mfma_f32_16x16x32_bf16 v[100:103], v[80:83], a[68:71], v[100:103]\n"
"s_add_u32 m0, m0, 1024\n"
"v_exp_f32 v46, v46\n"
"v_mfma_f32_16x16x32_bf16 v[104:107], v[80:83], a[72:75], v[104:107]\n"
"s_add_u32 s59, s59, 147456\n"
"v_exp_f32 v47, v47\n"
"v_mfma_f32_16x16x32_bf16 v[104:107], v[84:87], a[88:91], v[104:107]\n"
"v_fma_f32 v44, v44, s33, s33\n"
"v_fma_f32 v45, v45, s33, s33\n"
"v_mfma_f32_16x16x32_bf16 v[108:111], v[84:87], a[92:95], v[108:111]\n"
"v_fma_f32 v46, v46, s33, s33\n"
"v_fma_f32 v47, v47, s33, s33\n"
"v_mfma_f32_16x16x32_bf16 v[108:111], v[80:83], a[76:79], v[108:111]\n"
"s_waitcnt lgkmcnt(8)\n"
"v_rcp_f32 v44, v44\n"
"v_mfma_f32_16x16x32_bf16 v[96:99], v[88:91], a[96:99], v[96:99]\n"
"ds_read_b64_tr_b16 v[80:81], v36 offset:24576\n"
"ds_read_b64_tr_b16 v[82:83], v36 offset:25600\n"
"v_mfma_f32_16x16x32_bf16 v[96:99], v[92:95], a[112:115], v[96:99]\n"
"ds_read_b64_tr_b16 v[84:85], v36 offset:26624\n"
"ds_read_b64_tr_b16 v[86:87], v36 offset:27648\n"
"v_mfma_f32_16x16x32_bf16 v[100:103], v[92:95], a[116:119], v[100:103]\n"
"buffer_load_dwordx4 v2, s[16:19], s59 offen lds\n"
"v_mfma_f32_16x16x32_bf16 v[100:103], v[88:91], a[100:103], v[100:103]\n"
"s_add_u32 m0, m0, 1024\n"
"v_rcp_f32 v45, v45\n"
"v_mfma_f32_16x16x32_bf16 v[104:107], v[88:91], a[104:107], v[104:107]\n"
"s_add_u32 s59, s59, 147456\n"
"v_rcp_f32 v46, v46\n"
"v_mfma_f32_16x16x32_bf16 v[104:107], v[92:95], a[120:123], v[104:107]\n"
"v_rcp_f32 v47, v47\n"
"v_mfma_f32_16x16x32_bf16 v[108:111], v[92:95], a[124:127], v[108:111]\n"
"v_mul_f32 v136, v136, v44\n"
"v_mul_f32 v137, v137, v45\n"
"v_mfma_f32_16x16x32_bf16 v[108:111], v[88:91], a[108:111], v[108:111]\n"
"s_waitcnt lgkmcnt(8)\n"
"v_mul_f32 v138, v138, v46\n"
"v_mul_f32 v139, v139, v47\n"
"v_mfma_f32_16x16x32_bf16 v[96:99], v[64:67], a[128:131], v[96:99]\n"
"ds_read_b64_tr_b16 v[88:89], v36 offset:28672\n"
"ds_read_b64_tr_b16 v[90:91], v36 offset:29696\n"
"v_mfma_f32_16x16x32_bf16 v[96:99], v[68:71], a[144:147], v[96:99]\n"
"ds_read_b64_tr_b16 v[92:93], v36 offset:30720\n"
"ds_read_b64_tr_b16 v[94:95], v36 offset:31744\n"
"v_mfma_f32_16x16x32_bf16 v[100:103], v[68:71], a[148:151], v[100:103]\n"
"buffer_load_dwordx4 v2, s[16:19], s59 offen lds\n"
"v_mul_f32 v152, v152, v136\n"
"v_mfma_f32_16x16x32_bf16 v[100:103], v[64:67], a[132:135], v[100:103]\n"
"s_add_u32 m0, m0, 1024\n"
"v_mul_f32 v153, v153, v137\n"
"v_mul_f32 v154, v154, v138\n"
"v_mfma_f32_16x16x32_bf16 v[104:107], v[64:67], a[136:139], v[104:107]\n"
"s_add_u32 s59, s59, 147456\n"
"v_mul_f32 v155, v155, v139\n"
"v_cvt_pk_bf16_f32 v58, v152, v153\n"
"v_mfma_f32_16x16x32_bf16 v[104:107], v[68:71], a[152:155], v[104:107]\n"
"v_cvt_pk_bf16_f32 v59, v154, v155\n"
"v_mfma_f32_16x16x32_bf16 v[108:111], v[68:71], a[156:159], v[108:111]\n"
"buffer_store_dwordx4 v[56:59], v0, s[36:39], s57 offen sc1\n"
"v_mul_f32 v44, v140, v140\n"
"v_mfma_f32_16x16x32_bf16 v[108:111], v[64:67], a[140:143], v[108:111]\n"
"s_waitcnt lgkmcnt(8)\n"
"v_mul_f32 v45, v141, v141\n"
"v_mul_f32 v46, v142, v142\n"
"v_mfma_f32_16x16x32_bf16 v[96:99], v[72:75], a[160:163], v[96:99]\n"
"s_add_u32 s57, s57, 1024\n"
"ds_read_b64_tr_b16 v[64:65], v36 offset:32768\n"
"ds_read_b64_tr_b16 v[66:67], v36 offset:33792\n"
"v_mfma_f32_16x16x32_bf16 v[96:99], v[76:79], a[176:179], v[96:99]\n"
"ds_read_b64_tr_b16 v[68:69], v36 offset:34816\n"
"ds_read_b64_tr_b16 v[70:71], v36 offset:35840\n"
"v_mfma_f32_16x16x32_bf16 v[100:103], v[76:79], a[180:183], v[100:103]\n"
"buffer_load_dwordx4 v2, s[16:19], s59 offen lds\n"
"v_add_u32 v36, s61, v36\n"
"v_mfma_f32_16x16x32_bf16 v[100:103], v[72:75], a[164:167], v[100:103]\n"
"s_add_u32 m0, m0, 1024\n"
"v_mul_f32 v47, v143, v143\n"
"v_fma_f32 v44, v44, s63, v3\n"
"v_mfma_f32_16x16x32_bf16 v[104:107], v[72:75], a[168:171], v[104:107]\n"
"s_add_u32 s59, s59, 147456\n"
"v_fma_f32 v45, v45, s63, v3\n"
"v_fma_f32 v46, v46, s63, v3\n"
"v_mfma_f32_16x16x32_bf16 v[104:107], v[76:79], a[184:187], v[104:107]\n"
"v_fma_f32 v47, v47, s63, v3\n"
"v_mul_f32 v44, v44, v140\n"
"v_mfma_f32_16x16x32_bf16 v[108:111], v[76:79], a[188:191], v[108:111]\n"
"v_mul_f32 v45, v45, v141\n"
"v_mul_f32 v46, v46, v142\n"
"v_mfma_f32_16x16x32_bf16 v[108:111], v[72:75], a[172:175], v[108:111]\n"
"s_waitcnt lgkmcnt(8)\n"
"v_mul_f32 v47, v47, v143\n"
"v_mfma_f32_16x16x32_bf16 v[96:99], v[80:83], a[192:195], v[96:99]\n"
"ds_read_b64_tr_b16 v[72:73], v37 offset:0\n"
"ds_read_b64_tr_b16 v[74:75], v37 offset:1024\n"
"v_mfma_f32_16x16x32_bf16 v[96:99], v[84:87], a[208:211], v[96:99]\n"
"ds_read_b64_tr_b16 v[76:77], v37 offset:2048\n"
"ds_read_b64_tr_b16 v[78:79], v37 offset:3072\n"
"v_mfma_f32_16x16x32_bf16 v[100:103], v[84:87], a[212:215], v[100:103]\n"
"buffer_load_dwordx4 v2, s[16:19], s59 offen lds\n"
"v_mfma_f32_16x16x32_bf16 v[100:103], v[80:83], a[196:199], v[100:103]\n"
"s_add_u32 m0, m0, 1024\n"
"v_exp_f32 v44, v44\n"
"v_mfma_f32_16x16x32_bf16 v[104:107], v[80:83], a[200:203], v[104:107]\n"
"s_add_u32 s59, s59, 147456\n"
"v_exp_f32 v45, v45\n"
"v_mfma_f32_16x16x32_bf16 v[104:107], v[84:87], a[216:219], v[104:107]\n"
"v_exp_f32 v46, v46\n"
"v_mfma_f32_16x16x32_bf16 v[108:111], v[84:87], a[220:223], v[108:111]\n"
"v_exp_f32 v47, v47\n"
"v_mfma_f32_16x16x32_bf16 v[108:111], v[80:83], a[204:207], v[108:111]\n"
"s_waitcnt lgkmcnt(8)\n"
"v_fma_f32 v44, v44, s33, s33\n"
"v_fma_f32 v45, v45, s33, s33\n"
"v_mfma_f32_16x16x32_bf16 v[96:99], v[88:91], a[224:227], v[96:99]\n"
"ds_read_b64_tr_b16 v[80:81], v37 offset:4096\n"
"ds_read_b64_tr_b16 v[82:83], v37 offset:5120\n"
"v_mfma_f32_16x16x32_bf16 v[96:99], v[92:95], a[240:243], v[96:99]\n"
"ds_read_b64_tr_b16 v[84:85], v37 offset:6144\n"
"ds_read_b64_tr_b16 v[86:87], v37 offset:7168\n"
"v_mfma_f32_16x16x32_bf16 v[100:103], v[92:95], a[244:247], v[100:103]\n"
"buffer_load_dwordx4 v2, s[16:19], s59 offen lds\n"
"v_fma_f32 v46, v46, s33, s33\n"
"v_mfma_f32_16x16x32_bf16 v[100:103], v[88:91], a[228:231], v[100:103]\n"
"s_add_u32 m0, m0, 1024\n"
"v_fma_f32 v47, v47, s33, s33\n"
"v_mfma_f32_16x16x32_bf16 v[104:107], v[88:91], a[232:235], v[104:107]\n"
"s_add_u32 s59, s59, 147456\n"
"v_rcp_f32 v44, v44\n"
"v_mfma_f32_16x16x32_bf16 v[104:107], v[92:95], a[248:251], v[104:107]\n"
"v_rcp_f32 v45, v45\n"
"v_mfma_f32_16x16x32_bf16 v[108:111], v[92:95], a[252:255], v[108:111]\n"
"v_rcp_f32 v46, v46\n"
"v_mfma_f32_16x16x32_bf16 v[108:111], v[88:91], a[236:239], v[108:111]\n"
"s_waitcnt lgkmcnt(8)\n"
"v_rcp_f32 v47, v47\n"
"v_mfma_f32_16x16x32_bf16 v[96:99], v[64:67], v[224:227], v[96:99]\n"
"ds_read_b64_tr_b16 v[88:89], v37 offset:8192\n"
"ds_read_b64_tr_b16 v[90:91], v37 offset:9216\n"
"v_mfma_f32_16x16x32_bf16 v[96:99], v[68:71], v[240:243], v[96:99]\n"
"ds_read_b64_tr_b16 v[92:93], v37 offset:10240\n"
"ds_read_b64_tr_b16 v[94:95], v37 offset:11264\n"
"v_mfma_f32_16x16x32_bf16 v[100:103], v[68:71], v[244:247], v[100:103]\n"
"buffer_load_dwordx4 v2, s[16:19], s59 offen lds\n"
"v_mul_f32 v140, v140, v44\n"
"v_mfma_f32_16x16x32_bf16 v[100:103], v[64:67], v[228:231], v[100:103]\n"
"s_add_u32 m0, m0, 28672\n"
"v_mul_f32 v141, v141, v45\n"
"v_mul_f32 v142, v142, v46\n"
"v_mfma_f32_16x16x32_bf16 v[104:107], v[64:67], v[232:235], v[104:107]\n"
"s_sub_u32 s59, s59, 1179648\n"
"v_mul_f32 v143, v143, v47\n"
"v_mul_f32 v156, v156, v140\n"
"v_mfma_f32_16x16x32_bf16 v[104:107], v[68:71], v[248:251], v[104:107]\n"
"v_mul_f32 v157, v157, v141\n"
"v_mul_f32 v158, v158, v142\n"
"v_mfma_f32_16x16x32_bf16 v[108:111], v[68:71], v[252:255], v[108:111]\n"
"v_mul_f32 v159, v159, v143\n"
"v_cvt_pk_bf16_f32 v62, v156, v157\n"
"v_mfma_f32_16x16x32_bf16 v[108:111], v[64:67], v[236:239], v[108:111]\n"
"s_waitcnt lgkmcnt(8)\n"
"v_cvt_pk_bf16_f32 v63, v158, v159\n"
"v_mfma_f32_16x16x32_bf16 v[112:115], v[72:75], a[0:3], 0\n"
"ds_read_b64_tr_b16 v[64:65], v37 offset:12288\n"
"ds_read_b64_tr_b16 v[66:67], v37 offset:13312\n"
"v_mfma_f32_16x16x32_bf16 v[112:115], v[76:79], a[16:19], v[112:115]\n"
"ds_read_b64_tr_b16 v[68:69], v37 offset:14336\n"
"ds_read_b64_tr_b16 v[70:71], v37 offset:15360\n"
"v_mfma_f32_16x16x32_bf16 v[116:119], v[76:79], a[20:23], 0\n"
"buffer_load_dwordx4 v2, s[20:23], s59 offen lds\n"
"buffer_store_dwordx4 v[60:63], v0, s[36:39], s58 offen sc1\n"
"v_mfma_f32_16x16x32_bf16 v[116:119], v[72:75], a[4:7], v[116:119]\n"
"s_add_u32 m0, m0, 1024\n"
"v_mul_f32 v44, v96, v96\n"
"v_mul_f32 v45, v97, v97\n"
"v_mfma_f32_16x16x32_bf16 v[120:123], v[72:75], a[8:11], 0\n"
"s_add_u32 s59, s59, 147456\n"
"v_mul_f32 v46, v98, v98\n"
"v_mul_f32 v47, v99, v99\n"
"v_mfma_f32_16x16x32_bf16 v[120:123], v[76:79], a[24:27], v[120:123]\n"
"s_add_u32 s58, s58, 1024\n"
"v_fma_f32 v44, v44, s63, v3\n"
"v_fma_f32 v45, v45, s63, v3\n"
"v_mfma_f32_16x16x32_bf16 v[124:127], v[76:79], a[28:31], 0\n"
"s_mov_b32 s38, s87\n"
"v_fma_f32 v46, v46, s63, v3\n"
"v_fma_f32 v47, v47, s63, v3\n"
"v_mfma_f32_16x16x32_bf16 v[124:127], v[72:75], a[12:15], v[124:127]\n"
"s_waitcnt lgkmcnt(8)\n"
"v_mul_f32 v44, v44, v96\n"
"v_mul_f32 v45, v45, v97\n"
"v_mfma_f32_16x16x32_bf16 v[112:115], v[80:83], a[32:35], v[112:115]\n"
"ds_read_b64_tr_b16 v[72:73], v37 offset:16384\n"
"ds_read_b64_tr_b16 v[74:75], v37 offset:17408\n"
"v_mfma_f32_16x16x32_bf16 v[112:115], v[84:87], a[48:51], v[112:115]\n"
"ds_read_b64_tr_b16 v[76:77], v37 offset:18432\n"
"ds_read_b64_tr_b16 v[78:79], v37 offset:19456\n"
"v_mfma_f32_16x16x32_bf16 v[116:119], v[84:87], a[52:55], v[116:119]\n"
"buffer_load_dwordx4 v2, s[20:23], s59 offen lds\n"
"v_mul_f32 v46, v46, v98\n"
"v_mfma_f32_16x16x32_bf16 v[116:119], v[80:83], a[36:39], v[116:119]\n"
"s_add_u32 m0, m0, 1024\n"
"v_mul_f32 v47, v47, v99\n"
"v_mfma_f32_16x16x32_bf16 v[120:123], v[80:83], a[40:43], v[120:123]\n"
"s_add_u32 s59, s59, 147456\n"
"v_exp_f32 v44, v44\n"
"v_mfma_f32_16x16x32_bf16 v[120:123], v[84:87], a[56:59], v[120:123]\n"
"v_exp_f32 v45, v45\n"
"v_mfma_f32_16x16x32_bf16 v[124:127], v[84:87], a[60:63], v[124:127]\n"
"v_exp_f32 v46, v46\n"
"v_mfma_f32_16x16x32_bf16 v[124:127], v[80:83], a[44:47], v[124:127]\n"
"s_waitcnt lgkmcnt(8)\n"
"v_exp_f32 v47, v47\n"
"v_mfma_f32_16x16x32_bf16 v[112:115], v[88:91], a[64:67], v[112:115]\n"
"ds_read_b64_tr_b16 v[80:81], v37 offset:20480\n"
"ds_read_b64_tr_b16 v[82:83], v37 offset:21504\n"
"v_mfma_f32_16x16x32_bf16 v[112:115], v[92:95], a[80:83], v[112:115]\n"
"ds_read_b64_tr_b16 v[84:85], v37 offset:22528\n"
"ds_read_b64_tr_b16 v[86:87], v37 offset:23552\n"
"v_mfma_f32_16x16x32_bf16 v[116:119], v[92:95], a[84:87], v[116:119]\n"
"buffer_load_dwordx4 v2, s[20:23], s59 offen lds\n"
"v_fma_f32 v44, v44, s33, s33\n"
"v_mfma_f32_16x16x32_bf16 v[116:119], v[88:91], a[68:71], v[116:119]\n"
"s_add_u32 m0, m0, 1024\n"
"v_fma_f32 v45, v45, s33, s33\n"
"v_fma_f32 v46, v46, s33, s33\n"
"v_mfma_f32_16x16x32_bf16 v[120:123], v[88:91], a[72:75], v[120:123]\n"
"s_add_u32 s59, s59, 147456\n"
"v_fma_f32 v47, v47, s33, s33\n"
"v_mfma_f32_16x16x32_bf16 v[120:123], v[92:95], a[88:91], v[120:123]\n"
"v_rcp_f32 v44, v44\n"
"v_mfma_f32_16x16x32_bf16 v[124:127], v[92:95], a[92:95], v[124:127]\n"
"v_rcp_f32 v45, v45\n"
"v_mfma_f32_16x16x32_bf16 v[124:127], v[88:91], a[76:79], v[124:127]\n"
"s_waitcnt lgkmcnt(8)\n"
"v_rcp_f32 v46, v46\n"
"v_mfma_f32_16x16x32_bf16 v[112:115], v[64:67], a[96:99], v[112:115]\n"
"ds_read_b64_tr_b16 v[88:89], v37 offset:24576\n"
"ds_read_b64_tr_b16 v[90:91], v37 offset:25600\n"
"v_mfma_f32_16x16x32_bf16 v[112:115], v[68:71], a[112:115], v[112:115]\n"
"ds_read_b64_tr_b16 v[92:93], v37 offset:26624\n"
"ds_read_b64_tr_b16 v[94:95], v37 offset:27648\n"
"v_mfma_f32_16x16x32_bf16 v[116:119], v[68:71], a[116:119], v[116:119]\n"
"buffer_load_dwordx4 v2, s[20:23], s59 offen lds\n"
"v_mfma_f32_16x16x32_bf16 v[116:119], v[64:67], a[100:103], v[116:119]\n"
"s_add_u32 m0, m0, 1024\n"
"v_rcp_f32 v47, v47\n"
"v_mfma_f32_16x16x32_bf16 v[120:123], v[64:67], a[104:107], v[120:123]\n"
"s_add_u32 s59, s59, 147456\n"
"v_mul_f32 v96, v96, v44\n"
"v_mul_f32 v97, v97, v45\n"
"v_mfma_f32_16x16x32_bf16 v[120:123], v[68:71], a[120:123], v[120:123]\n"
"v_mul_f32 v98, v98, v46\n"
"v_mul_f32 v99, v99, v47\n"
"v_mfma_f32_16x16x32_bf16 v[124:127], v[68:71], a[124:127], v[124:127]\n"
"v_mul_f32 v44, v100, v100\n"
"v_mul_f32 v45, v101, v101\n"
"v_mfma_f32_16x16x32_bf16 v[124:127], v[64:67], a[108:111], v[124:127]\n"
"s_waitcnt lgkmcnt(8)\n"
"v_mul_f32 v46, v102, v102\n"
"v_mul_f32 v47, v103, v103\n"
"v_mfma_f32_16x16x32_bf16 v[112:115], v[72:75], a[128:131], v[112:115]\n"
"ds_read_b64_tr_b16 v[64:65], v37 offset:28672\n"
"ds_read_b64_tr_b16 v[66:67], v37 offset:29696\n"
"v_mfma_f32_16x16x32_bf16 v[112:115], v[76:79], a[144:147], v[112:115]\n"
"ds_read_b64_tr_b16 v[68:69], v37 offset:30720\n"
"ds_read_b64_tr_b16 v[70:71], v37 offset:31744\n"
"v_mfma_f32_16x16x32_bf16 v[116:119], v[76:79], a[148:151], v[116:119]\n"
"buffer_load_dwordx4 v2, s[20:23], s59 offen lds\n"
"v_fma_f32 v44, v44, s63, v3\n"
"v_mfma_f32_16x16x32_bf16 v[116:119], v[72:75], a[132:135], v[116:119]\n"
"s_add_u32 m0, m0, 1024\n"
"v_fma_f32 v45, v45, s63, v3\n"
"v_fma_f32 v46, v46, s63, v3\n"
"v_mfma_f32_16x16x32_bf16 v[120:123], v[72:75], a[136:139], v[120:123]\n"
"s_add_u32 s59, s59, 147456\n"
"v_fma_f32 v47, v47, s63, v3\n"
"v_mul_f32 v44, v44, v100\n"
"v_mfma_f32_16x16x32_bf16 v[120:123], v[76:79], a[152:155], v[120:123]\n"
"v_mul_f32 v45, v45, v101\n"
"v_mul_f32 v46, v46, v102\n"
"v_mfma_f32_16x16x32_bf16 v[124:127], v[76:79], a[156:159], v[124:127]\n"
"v_mul_f32 v47, v47, v103\n"
"v_mfma_f32_16x16x32_bf16 v[124:127], v[72:75], a[140:143], v[124:127]\n"
"s_waitcnt lgkmcnt(8)\n"
"v_exp_f32 v44, v44\n"
"v_mfma_f32_16x16x32_bf16 v[112:115], v[80:83], a[160:163], v[112:115]\n"
"ds_read_b64_tr_b16 v[72:73], v37 offset:32768\n"
"ds_read_b64_tr_b16 v[74:75], v37 offset:33792\n"
"v_mfma_f32_16x16x32_bf16 v[112:115], v[84:87], a[176:179], v[112:115]\n"
"ds_read_b64_tr_b16 v[76:77], v37 offset:34816\n"
"ds_read_b64_tr_b16 v[78:79], v37 offset:35840\n"
"v_mfma_f32_16x16x32_bf16 v[116:119], v[84:87], a[180:183], v[116:119]\n"
"buffer_load_dwordx4 v2, s[20:23], s59 offen lds\n"
"v_add_u32 v37, s61, v37\n"
"v_mfma_f32_16x16x32_bf16 v[116:119], v[80:83], a[164:167], v[116:119]\n"
"s_add_u32 m0, m0, 1024\n"
"v_exp_f32 v45, v45\n"
"v_mfma_f32_16x16x32_bf16 v[120:123], v[80:83], a[168:171], v[120:123]\n"
"s_add_u32 s59, s59, 147456\n"
"v_exp_f32 v46, v46\n"
"v_mfma_f32_16x16x32_bf16 v[120:123], v[84:87], a[184:187], v[120:123]\n"
"v_exp_f32 v47, v47\n"
"v_mfma_f32_16x16x32_bf16 v[124:127], v[84:87], a[188:191], v[124:127]\n"
"v_fma_f32 v44, v44, s33, s33\n"
"v_fma_f32 v45, v45, s33, s33\n"
"v_mfma_f32_16x16x32_bf16 v[124:127], v[80:83], a[172:175], v[124:127]\n"
"s_waitcnt lgkmcnt(8)\n"
"v_fma_f32 v46, v46, s33, s33\n"
"v_fma_f32 v47, v47, s33, s33\n"
"v_mfma_f32_16x16x32_bf16 v[112:115], v[88:91], a[192:195], v[112:115]\n"
"ds_read_b64_tr_b16 v[80:81], v38 offset:0\n"
"ds_read_b64_tr_b16 v[82:83], v38 offset:1024\n"
"v_mfma_f32_16x16x32_bf16 v[112:115], v[92:95], a[208:211], v[112:115]\n"
"ds_read_b64_tr_b16 v[84:85], v38 offset:2048\n"
"ds_read_b64_tr_b16 v[86:87], v38 offset:3072\n"
"v_mfma_f32_16x16x32_bf16 v[116:119], v[92:95], a[212:215], v[116:119]\n"
"buffer_load_dwordx4 v2, s[20:23], s59 offen lds\n"
"v_mfma_f32_16x16x32_bf16 v[116:119], v[88:91], a[196:199], v[116:119]\n"
"s_add_u32 m0, m0, 1024\n"
"v_rcp_f32 v44, v44\n"
"v_mfma_f32_16x16x32_bf16 v[120:123], v[88:91], a[200:203], v[120:123]\n"
"s_add_u32 s59, s59, 147456\n"
"v_rcp_f32 v45, v45\n"
"v_mfma_f32_16x16x32_bf16 v[120:123], v[92:95], a[216:219], v[120:123]\n"
"v_rcp_f32 v46, v46\n"
"v_mfma_f32_16x16x32_bf16 v[124:127], v[92:95], a[220:223], v[124:127]\n"
"v_rcp_f32 v47, v47\n"
"v_mfma_f32_16x16x32_bf16 v[124:127], v[88:91], a[204:207], v[124:127]\n"
"s_waitcnt lgkmcnt(8)\n"
"v_mul_f32 v100, v100, v44\n"
"v_mul_f32 v101, v101, v45\n"
"v_mfma_f32_16x16x32_bf16 v[112:115], v[64:67], a[224:227], v[112:115]\n"
"ds_read_b64_tr_b16 v[88:89], v38 offset:4096\n"
"ds_read_b64_tr_b16 v[90:91], v38 offset:5120\n"
"v_mfma_f32_16x16x32_bf16 v[112:115], v[68:71], a[240:243], v[112:115]\n"
"ds_read_b64_tr_b16 v[92:93], v38 offset:6144\n"
"ds_read_b64_tr_b16 v[94:95], v38 offset:7168\n"
"v_mfma_f32_16x16x32_bf16 v[116:119], v[68:71], a[244:247], v[116:119]\n"
"buffer_load_dwordx4 v2, s[20:23], s59 offen lds\n"
"v_mul_f32 v102, v102, v46\n"
"v_mfma_f32_16x16x32_bf16 v[116:119], v[64:67], a[228:231], v[116:119]\n"
"s_add_u32 m0, m0, 1024\n"
"v_mul_f32 v103, v103, v47\n"
"v_mul_f32 v44, v104, v104\n"
"v_mfma_f32_16x16x32_bf16 v[120:123], v[64:67], a[232:235], v[120:123]\n"
"s_add_u32 s59, s59, 147456\n"
"v_mul_f32 v45, v105, v105\n"
"v_mul_f32 v46, v106, v106\n"
"v_mfma_f32_16x16x32_bf16 v[120:123], v[68:71], a[248:251], v[120:123]\n"
"v_mul_f32 v47, v107, v107\n"
"v_fma_f32 v44, v44, s63, v3\n"
"v_mfma_f32_16x16x32_bf16 v[124:127], v[68:71], a[252:255], v[124:127]\n"
"v_fma_f32 v45, v45, s63, v3\n"
"v_fma_f32 v46, v46, s63, v3\n"
"v_mfma_f32_16x16x32_bf16 v[124:127], v[64:67], a[236:239], v[124:127]\n"
"s_waitcnt lgkmcnt(8)\n"
"v_fma_f32 v47, v47, s63, v3\n"
"v_mul_f32 v44, v44, v104\n"
"v_mfma_f32_16x16x32_bf16 v[112:115], v[72:75], v[224:227], v[112:115]\n"
"ds_read_b64_tr_b16 v[64:65], v38 offset:8192\n"
"ds_read_b64_tr_b16 v[66:67], v38 offset:9216\n"
"v_mfma_f32_16x16x32_bf16 v[112:115], v[76:79], v[240:243], v[112:115]\n"
"ds_read_b64_tr_b16 v[68:69], v38 offset:10240\n"
"ds_read_b64_tr_b16 v[70:71], v38 offset:11264\n"
"v_mfma_f32_16x16x32_bf16 v[116:119], v[76:79], v[244:247], v[116:119]\n"
"buffer_load_dwordx4 v2, s[20:23], s59 offen lds\n"
"v_mul_f32 v45, v45, v105\n"
"v_mfma_f32_16x16x32_bf16 v[116:119], v[72:75], v[228:231], v[116:119]\n"
"v_mul_f32 v46, v46, v106\n"
"v_mul_f32 v47, v47, v107\n"
"v_mfma_f32_16x16x32_bf16 v[120:123], v[72:75], v[232:235], v[120:123]\n"
"v_mfma_f32_16x16x32_bf16 v[120:123], v[76:79], v[248:251], v[120:123]\n"
"v_mfma_f32_16x16x32_bf16 v[124:127], v[76:79], v[252:255], v[124:127]\n"
"v_mfma_f32_16x16x32_bf16 v[124:127], v[72:75], v[236:239], v[124:127]\n"
"s_waitcnt lgkmcnt(8)\n"
"v_mfma_f32_16x16x32_bf16 v[128:131], v[80:83], a[0:3], 0\n"
"ds_read_b64_tr_b16 v[72:73], v38 offset:12288\n"
"ds_read_b64_tr_b16 v[74:75], v38 offset:13312\n"
"v_mfma_f32_16x16x32_bf16 v[128:131], v[84:87], a[16:19], v[128:131]\n"
"ds_read_b64_tr_b16 v[76:77], v38 offset:14336\n"
"ds_read_b64_tr_b16 v[78:79], v38 offset:15360\n"
"v_mfma_f32_16x16x32_bf16 v[132:135], v[84:87], a[20:23], 0\n"
"v_mfma_f32_16x16x32_bf16 v[132:135], v[80:83], a[4:7], v[132:135]\n"
"v_mul_f32 v112, v112, v96\n"
"v_mul_f32 v113, v113, v97\n"
"v_mfma_f32_16x16x32_bf16 v[136:139], v[80:83], a[8:11], 0\n"
"v_mul_f32 v114, v114, v98\n"
"v_mul_f32 v115, v115, v99\n"
"v_mfma_f32_16x16x32_bf16 v[136:139], v[84:87], a[24:27], v[136:139]\n"
"v_cvt_pk_bf16_f32 v48, v112, v113\n"
"v_cvt_pk_bf16_f32 v49, v114, v115\n"
"v_mfma_f32_16x16x32_bf16 v[140:143], v[84:87], a[28:31], 0\n"
"v_mul_f32 v116, v116, v100\n"
"v_mul_f32 v117, v117, v101\n"
"v_mfma_f32_16x16x32_bf16 v[140:143], v[80:83], a[12:15], v[140:143]\n"
"s_waitcnt lgkmcnt(8)\n"
"v_mul_f32 v118, v118, v102\n"
"v_mul_f32 v119, v119, v103\n"
"v_mfma_f32_16x16x32_bf16 v[128:131], v[88:91], a[32:35], v[128:131]\n"
"ds_read_b64_tr_b16 v[80:81], v38 offset:16384\n"
"ds_read_b64_tr_b16 v[82:83], v38 offset:17408\n"
"v_mfma_f32_16x16x32_bf16 v[128:131], v[92:95], a[48:51], v[128:131]\n"
"ds_read_b64_tr_b16 v[84:85], v38 offset:18432\n"
"ds_read_b64_tr_b16 v[86:87], v38 offset:19456\n"
"v_mfma_f32_16x16x32_bf16 v[132:135], v[92:95], a[52:55], v[132:135]\n"
"v_cvt_pk_bf16_f32 v52, v116, v117\n"
"v_cvt_pk_bf16_f32 v53, v118, v119\n"
"v_mfma_f32_16x16x32_bf16 v[132:135], v[88:91], a[36:39], v[132:135]\n"
"v_exp_f32 v44, v44\n"
"v_mfma_f32_16x16x32_bf16 v[136:139], v[88:91], a[40:43], v[136:139]\n"
"v_exp_f32 v45, v45\n"
"v_mfma_f32_16x16x32_bf16 v[136:139], v[92:95], a[56:59], v[136:139]\n"
"v_exp_f32 v46, v46\n"
"v_mfma_f32_16x16x32_bf16 v[140:143], v[92:95], a[60:63], v[140:143]\n"
"v_exp_f32 v47, v47\n"
"v_mfma_f32_16x16x32_bf16 v[140:143], v[88:91], a[44:47], v[140:143]\n"
"s_waitcnt lgkmcnt(8)\n"
"v_fma_f32 v44, v44, s33, s33\n"
"v_fma_f32 v45, v45, s33, s33\n"
"v_mfma_f32_16x16x32_bf16 v[128:131], v[64:67], a[64:67], v[128:131]\n"
"ds_read_b64_tr_b16 v[88:89], v38 offset:20480\n"
"ds_read_b64_tr_b16 v[90:91], v38 offset:21504\n"
"v_mfma_f32_16x16x32_bf16 v[128:131], v[68:71], a[80:83], v[128:131]\n"
"ds_read_b64_tr_b16 v[92:93], v38 offset:22528\n"
"ds_read_b64_tr_b16 v[94:95], v38 offset:23552\n"
"v_mfma_f32_16x16x32_bf16 v[132:135], v[68:71], a[84:87], v[132:135]\n"
"global_load_dword v42, v43, s[4:5]\n"
"v_fma_f32 v46, v46, s33, s33\n"
"v_mfma_f32_16x16x32_bf16 v[132:135], v[64:67], a[68:71], v[132:135]\n"
"s_add_u32 s4, s4, 131072\n"
"s_addc_u32 s5, s5, 0\n"
"v_fma_f32 v47, v47, s33, s33\n"
"v_mfma_f32_16x16x32_bf16 v[136:139], v[64:67], a[72:75], v[136:139]\n"
"v_rcp_f32 v44, v44\n"
"v_mfma_f32_16x16x32_bf16 v[136:139], v[68:71], a[88:91], v[136:139]\n"
"v_rcp_f32 v45, v45\n"
"v_mfma_f32_16x16x32_bf16 v[140:143], v[68:71], a[92:95], v[140:143]\n"
"v_rcp_f32 v46, v46\n"
"v_mfma_f32_16x16x32_bf16 v[140:143], v[64:67], a[76:79], v[140:143]\n"
"s_waitcnt lgkmcnt(8)\n"
"v_rcp_f32 v47, v47\n"
"v_mfma_f32_16x16x32_bf16 v[128:131], v[72:75], a[96:99], v[128:131]\n"
"ds_read_b64_tr_b16 v[64:65], v38 offset:24576\n"
"ds_read_b64_tr_b16 v[66:67], v38 offset:25600\n"
"v_mfma_f32_16x16x32_bf16 v[128:131], v[76:79], a[112:115], v[128:131]\n"
"ds_read_b64_tr_b16 v[68:69], v38 offset:26624\n"
"ds_read_b64_tr_b16 v[70:71], v38 offset:27648\n"
"v_mfma_f32_16x16x32_bf16 v[132:135], v[76:79], a[116:119], v[132:135]\n"
"v_mul_f32 v104, v104, v44\n"
"v_mul_f32 v105, v105, v45\n"
"v_mfma_f32_16x16x32_bf16 v[132:135], v[72:75], a[100:103], v[132:135]\n"
"v_mul_f32 v106, v106, v46\n"
"v_mul_f32 v107, v107, v47\n"
"v_mfma_f32_16x16x32_bf16 v[136:139], v[72:75], a[104:107], v[136:139]\n"
"v_mul_f32 v120, v120, v104\n"
"v_mul_f32 v121, v121, v105\n"
"v_mfma_f32_16x16x32_bf16 v[136:139], v[76:79], a[120:123], v[136:139]\n"
"v_mul_f32 v122, v122, v106\n"
"v_mul_f32 v123, v123, v107\n"
"v_mfma_f32_16x16x32_bf16 v[140:143], v[76:79], a[124:127], v[140:143]\n"
"v_cvt_pk_bf16_f32 v56, v120, v121\n"
"v_cvt_pk_bf16_f32 v57, v122, v123\n"
"v_mfma_f32_16x16x32_bf16 v[140:143], v[72:75], a[108:111], v[140:143]\n"
"s_waitcnt lgkmcnt(8)\n"
"v_mul_f32 v44, v108, v108\n"
"v_mul_f32 v45, v109, v109\n"
"v_mfma_f32_16x16x32_bf16 v[128:131], v[80:83], a[128:131], v[128:131]\n"
"ds_read_b64_tr_b16 v[72:73], v38 offset:28672\n"
"ds_read_b64_tr_b16 v[74:75], v38 offset:29696\n"
"v_mfma_f32_16x16x32_bf16 v[128:131], v[84:87], a[144:147], v[128:131]\n"
"ds_read_b64_tr_b16 v[76:77], v38 offset:30720\n"
"ds_read_b64_tr_b16 v[78:79], v38 offset:31744\n"
"v_mfma_f32_16x16x32_bf16 v[132:135], v[84:87], a[148:151], v[132:135]\n"
"v_mul_f32 v46, v110, v110\n"
"v_mul_f32 v47, v111, v111\n"
"v_mfma_f32_16x16x32_bf16 v[132:135], v[80:83], a[132:135], v[132:135]\n"
"v_fma_f32 v44, v44, s63, v3\n"
"v_fma_f32 v45, v45, s63, v3\n"
"v_mfma_f32_16x16x32_bf16 v[136:139], v[80:83], a[136:139], v[136:139]\n"
"v_fma_f32 v46, v46, s63, v3\n"
"v_fma_f32 v47, v47, s63, v3\n"
"v_mfma_f32_16x16x32_bf16 v[136:139], v[84:87], a[152:155], v[136:139]\n"
"v_mul_f32 v44, v44, v108\n"
"v_mul_f32 v45, v45, v109\n"
"v_mfma_f32_16x16x32_bf16 v[140:143], v[84:87], a[156:159], v[140:143]\n"
"v_mul_f32 v46, v46, v110\n"
"v_mul_f32 v47, v47, v111\n"
"v_mfma_f32_16x16x32_bf16 v[140:143], v[80:83], a[140:143], v[140:143]\n"
"s_waitcnt lgkmcnt(8)\n"
"v_exp_f32 v44, v44\n"
"v_mfma_f32_16x16x32_bf16 v[128:131], v[88:91], a[160:163], v[128:131]\n"
"ds_read_b64_tr_b16 v[80:81], v38 offset:32768\n"
"ds_read_b64_tr_b16 v[82:83], v38 offset:33792\n"
"v_mfma_f32_16x16x32_bf16 v[128:131], v[92:95], a[176:179], v[128:131]\n"
"ds_read_b64_tr_b16 v[84:85], v38 offset:34816\n"
"ds_read_b64_tr_b16 v[86:87], v38 offset:35840\n"
"v_mfma_f32_16x16x32_bf16 v[132:135], v[92:95], a[180:183], v[132:135]\n"
"v_add_u32 v38, s61, v38\n"
"v_mfma_f32_16x16x32_bf16 v[132:135], v[88:91], a[164:167], v[132:135]\n"
"v_exp_f32 v45, v45\n"
"v_mfma_f32_16x16x32_bf16 v[136:139], v[88:91], a[168:171], v[136:139]\n"
"v_exp_f32 v46, v46\n"
"v_mfma_f32_16x16x32_bf16 v[136:139], v[92:95], a[184:187], v[136:139]\n"
"v_exp_f32 v47, v47\n"
"v_mfma_f32_16x16x32_bf16 v[140:143], v[92:95], a[188:191], v[140:143]\n"
"v_fma_f32 v44, v44, s33, s33\n"
"v_fma_f32 v45, v45, s33, s33\n"
"v_mfma_f32_16x16x32_bf16 v[140:143], v[88:91], a[172:175], v[140:143]\n"
"s_waitcnt lgkmcnt(8)\n"
"v_fma_f32 v46, v46, s33, s33\n"
"v_fma_f32 v47, v47, s33, s33\n"
"v_mfma_f32_16x16x32_bf16 v[128:131], v[64:67], a[192:195], v[128:131]\n"
"ds_read_b64_tr_b16 v[88:89], v39 offset:0\n"
"ds_read_b64_tr_b16 v[90:91], v39 offset:1024\n"
"v_mfma_f32_16x16x32_bf16 v[128:131], v[68:71], a[208:211], v[128:131]\n"
"ds_read_b64_tr_b16 v[92:93], v39 offset:2048\n"
"ds_read_b64_tr_b16 v[94:95], v39 offset:3072\n"
"v_mfma_f32_16x16x32_bf16 v[132:135], v[68:71], a[212:215], v[132:135]\n"
"v_rcp_f32 v44, v44\n"
"v_mfma_f32_16x16x32_bf16 v[132:135], v[64:67], a[196:199], v[132:135]\n"
"v_rcp_f32 v45, v45\n"
"v_mfma_f32_16x16x32_bf16 v[136:139], v[64:67], a[200:203], v[136:139]\n"
"v_rcp_f32 v46, v46\n"
"v_mfma_f32_16x16x32_bf16 v[136:139], v[68:71], a[216:219], v[136:139]\n"
"v_rcp_f32 v47, v47\n"
"v_mfma_f32_16x16x32_bf16 v[140:143], v[68:71], a[220:223], v[140:143]\n"
"v_mul_f32 v108, v108, v44\n"
"v_mul_f32 v109, v109, v45\n"
"v_mfma_f32_16x16x32_bf16 v[140:143], v[64:67], a[204:207], v[140:143]\n"
"s_waitcnt lgkmcnt(8)\n"
"v_mul_f32 v110, v110, v46\n"
"v_mul_f32 v111, v111, v47\n"
"v_mfma_f32_16x16x32_bf16 v[128:131], v[72:75], a[224:227], v[128:131]\n"
"ds_read_b64_tr_b16 v[64:65], v39 offset:4096\n"
"ds_read_b64_tr_b16 v[66:67], v39 offset:5120\n"
"v_mfma_f32_16x16x32_bf16 v[128:131], v[76:79], a[240:243], v[128:131]\n"
"ds_read_b64_tr_b16 v[68:69], v39 offset:6144\n"
"ds_read_b64_tr_b16 v[70:71], v39 offset:7168\n"
"v_mfma_f32_16x16x32_bf16 v[132:135], v[76:79], a[244:247], v[132:135]\n"
"v_mul_f32 v124, v124, v108\n"
"v_mul_f32 v125, v125, v109\n"
"v_mfma_f32_16x16x32_bf16 v[132:135], v[72:75], a[228:231], v[132:135]\n"
"v_mul_f32 v126, v126, v110\n"
"v_mul_f32 v127, v127, v111\n"
"v_mfma_f32_16x16x32_bf16 v[136:139], v[72:75], a[232:235], v[136:139]\n"
"v_cvt_pk_bf16_f32 v60, v124, v125\n"
"v_cvt_pk_bf16_f32 v61, v126, v127\n"
"v_mfma_f32_16x16x32_bf16 v[136:139], v[76:79], a[248:251], v[136:139]\n"
"v_mfma_f32_16x16x32_bf16 v[140:143], v[76:79], a[252:255], v[140:143]\n"
"v_mfma_f32_16x16x32_bf16 v[140:143], v[72:75], a[236:239], v[140:143]\n"
"s_waitcnt lgkmcnt(8)\n"
"v_mfma_f32_16x16x32_bf16 v[128:131], v[80:83], v[224:227], v[128:131]\n"
"ds_read_b64_tr_b16 v[72:73], v39 offset:8192\n"
"ds_read_b64_tr_b16 v[74:75], v39 offset:9216\n"
"v_mfma_f32_16x16x32_bf16 v[128:131], v[84:87], v[240:243], v[128:131]\n"
"ds_read_b64_tr_b16 v[76:77], v39 offset:10240\n"
"ds_read_b64_tr_b16 v[78:79], v39 offset:11264\n"
"v_mfma_f32_16x16x32_bf16 v[132:135], v[84:87], v[244:247], v[132:135]\n"
"v_mfma_f32_16x16x32_bf16 v[132:135], v[80:83], v[228:231], v[132:135]\n"
"v_mfma_f32_16x16x32_bf16 v[136:139], v[80:83], v[232:235], v[136:139]\n"
"v_mfma_f32_16x16x32_bf16 v[136:139], v[84:87], v[248:251], v[136:139]\n"
"v_mfma_f32_16x16x32_bf16 v[140:143], v[84:87], v[252:255], v[140:143]\n"
"v_mfma_f32_16x16x32_bf16 v[140:143], v[80:83], v[236:239], v[140:143]\n"
"s_waitcnt lgkmcnt(8)\n"
"v_mfma_f32_16x16x32_bf16 v[144:147], v[88:91], a[0:3], 0\n"
"ds_read_b64_tr_b16 v[80:81], v39 offset:12288\n"
"ds_read_b64_tr_b16 v[82:83], v39 offset:13312\n"
"v_mfma_f32_16x16x32_bf16 v[144:147], v[92:95], a[16:19], v[144:147]\n"
"ds_read_b64_tr_b16 v[84:85], v39 offset:14336\n"
"ds_read_b64_tr_b16 v[86:87], v39 offset:15360\n"
"v_mfma_f32_16x16x32_bf16 v[148:151], v[92:95], a[20:23], 0\n"
"v_mfma_f32_16x16x32_bf16 v[148:151], v[88:91], a[4:7], v[148:151]\n"
"v_mul_f32 v44, v128, v128\n"
"v_mul_f32 v45, v129, v129\n"
"v_mfma_f32_16x16x32_bf16 v[152:155], v[88:91], a[8:11], 0\n"
"v_mul_f32 v46, v130, v130\n"
"v_mul_f32 v47, v131, v131\n"
"v_mfma_f32_16x16x32_bf16 v[152:155], v[92:95], a[24:27], v[152:155]\n"
"v_fma_f32 v44, v44, s63, v3\n"
"v_fma_f32 v45, v45, s63, v3\n"
"v_mfma_f32_16x16x32_bf16 v[156:159], v[92:95], a[28:31], 0\n"
"v_fma_f32 v46, v46, s63, v3\n"
"v_fma_f32 v47, v47, s63, v3\n"
"v_mfma_f32_16x16x32_bf16 v[156:159], v[88:91], a[12:15], v[156:159]\n"
"s_waitcnt lgkmcnt(8)\n"
"v_mul_f32 v44, v44, v128\n"
"v_mul_f32 v45, v45, v129\n"
"v_mfma_f32_16x16x32_bf16 v[144:147], v[64:67], a[32:35], v[144:147]\n"
"ds_read_b64_tr_b16 v[88:89], v39 offset:16384\n"
"ds_read_b64_tr_b16 v[90:91], v39 offset:17408\n"
"v_mfma_f32_16x16x32_bf16 v[144:147], v[68:71], a[48:51], v[144:147]\n"
"ds_read_b64_tr_b16 v[92:93], v39 offset:18432\n"
"ds_read_b64_tr_b16 v[94:95], v39 offset:19456\n"
"v_mfma_f32_16x16x32_bf16 v[148:151], v[68:71], a[52:55], v[148:151]\n"
"v_mul_f32 v46, v46, v130\n"
"v_mul_f32 v47, v47, v131\n"
"v_mfma_f32_16x16x32_bf16 v[148:151], v[64:67], a[36:39], v[148:151]\n"
"v_exp_f32 v44, v44\n"
"v_mfma_f32_16x16x32_bf16 v[152:155], v[64:67], a[40:43], v[152:155]\n"
"v_exp_f32 v45, v45\n"
"v_mfma_f32_16x16x32_bf16 v[152:155], v[68:71], a[56:59], v[152:155]\n"
"v_exp_f32 v46, v46\n"
"v_mfma_f32_16x16x32_bf16 v[156:159], v[68:71], a[60:63], v[156:159]\n"
"v_exp_f32 v47, v47\n"
"v_mfma_f32_16x16x32_bf16 v[156:159], v[64:67], a[44:47], v[156:159]\n"
"s_waitcnt lgkmcnt(8)\n"
"v_fma_f32 v44, v44, s33, s33\n"
"v_fma_f32 v45, v45, s33, s33\n"
"v_mfma_f32_16x16x32_bf16 v[144:147], v[72:75], a[64:67], v[144:147]\n"
"ds_read_b64_tr_b16 v[64:65], v39 offset:20480\n"
"ds_read_b64_tr_b16 v[66:67], v39 offset:21504\n"
"v_mfma_f32_16x16x32_bf16 v[144:147], v[76:79], a[80:83], v[144:147]\n"
"ds_read_b64_tr_b16 v[68:69], v39 offset:22528\n"
"ds_read_b64_tr_b16 v[70:71], v39 offset:23552\n"
"v_mfma_f32_16x16x32_bf16 v[148:151], v[76:79], a[84:87], v[148:151]\n"
"v_fma_f32 v46, v46, s33, s33\n"
"v_fma_f32 v47, v47, s33, s33\n"
"v_mfma_f32_16x16x32_bf16 v[148:151], v[72:75], a[68:71], v[148:151]\n"
"v_rcp_f32 v44, v44\n"
"v_mfma_f32_16x16x32_bf16 v[152:155], v[72:75], a[72:75], v[152:155]\n"
"v_rcp_f32 v45, v45\n"
"v_mfma_f32_16x16x32_bf16 v[152:155], v[76:79], a[88:91], v[152:155]\n"
"v_rcp_f32 v46, v46\n"
"v_mfma_f32_16x16x32_bf16 v[156:159], v[76:79], a[92:95], v[156:159]\n"
"v_rcp_f32 v47, v47\n"
"v_mfma_f32_16x16x32_bf16 v[156:159], v[72:75], a[76:79], v[156:159]\n"
"s_waitcnt lgkmcnt(8)\n"
"v_mul_f32 v128, v128, v44\n"
"v_mul_f32 v129, v129, v45\n"
"v_mfma_f32_16x16x32_bf16 v[144:147], v[80:83], a[96:99], v[144:147]\n"
"ds_read_b64_tr_b16 v[72:73], v39 offset:24576\n"
"ds_read_b64_tr_b16 v[74:75], v39 offset:25600\n"
"v_mfma_f32_16x16x32_bf16 v[144:147], v[84:87], a[112:115], v[144:147]\n"
"ds_read_b64_tr_b16 v[76:77], v39 offset:26624\n"
"ds_read_b64_tr_b16 v[78:79], v39 offset:27648\n"
"v_mfma_f32_16x16x32_bf16 v[148:151], v[84:87], a[116:119], v[148:151]\n"
"v_mul_f32 v130, v130, v46\n"
"v_mul_f32 v131, v131, v47\n"
"v_mfma_f32_16x16x32_bf16 v[148:151], v[80:83], a[100:103], v[148:151]\n"
"v_mul_f32 v44, v132, v132\n"
"v_mul_f32 v45, v133, v133\n"
"v_mfma_f32_16x16x32_bf16 v[152:155], v[80:83], a[104:107], v[152:155]\n"
"v_mul_f32 v46, v134, v134\n"
"v_mul_f32 v47, v135, v135\n"
"v_mfma_f32_16x16x32_bf16 v[152:155], v[84:87], a[120:123], v[152:155]\n"
"v_fma_f32 v44, v44, s63, v3\n"
"v_fma_f32 v45, v45, s63, v3\n"
"v_mfma_f32_16x16x32_bf16 v[156:159], v[84:87], a[124:127], v[156:159]\n"
"v_fma_f32 v46, v46, s63, v3\n"
"v_fma_f32 v47, v47, s63, v3\n"
"v_mfma_f32_16x16x32_bf16 v[156:159], v[80:83], a[108:111], v[156:159]\n"
"s_waitcnt lgkmcnt(8)\n"
"v_mul_f32 v44, v44, v132\n"
"v_mul_f32 v45, v45, v133\n"
"v_mfma_f32_16x16x32_bf16 v[144:147], v[88:91], a[128:131], v[144:147]\n"
"ds_read_b64_tr_b16 v[80:81], v39 offset:28672\n"
"ds_read_b64_tr_b16 v[82:83], v39 offset:29696\n"
"v_mfma_f32_16x16x32_bf16 v[144:147], v[92:95], a[144:147], v[144:147]\n"
"ds_read_b64_tr_b16 v[84:85], v39 offset:30720\n"
"ds_read_b64_tr_b16 v[86:87], v39 offset:31744\n"
"v_mfma_f32_16x16x32_bf16 v[148:151], v[92:95], a[148:151], v[148:151]\n"
"v_mul_f32 v46, v46, v134\n"
"v_mul_f32 v47, v47, v135\n"
"v_mfma_f32_16x16x32_bf16 v[148:151], v[88:91], a[132:135], v[148:151]\n"
"v_exp_f32 v44, v44\n"
"v_mfma_f32_16x16x32_bf16 v[152:155], v[88:91], a[136:139], v[152:155]\n"
"v_exp_f32 v45, v45\n"
"v_mfma_f32_16x16x32_bf16 v[152:155], v[92:95], a[152:155], v[152:155]\n"
"v_exp_f32 v46, v46\n"
"v_mfma_f32_16x16x32_bf16 v[156:159], v[92:95], a[156:159], v[156:159]\n"
"v_exp_f32 v47, v47\n"
"v_mfma_f32_16x16x32_bf16 v[156:159], v[88:91], a[140:143], v[156:159]\n"
"s_waitcnt lgkmcnt(8)\n"
"v_fma_f32 v44, v44, s33, s33\n"
"v_fma_f32 v45, v45, s33, s33\n"
"v_mfma_f32_16x16x32_bf16 v[144:147], v[64:67], a[160:163], v[144:147]\n"
"ds_read_b64_tr_b16 v[88:89], v39 offset:32768\n"
"ds_read_b64_tr_b16 v[90:91], v39 offset:33792\n"
"v_mfma_f32_16x16x32_bf16 v[144:147], v[68:71], a[176:179], v[144:147]\n"
"ds_read_b64_tr_b16 v[92:93], v39 offset:34816\n"
"ds_read_b64_tr_b16 v[94:95], v39 offset:35840\n"
"v_mfma_f32_16x16x32_bf16 v[148:151], v[68:71], a[180:183], v[148:151]\n"
"v_add_u32 v39, s61, v39\n"
"v_fma_f32 v46, v46, s33, s33\n"
"v_mfma_f32_16x16x32_bf16 v[148:151], v[64:67], a[164:167], v[148:151]\n"
"v_fma_f32 v47, v47, s33, s33\n"
"v_mfma_f32_16x16x32_bf16 v[152:155], v[64:67], a[168:171], v[152:155]\n"
"v_rcp_f32 v44, v44\n"
"v_mfma_f32_16x16x32_bf16 v[152:155], v[68:71], a[184:187], v[152:155]\n"
"v_rcp_f32 v45, v45\n"
"v_mfma_f32_16x16x32_bf16 v[156:159], v[68:71], a[188:191], v[156:159]\n"
"v_rcp_f32 v46, v46\n"
"v_mfma_f32_16x16x32_bf16 v[156:159], v[64:67], a[172:175], v[156:159]\n"
"s_waitcnt vmcnt(1) lgkmcnt(0)\n"
"v_rcp_f32 v47, v47\n"
"v_mfma_f32_16x16x32_bf16 v[144:147], v[72:75], a[192:195], v[144:147]\n"
"s_barrier\n"
"ds_read_b64_tr_b16 v[64:65], v36 offset:0\n"
"ds_read_b64_tr_b16 v[66:67], v36 offset:1024\n"
"v_mfma_f32_16x16x32_bf16 v[144:147], v[76:79], a[208:211], v[144:147]\n"
"ds_read_b64_tr_b16 v[68:69], v36 offset:2048\n"
"ds_read_b64_tr_b16 v[70:71], v36 offset:3072\n"
"v_mfma_f32_16x16x32_bf16 v[148:151], v[76:79], a[212:215], v[148:151]\n"
"v_mul_f32 v132, v132, v44\n"
"v_mul_f32 v133, v133, v45\n"
"v_mfma_f32_16x16x32_bf16 v[148:151], v[72:75], a[196:199], v[148:151]\n"
"s_sub_u32 s60, s60, s61\n"
"v_mul_f32 v134, v134, v46\n"
"v_mul_f32 v135, v135, v47\n"
"v_mfma_f32_16x16x32_bf16 v[152:155], v[72:75], a[200:203], v[152:155]\n"
"s_sub_u32 s61, 0, s61\n"
"v_mul_f32 v44, v136, v136\n"
"v_mul_f32 v45, v137, v137\n"
"v_mfma_f32_16x16x32_bf16 v[152:155], v[76:79], a[216:219], v[152:155]\n"
"s_add_u32 s62, s62, 1\n"
"v_mul_f32 v46, v138, v138\n"
"v_mul_f32 v47, v139, v139\n"
"v_mfma_f32_16x16x32_bf16 v[156:159], v[76:79], a[220:223], v[156:159]\n"
"s_add_u32 s86, s86, 64\n"
"v_fma_f32 v44, v44, s63, v3\n"
"v_fma_f32 v45, v45, s63, v3\n"
"v_mfma_f32_16x16x32_bf16 v[156:159], v[72:75], a[204:207], v[156:159]\n"
"s_waitcnt lgkmcnt(8)\n"
"v_fma_f32 v46, v46, s63, v3\n"
"v_fma_f32 v47, v47, s63, v3\n"
"v_mfma_f32_16x16x32_bf16 v[144:147], v[80:83], a[224:227], v[144:147]\n"
"ds_read_b64_tr_b16 v[72:73], v36 offset:4096\n"
"ds_read_b64_tr_b16 v[74:75], v36 offset:5120\n"
"v_mfma_f32_16x16x32_bf16 v[144:147], v[84:87], a[240:243], v[144:147]\n"
"ds_read_b64_tr_b16 v[76:77], v36 offset:6144\n"
"ds_read_b64_tr_b16 v[78:79], v36 offset:7168\n"
"v_mfma_f32_16x16x32_bf16 v[148:151], v[84:87], a[244:247], v[148:151]\n"
"v_mul_f32 v44, v44, v136\n"
"v_mul_f32 v45, v45, v137\n"
"v_mfma_f32_16x16x32_bf16 v[148:151], v[80:83], a[228:231], v[148:151]\n"
"v_mul_f32 v46, v46, v138\n"
"v_mul_f32 v47, v47, v139\n"
"v_mfma_f32_16x16x32_bf16 v[152:155], v[80:83], a[232:235], v[152:155]\n"
"v_mfma_f32_16x16x32_bf16 v[152:155], v[84:87], a[248:251], v[152:155]\n"
"v_mfma_f32_16x16x32_bf16 v[156:159], v[84:87], a[252:255], v[156:159]\n"
"v_mfma_f32_16x16x32_bf16 v[156:159], v[80:83], a[236:239], v[156:159]\n"
"s_waitcnt lgkmcnt(8)\n"
"v_mfma_f32_16x16x32_bf16 v[144:147], v[88:91], v[224:227], v[144:147]\n"
"ds_read_b64_tr_b16 v[80:81], v36 offset:8192\n"
"ds_read_b64_tr_b16 v[82:83], v36 offset:9216\n"
"v_mfma_f32_16x16x32_bf16 v[144:147], v[92:95], v[240:243], v[144:147]\n"
"ds_read_b64_tr_b16 v[84:85], v36 offset:10240\n"
"ds_read_b64_tr_b16 v[86:87], v36 offset:11264\n"
"v_mfma_f32_16x16x32_bf16 v[148:151], v[92:95], v[244:247], v[148:151]\n"
"v_mfma_f32_16x16x32_bf16 v[148:151], v[88:91], v[228:231], v[148:151]\n"
"v_mfma_f32_16x16x32_bf16 v[152:155], v[88:91], v[232:235], v[152:155]\n"
"v_mfma_f32_16x16x32_bf16 v[152:155], v[92:95], v[248:251], v[152:155]\n"
"s_waitcnt lgkmcnt(8)\n"
"v_mfma_f32_16x16x32_bf16 v[156:159], v[92:95], v[252:255], v[156:159]\n"
"s_cmp_lt_u32 s62, 4\n"
"v_mfma_f32_16x16x32_bf16 v[156:159], v[88:91], v[236:239], v[156:159]\n"
"s_cbranch_scc1 L_gu\n"
"v_mfma_f32_16x16x32_bf16 v[96:99], v[64:67], a[0:3], 0\n"
"ds_read_b64_tr_b16 v[88:89], v36 offset:12288\n"
"ds_read_b64_tr_b16 v[90:91], v36 offset:13312\n"
"v_mfma_f32_16x16x32_bf16 v[96:99], v[68:71], a[16:19], v[96:99]\n"
"ds_read_b64_tr_b16 v[92:93], v36 offset:14336\n"
"ds_read_b64_tr_b16 v[94:95], v36 offset:15360\n"
"v_mfma_f32_16x16x32_bf16 v[100:103], v[68:71], a[20:23], 0\n"
"s_lshl_b32 s80, s52, 12\n"
"v_mfma_f32_16x16x32_bf16 v[100:103], v[64:67], a[4:7], v[100:103]\n"
"s_add_u32 m0, s92, 141312\n"
"v_mul_f32 v144, v144, v128\n"
"v_mul_f32 v145, v145, v129\n"
"v_mfma_f32_16x16x32_bf16 v[104:107], v[64:67], a[8:11], 0\n"
"buffer_load_dwordx4 v0, s[24:27], s80 offen lds\n"
"v_mul_f32 v146, v146, v130\n"
"v_mfma_f32_16x16x32_bf16 v[104:107], v[68:71], a[24:27], v[104:107]\n"
"s_add_u32 m0, m0, 32\n"
"v_mul_f32 v147, v147, v131\n"
"v_cvt_pk_bf16_f32 v50, v144, v145\n"
"v_mfma_f32_16x16x32_bf16 v[108:111], v[68:71], a[28:31], 0\n"
"v_cvt_pk_bf16_f32 v51, v146, v147\n"
"v_mfma_f32_16x16x32_bf16 v[108:111], v[64:67], a[12:15], v[108:111]\n"
"s_waitcnt lgkmcnt(8)\n"
"buffer_store_dwordx4 v[48:51], v0, s[36:39], s55 offen sc1\n"
"v_mul_f32 v148, v148, v132\n"
"v_mfma_f32_16x16x32_bf16 v[96:99], v[72:75], a[32:35], v[96:99]\n"
"s_add_u32 s55, s55, 1024\n"
"ds_read_b64_tr_b16 v[64:65], v36 offset:16384\n"
"ds_read_b64_tr_b16 v[66:67], v36 offset:17408\n"
"v_mfma_f32_16x16x32_bf16 v[96:99], v[76:79], a[48:51], v[96:99]\n"
"ds_read_b64_tr_b16 v[68:69], v36 offset:18432\n"
"ds_read_b64_tr_b16 v[70:71], v36 offset:19456\n"
"v_mfma_f32_16x16x32_bf16 v[100:103], v[76:79], a[52:55], v[100:103]\n"
"buffer_load_dwordx4 v0, s[24:27], s80 offen offset:1024 lds\n"
"v_mul_f32 v149, v149, v133\n"
"v_mfma_f32_16x16x32_bf16 v[100:103], v[72:75], a[36:39], v[100:103]\n"
"s_add_u32 m0, m0, 32\n"
"v_mul_f32 v150, v150, v134\n"
"v_mul_f32 v151, v151, v135\n"
"v_mfma_f32_16x16x32_bf16 v[104:107], v[72:75], a[40:43], v[104:107]\n"
"v_cvt_pk_bf16_f32 v54, v148, v149\n"
"v_cvt_pk_bf16_f32 v55, v150, v151\n"
"v_mfma_f32_16x16x32_bf16 v[104:107], v[76:79], a[56:59], v[104:107]\n"
"buffer_store_dwordx4 v[52:55], v0, s[36:39], s56 offen sc1\n"
"v_mfma_f32_16x16x32_bf16 v[108:111], v[76:79], a[60:63], v[108:111]\n"
"s_add_u32 s56, s56, 1024\n"
"v_exp_f32 v44, v44\n"
"v_mfma_f32_16x16x32_bf16 v[108:111], v[72:75], a[44:47], v[108:111]\n"
"s_waitcnt lgkmcnt(8)\n"
"v_exp_f32 v45, v45\n"
"v_mfma_f32_16x16x32_bf16 v[96:99], v[80:83], a[64:67], v[96:99]\n"
"ds_read_b64_tr_b16 v[72:73], v36 offset:20480\n"
"ds_read_b64_tr_b16 v[74:75], v36 offset:21504\n"
"v_mfma_f32_16x16x32_bf16 v[96:99], v[84:87], a[80:83], v[96:99]\n"
"ds_read_b64_tr_b16 v[76:77], v36 offset:22528\n"
"ds_read_b64_tr_b16 v[78:79], v36 offset:23552\n"
"v_mfma_f32_16x16x32_bf16 v[100:103], v[84:87], a[84:87], v[100:103]\n"
"buffer_load_dwordx4 v0, s[24:27], s80 offen offset:2048 lds\n"
"v_mfma_f32_16x16x32_bf16 v[100:103], v[80:83], a[68:71], v[100:103]\n"
"s_add_u32 m0, m0, 32\n"
"v_exp_f32 v46, v46\n"
"v_mfma_f32_16x16x32_bf16 v[104:107], v[80:83], a[72:75], v[104:107]\n"
"v_exp_f32 v47, v47\n"
"v_mfma_f32_16x16x32_bf16 v[104:107], v[84:87], a[88:91], v[104:107]\n"
"v_fma_f32 v44, v44, s33, s33\n"
"v_fma_f32 v45, v45, s33, s33\n"
"v_mfma_f32_16x16x32_bf16 v[108:111], v[84:87], a[92:95], v[108:111]\n"
"v_fma_f32 v46, v46, s33, s33\n"
"v_fma_f32 v47, v47, s33, s33\n"
"v_mfma_f32_16x16x32_bf16 v[108:111], v[80:83], a[76:79], v[108:111]\n"
"s_waitcnt lgkmcnt(8)\n"
"v_rcp_f32 v44, v44\n"
"v_mfma_f32_16x16x32_bf16 v[96:99], v[88:91], a[96:99], v[96:99]\n"
"ds_read_b64_tr_b16 v[80:81], v36 offset:24576\n"
"ds_read_b64_tr_b16 v[82:83], v36 offset:25600\n"
"v_mfma_f32_16x16x32_bf16 v[96:99], v[92:95], a[112:115], v[96:99]\n"
"ds_read_b64_tr_b16 v[84:85], v36 offset:26624\n"
"ds_read_b64_tr_b16 v[86:87], v36 offset:27648\n"
"v_mfma_f32_16x16x32_bf16 v[100:103], v[92:95], a[116:119], v[100:103]\n"
"buffer_load_dwordx4 v0, s[24:27], s80 offen offset:3072 lds\n"
"v_mfma_f32_16x16x32_bf16 v[100:103], v[88:91], a[100:103], v[100:103]\n"
"s_lshl_b32 s81, s52, 12\n"
"v_rcp_f32 v45, v45\n"
"v_mfma_f32_16x16x32_bf16 v[104:107], v[88:91], a[104:107], v[104:107]\n"
"s_add_u32 m0, s92, 73728\n"
"v_rcp_f32 v46, v46\n"
"v_mfma_f32_16x16x32_bf16 v[104:107], v[92:95], a[120:123], v[104:107]\n"
"v_rcp_f32 v47, v47\n"
"v_mfma_f32_16x16x32_bf16 v[108:111], v[92:95], a[124:127], v[108:111]\n"
"v_mul_f32 v136, v136, v44\n"
"v_mul_f32 v137, v137, v45\n"
"v_mfma_f32_16x16x32_bf16 v[108:111], v[88:91], a[108:111], v[108:111]\n"
"s_waitcnt lgkmcnt(8)\n"
"v_mul_f32 v138, v138, v46\n"
"v_mul_f32 v139, v139, v47\n"
"v_mfma_f32_16x16x32_bf16 v[96:99], v[64:67], a[128:131], v[96:99]\n"
"ds_read_b64_tr_b16 v[88:89], v36 offset:28672\n"
"ds_read_b64_tr_b16 v[90:91], v36 offset:29696\n"
"v_mfma_f32_16x16x32_bf16 v[96:99], v[68:71], a[144:147], v[96:99]\n"
"ds_read_b64_tr_b16 v[92:93], v36 offset:30720\n"
"ds_read_b64_tr_b16 v[94:95], v36 offset:31744\n"
"v_mfma_f32_16x16x32_bf16 v[100:103], v[68:71], a[148:151], v[100:103]\n"
"buffer_load_dwordx4 v0, s[28:31], s81 offen lds\n"
"v_mul_f32 v152, v152, v136\n"
"v_mfma_f32_16x16x32_bf16 v[100:103], v[64:67], a[132:135], v[100:103]\n"
"s_add_u32 m0, m0, 32\n"
"v_mul_f32 v153, v153, v137\n"
"v_mul_f32 v154, v154, v138\n"
"v_mfma_f32_16x16x32_bf16 v[104:107], v[64:67], a[136:139], v[104:107]\n"
"v_mul_f32 v155, v155, v139\n"
"v_cvt_pk_bf16_f32 v58, v152, v153\n"
"v_mfma_f32_16x16x32_bf16 v[104:107], v[68:71], a[152:155], v[104:107]\n"
"v_cvt_pk_bf16_f32 v59, v154, v155\n"
"v_mfma_f32_16x16x32_bf16 v[108:111], v[68:71], a[156:159], v[108:111]\n"
"buffer_store_dwordx4 v[56:59], v0, s[36:39], s57 offen sc1\n"
"v_mul_f32 v44, v140, v140\n"
"v_mfma_f32_16x16x32_bf16 v[108:111], v[64:67], a[140:143], v[108:111]\n"
"s_waitcnt lgkmcnt(8)\n"
"v_mul_f32 v45, v141, v141\n"
"v_mul_f32 v46, v142, v142\n"
"v_mfma_f32_16x16x32_bf16 v[96:99], v[72:75], a[160:163], v[96:99]\n"
"s_add_u32 s57, s57, 1024\n"
"ds_read_b64_tr_b16 v[64:65], v36 offset:32768\n"
"ds_read_b64_tr_b16 v[66:67], v36 offset:33792\n"
"v_mfma_f32_16x16x32_bf16 v[96:99], v[76:79], a[176:179], v[96:99]\n"
"ds_read_b64_tr_b16 v[68:69], v36 offset:34816\n"
"ds_read_b64_tr_b16 v[70:71], v36 offset:35840\n"
"v_mfma_f32_16x16x32_bf16 v[100:103], v[76:79], a[180:183], v[100:103]\n"
"buffer_load_dwordx4 v0, s[28:31], s81 offen offset:1024 lds\n"
"v_mul_f32 v47, v143, v143\n"
"v_mfma_f32_16x16x32_bf16 v[100:103], v[72:75], a[164:167], v[100:103]\n"
"s_add_u32 m0, m0, 32\n"
"v_fma_f32 v44, v44, s63, v3\n"
"v_fma_f32 v45, v45, s63, v3\n"
"v_mfma_f32_16x16x32_bf16 v[104:107], v[72:75], a[168:171], v[104:107]\n"
"v_fma_f32 v46, v46, s63, v3\n"
"v_fma_f32 v47, v47, s63, v3\n"
"v_mfma_f32_16x16x32_bf16 v[104:107], v[76:79], a[184:187], v[104:107]\n"
"v_mul_f32 v44, v44, v140\n"
"v_mul_f32 v45, v45, v141\n"
"v_mfma_f32_16x16x32_bf16 v[108:111], v[76:79], a[188:191], v[108:111]\n"
"v_mul_f32 v46, v46, v142\n"
"v_mul_f32 v47, v47, v143\n"
"v_mfma_f32_16x16x32_bf16 v[108:111], v[72:75], a[172:175], v[108:111]\n"
"s_waitcnt lgkmcnt(8)\n"
"v_exp_f32 v44, v44\n"
"v_mfma_f32_16x16x32_bf16 v[96:99], v[80:83], a[192:195], v[96:99]\n"
"ds_read_b64_tr_b16 v[72:73], v37 offset:0\n"
"ds_read_b64_tr_b16 v[74:75], v37 offset:1024\n"
"v_mfma_f32_16x16x32_bf16 v[96:99], v[84:87], a[208:211], v[96:99]\n"
"ds_read_b64_tr_b16 v[76:77], v37 offset:2048\n"
"ds_read_b64_tr_b16 v[78:79], v37 offset:3072\n"
"v_mfma_f32_16x16x32_bf16 v[100:103], v[84:87], a[212:215], v[100:103]\n"
"buffer_load_dwordx4 v0, s[28:31], s81 offen offset:2048 lds\n"
"v_mfma_f32_16x16x32_bf16 v[100:103], v[80:83], a[196:199], v[100:103]\n"
"s_add_u32 m0, m0, 32\n"
"v_exp_f32 v45, v45\n"
"v_mfma_f32_16x16x32_bf16 v[104:107], v[80:83], a[200:203], v[104:107]\n"
"v_exp_f32 v46, v46\n"
"v_mfma_f32_16x16x32_bf16 v[104:107], v[84:87], a[216:219], v[104:107]\n"
"v_exp_f32 v47, v47\n"
"v_mfma_f32_16x16x32_bf16 v[108:111], v[84:87], a[220:223], v[108:111]\n"
"v_fma_f32 v44, v44, s33, s33\n"
"v_fma_f32 v45, v45, s33, s33\n"
"v_mfma_f32_16x16x32_bf16 v[108:111], v[80:83], a[204:207], v[108:111]\n"
"s_waitcnt lgkmcnt(8)\n"
"v_fma_f32 v46, v46, s33, s33\n"
"v_fma_f32 v47, v47, s33, s33\n"
"v_mfma_f32_16x16x32_bf16 v[96:99], v[88:91], a[224:227], v[96:99]\n"
"ds_read_b64_tr_b16 v[80:81], v37 offset:4096\n"
"ds_read_b64_tr_b16 v[82:83], v37 offset:5120\n"
"v_mfma_f32_16x16x32_bf16 v[96:99], v[92:95], a[240:243], v[96:99]\n"
"ds_read_b64_tr_b16 v[84:85], v37 offset:6144\n"
"ds_read_b64_tr_b16 v[86:87], v37 offset:7168\n"
"v_mfma_f32_16x16x32_bf16 v[100:103], v[92:95], a[244:247], v[100:103]\n"
"buffer_load_dwordx4 v0, s[28:31], s81 offen offset:3072 lds\n"
"v_mfma_f32_16x16x32_bf16 v[100:103], v[88:91], a[228:231], v[100:103]\n"
"s_add_u32 s81, s81, 16384\n"
"v_rcp_f32 v44, v44\n"
"v_mfma_f32_16x16x32_bf16 v[104:107], v[88:91], a[232:235], v[104:107]\n"
"s_add_u32 m0, s92, 90624\n"
"v_rcp_f32 v45, v45\n"
"v_mfma_f32_16x16x32_bf16 v[104:107], v[92:95], a[248:251], v[104:107]\n"
"v_rcp_f32 v46, v46\n"
"v_mfma_f32_16x16x32_bf16 v[108:111], v[92:95], a[252:255], v[108:111]\n"
"v_rcp_f32 v47, v47\n"
"v_mfma_f32_16x16x32_bf16 v[108:111], v[88:91], a[236:239], v[108:111]\n"
"s_waitcnt lgkmcnt(8)\n"
"v_mul_f32 v140, v140, v44\n"
"v_mul_f32 v141, v141, v45\n"
"v_mfma_f32_16x16x32_bf16 v[96:99], v[64:67], v[224:227], v[96:99]\n"
"ds_read_b64_tr_b16 v[88:89], v37 offset:8192\n"
"ds_read_b64_tr_b16 v[90:91], v37 offset:9216\n"
"v_mfma_f32_16x16x32_bf16 v[96:99], v[68:71], v[240:243], v[96:99]\n"
"ds_read_b64_tr_b16 v[92:93], v37 offset:10240\n"
"ds_read_b64_tr_b16 v[94:95], v37 offset:11264\n"
"v_mfma_f32_16x16x32_bf16 v[100:103], v[68:71], v[244:247], v[100:103]\n"
"buffer_load_dwordx4 v0, s[28:31], s81 offen lds\n"
"v_mul_f32 v142, v142, v46\n"
"v_mfma_f32_16x16x32_bf16 v[100:103], v[64:67], v[228:231], v[100:103]\n"
"s_add_u32 m0, m0, 32\n"
"v_mul_f32 v143, v143, v47\n"
"v_mul_f32 v156, v156, v140\n"
"v_mfma_f32_16x16x32_bf16 v[104:107], v[64:67], v[232:235], v[104:107]\n"
"v_mul_f32 v157, v157, v141\n"
"v_mul_f32 v158, v158, v142\n"
"v_mfma_f32_16x16x32_bf16 v[104:107], v[68:71], v[248:251], v[104:107]\n"
"v_mul_f32 v159, v159, v143\n"
"v_cvt_pk_bf16_f32 v62, v156, v157\n"
"v_mfma_f32_16x16x32_bf16 v[108:111], v[68:71], v[252:255], v[108:111]\n"
"v_cvt_pk_bf16_f32 v63, v158, v159\n"
"v_mfma_f32_16x16x32_bf16 v[108:111], v[64:67], v[236:239], v[108:111]\n"
"s_waitcnt lgkmcnt(8)\n"
"buffer_store_dwordx4 v[60:63], v0, s[36:39], s58 offen sc1\n"
"v_mfma_f32_16x16x32_bf16 v[112:115], v[72:75], a[0:3], 0\n"
"s_add_u32 s58, s58, 1024\n"
"ds_read_b64_tr_b16 v[64:65], v37 offset:12288\n"
"ds_read_b64_tr_b16 v[66:67], v37 offset:13312\n"
"v_mfma_f32_16x16x32_bf16 v[112:115], v[76:79], a[16:19], v[112:115]\n"
"ds_read_b64_tr_b16 v[68:69], v37 offset:14336\n"
"ds_read_b64_tr_b16 v[70:71], v37 offset:15360\n"
"v_mfma_f32_16x16x32_bf16 v[116:119], v[76:79], a[20:23], 0\n"
"buffer_load_dwordx4 v0, s[28:31], s81 offen offset:1024 lds\n"
"v_mfma_f32_16x16x32_bf16 v[116:119], v[72:75], a[4:7], v[116:119]\n"
"s_add_u32 m0, m0, 32\n"
"v_mul_f32 v44, v96, v96\n"
"v_mul_f32 v45, v97, v97\n"
"v_mfma_f32_16x16x32_bf16 v[120:123], v[72:75], a[8:11], 0\n"
"v_mul_f32 v46, v98, v98\n"
"v_mul_f32 v47, v99, v99\n"
"v_mfma_f32_16x16x32_bf16 v[120:123], v[76:79], a[24:27], v[120:123]\n"
"v_fma_f32 v44, v44, s63, v3\n"
"v_fma_f32 v45, v45, s63, v3\n"
"v_mfma_f32_16x16x32_bf16 v[124:127], v[76:79], a[28:31], 0\n"
"v_fma_f32 v46, v46, s63, v3\n"
"v_fma_f32 v47, v47, s63, v3\n"
"v_mfma_f32_16x16x32_bf16 v[124:127], v[72:75], a[12:15], v[124:127]\n"
"s_waitcnt lgkmcnt(8)\n"
"v_mul_f32 v44, v44, v96\n"
"v_mul_f32 v45, v45, v97\n"
"v_mfma_f32_16x16x32_bf16 v[112:115], v[80:83], a[32:35], v[112:115]\n"
"ds_read_b64_tr_b16 v[72:73], v37 offset:16384\n"
"ds_read_b64_tr_b16 v[74:75], v37 offset:17408\n"
"v_mfma_f32_16x16x32_bf16 v[112:115], v[84:87], a[48:51], v[112:115]\n"
"ds_read_b64_tr_b16 v[76:77], v37 offset:18432\n"
"ds_read_b64_tr_b16 v[78:79], v37 offset:19456\n"
"v_mfma_f32_16x16x32_bf16 v[116:119], v[84:87], a[52:55], v[116:119]\n"
"buffer_load_dwordx4 v0, s[28:31], s81 offen offset:2048 lds\n"
"v_mul_f32 v46, v46, v98\n"
"v_mfma_f32_16x16x32_bf16 v[116:119], v[80:83], a[36:39], v[116:119]\n"
"s_add_u32 m0, m0, 32\n"
"v_mul_f32 v47, v47, v99\n"
"v_bfe_u32 v2, v0, 4, 1\n"
"v_mfma_f32_16x16x32_bf16 v[120:123], v[80:83], a[40:43], v[120:123]\n"
"v_exp_f32 v44, v44\n"
"v_mfma_f32_16x16x32_bf16 v[120:123], v[84:87], a[56:59], v[120:123]\n"
"v_exp_f32 v45, v45\n"
"v_mfma_f32_16x16x32_bf16 v[124:127], v[84:87], a[60:63], v[124:127]\n"
"v_exp_f32 v46, v46\n"
"v_mfma_f32_16x16x32_bf16 v[124:127], v[80:83], a[44:47], v[124:127]\n"
"s_waitcnt lgkmcnt(8)\n"
"v_exp_f32 v47, v47\n"
"v_mfma_f32_16x16x32_bf16 v[112:115], v[88:91], a[64:67], v[112:115]\n"
"ds_read_b64_tr_b16 v[80:81], v37 offset:20480\n"
"ds_read_b64_tr_b16 v[82:83], v37 offset:21504\n"
"v_mfma_f32_16x16x32_bf16 v[112:115], v[92:95], a[80:83], v[112:115]\n"
"ds_read_b64_tr_b16 v[84:85], v37 offset:22528\n"
"ds_read_b64_tr_b16 v[86:87], v37 offset:23552\n"
"v_mfma_f32_16x16x32_bf16 v[116:119], v[92:95], a[84:87], v[116:119]\n"
"buffer_load_dwordx4 v0, s[28:31], s81 offen offset:3072 lds\n"
"v_fma_f32 v44, v44, s33, s33\n"
"v_mfma_f32_16x16x32_bf16 v[116:119], v[88:91], a[68:71], v[116:119]\n"
"s_add_u32 s80, s80, 32768\n"
"v_fma_f32 v45, v45, s33, s33\n"
"v_fma_f32 v46, v46, s33, s33\n"
"v_mfma_f32_16x16x32_bf16 v[120:123], v[88:91], a[72:75], v[120:123]\n"
"s_add_u32 m0, s92, 107520\n"
"v_fma_f32 v47, v47, s33, s33\n"
"v_mul_u32_u24 v2, 0xaaaa, v2\n"
"v_mfma_f32_16x16x32_bf16 v[120:123], v[92:95], a[88:91], v[120:123]\n"
"v_rcp_f32 v44, v44\n"
"v_mfma_f32_16x16x32_bf16 v[124:127], v[92:95], a[92:95], v[124:127]\n"
"v_rcp_f32 v45, v45\n"
"v_mfma_f32_16x16x32_bf16 v[124:127], v[88:91], a[76:79], v[124:127]\n"
"s_waitcnt lgkmcnt(8)\n"
"v_rcp_f32 v46, v46\n"
"v_mfma_f32_16x16x32_bf16 v[112:115], v[64:67], a[96:99], v[112:115]\n"
"ds_read_b64_tr_b16 v[88:89], v37 offset:24576\n"
"ds_read_b64_tr_b16 v[90:91], v37 offset:25600\n"
"v_mfma_f32_16x16x32_bf16 v[112:115], v[68:71], a[112:115], v[112:115]\n"
"ds_read_b64_tr_b16 v[92:93], v37 offset:26624\n"
"ds_read_b64_tr_b16 v[94:95], v37 offset:27648\n"
"v_mfma_f32_16x16x32_bf16 v[116:119], v[68:71], a[116:119], v[116:119]\n"
"buffer_load_dwordx4 v0, s[24:27], s80 offen lds\n"
"v_add_u32 v2, 0x4444, v2\n"
"v_mfma_f32_16x16x32_bf16 v[116:119], v[64:67], a[100:103], v[116:119]\n"
"s_add_u32 m0, m0, 32\n"
"v_rcp_f32 v47, v47\n"
"v_mfma_f32_16x16x32_bf16 v[120:123], v[64:67], a[104:107], v[120:123]\n"
"v_mul_f32 v96, v96, v44\n"
"v_mul_f32 v97, v97, v45\n"
"v_mfma_f32_16x16x32_bf16 v[120:123], v[68:71], a[120:123], v[120:123]\n"
"v_mul_f32 v98, v98, v46\n"
"v_mul_f32 v99, v99, v47\n"
"v_mfma_f32_16x16x32_bf16 v[124:127], v[68:71], a[124:127], v[124:127]\n"
"v_mul_f32 v44, v100, v100\n"
"v_mul_f32 v45, v101, v101\n"
"v_mfma_f32_16x16x32_bf16 v[124:127], v[64:67], a[108:111], v[124:127]\n"
"s_waitcnt lgkmcnt(8)\n"
"v_mul_f32 v46, v102, v102\n"
"v_mul_f32 v47, v103, v103\n"
"v_mfma_f32_16x16x32_bf16 v[112:115], v[72:75], a[128:131], v[112:115]\n"
"ds_read_b64_tr_b16 v[64:65], v37 offset:28672\n"
"ds_read_b64_tr_b16 v[66:67], v37 offset:29696\n"
"v_mfma_f32_16x16x32_bf16 v[112:115], v[76:79], a[144:147], v[112:115]\n"
"ds_read_b64_tr_b16 v[68:69], v37 offset:30720\n"
"ds_read_b64_tr_b16 v[70:71], v37 offset:31744\n"
"v_mfma_f32_16x16x32_bf16 v[116:119], v[76:79], a[148:151], v[116:119]\n"
"buffer_load_dwordx4 v0, s[24:27], s80 offen offset:1024 lds\n"
"v_fma_f32 v44, v44, s63, v3\n"
"v_mfma_f32_16x16x32_bf16 v[116:119], v[72:75], a[132:135], v[116:119]\n"
"s_add_u32 m0, m0, 32\n"
"v_fma_f32 v45, v45, s63, v3\n"
"v_fma_f32 v46, v46, s63, v3\n"
"v_mfma_f32_16x16x32_bf16 v[120:123], v[72:75], a[136:139], v[120:123]\n"
"v_fma_f32 v47, v47, s63, v3\n"
"v_mul_f32 v44, v44, v100\n"
"v_mfma_f32_16x16x32_bf16 v[120:123], v[76:79], a[152:155], v[120:123]\n"
"v_mul_f32 v45, v45, v101\n"
"v_mul_f32 v46, v46, v102\n"
"v_mfma_f32_16x16x32_bf16 v[124:127], v[76:79], a[156:159], v[124:127]\n"
"v_mul_f32 v47, v47, v103\n"
"v_mfma_f32_16x16x32_bf16 v[124:127], v[72:75], a[140:143], v[124:127]\n"
"s_waitcnt lgkmcnt(8)\n"
"v_exp_f32 v44, v44\n"
"v_mfma_f32_16x16x32_bf16 v[112:115], v[80:83], a[160:163], v[112:115]\n"
"ds_read_b64_tr_b16 v[72:73], v37 offset:32768\n"
"ds_read_b64_tr_b16 v[74:75], v37 offset:33792\n"
"v_mfma_f32_16x16x32_bf16 v[112:115], v[84:87], a[176:179], v[112:115]\n"
"ds_read_b64_tr_b16 v[76:77], v37 offset:34816\n"
"ds_read_b64_tr_b16 v[78:79], v37 offset:35840\n"
"v_mfma_f32_16x16x32_bf16 v[116:119], v[84:87], a[180:183], v[116:119]\n"
"buffer_load_dwordx4 v0, s[24:27], s80 offen offset:2048 lds\n"
"v_mfma_f32_16x16x32_bf16 v[116:119], v[80:83], a[164:167], v[116:119]\n"
"s_add_u32 m0, m0, 32\n"
"v_exp_f32 v45, v45\n"
"v_mfma_f32_16x16x32_bf16 v[120:123], v[80:83], a[168:171], v[120:123]\n"
"v_exp_f32 v46, v46\n"
"v_mfma_f32_16x16x32_bf16 v[120:123], v[84:87], a[184:187], v[120:123]\n"
"v_exp_f32 v47, v47\n"
"v_mfma_f32_16x16x32_bf16 v[124:127], v[84:87], a[188:191], v[124:127]\n"
"v_fma_f32 v44, v44, s33, s33\n"
"v_fma_f32 v45, v45, s33, s33\n"
"v_mfma_f32_16x16x32_bf16 v[124:127], v[80:83], a[172:175], v[124:127]\n"
"s_waitcnt lgkmcnt(8)\n"
"v_fma_f32 v46, v46, s33, s33\n"
"v_fma_f32 v47, v47, s33, s33\n"
"v_mfma_f32_16x16x32_bf16 v[112:115], v[88:91], a[192:195], v[112:115]\n"
"ds_read_b64_tr_b16 v[80:81], v38 offset:0\n"
"ds_read_b64_tr_b16 v[82:83], v38 offset:1024\n"
"v_mfma_f32_16x16x32_bf16 v[112:115], v[92:95], a[208:211], v[112:115]\n"
"ds_read_b64_tr_b16 v[84:85], v38 offset:2048\n"
"ds_read_b64_tr_b16 v[86:87], v38 offset:3072\n"
"v_mfma_f32_16x16x32_bf16 v[116:119], v[92:95], a[212:215], v[116:119]\n"
"buffer_load_dwordx4 v0, s[24:27], s80 offen offset:3072 lds\n"
"v_mfma_f32_16x16x32_bf16 v[116:119], v[88:91], a[196:199], v[116:119]\n"
"s_add_u32 s80, s80, 16384\n"
"v_rcp_f32 v44, v44\n"
"v_mfma_f32_16x16x32_bf16 v[120:123], v[88:91], a[200:203], v[120:123]\n"
"s_add_u32 m0, s92, 124416\n"
"v_rcp_f32 v45, v45\n"
"v_mfma_f32_16x16x32_bf16 v[120:123], v[92:95], a[216:219], v[120:123]\n"
"v_rcp_f32 v46, v46\n"
"v_mfma_f32_16x16x32_bf16 v[124:127], v[92:95], a[220:223], v[124:127]\n"
"v_rcp_f32 v47, v47\n"
"v_mfma_f32_16x16x32_bf16 v[124:127], v[88:91], a[204:207], v[124:127]\n"
"s_waitcnt lgkmcnt(8)\n"
"v_mul_f32 v100, v100, v44\n"
"v_mul_f32 v101, v101, v45\n"
"v_mfma_f32_16x16x32_bf16 v[112:115], v[64:67], a[224:227], v[112:115]\n"
"ds_read_b64_tr_b16 v[88:89], v38 offset:4096\n"
"ds_read_b64_tr_b16 v[90:91], v38 offset:5120\n"
"v_mfma_f32_16x16x32_bf16 v[112:115], v[68:71], a[240:243], v[112:115]\n"
"ds_read_b64_tr_b16 v[92:93], v38 offset:6144\n"
"ds_read_b64_tr_b16 v[94:95], v38 offset:7168\n"
"v_mfma_f32_16x16x32_bf16 v[116:119], v[68:71], a[244:247], v[116:119]\n"
"buffer_load_dwordx4 v0, s[24:27], s80 offen lds\n"
"v_mul_f32 v102, v102, v46\n"
"v_mfma_f32_16x16x32_bf16 v[116:119], v[64:67], a[228:231], v[116:119]\n"
"s_add_u32 m0, m0, 32\n"
"v_mul_f32 v103, v103, v47\n"
"v_mul_f32 v44, v104, v104\n"
"v_mfma_f32_16x16x32_bf16 v[120:123], v[64:67], a[232:235], v[120:123]\n"
"v_mul_f32 v45, v105, v105\n"
"v_mul_f32 v46, v106, v106\n"
"v_mfma_f32_16x16x32_bf16 v[120:123], v[68:71], a[248:251], v[120:123]\n"
"v_mul_f32 v47, v107, v107\n"
"v_fma_f32 v44, v44, s63, v3\n"
"v_mfma_f32_16x16x32_bf16 v[124:127], v[68:71], a[252:255], v[124:127]\n"
"v_fma_f32 v45, v45, s63, v3\n"
"v_fma_f32 v46, v46, s63, v3\n"
"v_mfma_f32_16x16x32_bf16 v[124:127], v[64:67], a[236:239], v[124:127]\n"
"s_waitcnt lgkmcnt(8)\n"
"v_fma_f32 v47, v47, s63, v3\n"
"v_mul_f32 v44, v44, v104\n"
"v_mfma_f32_16x16x32_bf16 v[112:115], v[72:75], v[224:227], v[112:115]\n"
"ds_read_b64_tr_b16 v[64:65], v38 offset:8192\n"
"ds_read_b64_tr_b16 v[66:67], v38 offset:9216\n"
"v_mfma_f32_16x16x32_bf16 v[112:115], v[76:79], v[240:243], v[112:115]\n"
"ds_read_b64_tr_b16 v[68:69], v38 offset:10240\n"
"ds_read_b64_tr_b16 v[70:71], v38 offset:11264\n"
"v_mfma_f32_16x16x32_bf16 v[116:119], v[76:79], v[244:247], v[116:119]\n"
"buffer_load_dwordx4 v0, s[24:27], s80 offen offset:1024 lds\n"
"v_mul_f32 v45, v45, v105\n"
"v_mfma_f32_16x16x32_bf16 v[116:119], v[72:75], v[228:231], v[116:119]\n"
"s_add_u32 m0, m0, 32\n"
"v_mul_f32 v46, v46, v106\n"
"v_mul_f32 v47, v47, v107\n"
"v_mfma_f32_16x16x32_bf16 v[120:123], v[72:75], v[232:235], v[120:123]\n"
"v_mfma_f32_16x16x32_bf16 v[120:123], v[76:79], v[248:251], v[120:123]\n"
"v_mfma_f32_16x16x32_bf16 v[124:127], v[76:79], v[252:255], v[124:127]\n"
"v_mfma_f32_16x16x32_bf16 v[124:127], v[72:75], v[236:239], v[124:127]\n"
"s_waitcnt lgkmcnt(8)\n"
"v_mfma_f32_16x16x32_bf16 v[144:147], v[80:83], a[0:3], 0\n"
"ds_read_b64_tr_b16 v[72:73], v38 offset:12288\n"
"ds_read_b64_tr_b16 v[74:75], v38 offset:13312\n"
"v_mfma_f32_16x16x32_bf16 v[144:147], v[84:87], a[16:19], v[144:147]\n"
"ds_read_b64_tr_b16 v[76:77], v38 offset:14336\n"
"ds_read_b64_tr_b16 v[78:79], v38 offset:15360\n"
"v_mfma_f32_16x16x32_bf16 v[148:151], v[84:87], a[20:23], 0\n"
"buffer_load_dwordx4 v0, s[24:27], s80 offen offset:2048 lds\n"
"v_mfma_f32_16x16x32_bf16 v[148:151], v[80:83], a[4:7], v[148:151]\n"
"s_add_u32 m0, m0, 32\n"
"v_mul_f32 v112, v112, v96\n"
"v_mul_f32 v113, v113, v97\n"
"v_mfma_f32_16x16x32_bf16 v[152:155], v[80:83], a[8:11], 0\n"
"v_mul_f32 v114, v114, v98\n"
"v_mul_f32 v115, v115, v99\n"
"v_mfma_f32_16x16x32_bf16 v[152:155], v[84:87], a[24:27], v[152:155]\n"
"v_cvt_pk_bf16_f32 v48, v112, v113\n"
"v_cvt_pk_bf16_f32 v49, v114, v115\n"
"v_mfma_f32_16x16x32_bf16 v[156:159], v[84:87], a[28:31], 0\n"
"v_mul_f32 v116, v116, v100\n"
"v_mul_f32 v117, v117, v101\n"
"v_mfma_f32_16x16x32_bf16 v[156:159], v[80:83], a[12:15], v[156:159]\n"
"s_waitcnt lgkmcnt(8)\n"
"v_mul_f32 v118, v118, v102\n"
"v_mul_f32 v119, v119, v103\n"
"v_mfma_f32_16x16x32_bf16 v[144:147], v[88:91], a[32:35], v[144:147]\n"
"ds_read_b64_tr_b16 v[80:81], v38 offset:16384\n"
"ds_read_b64_tr_b16 v[82:83], v38 offset:17408\n"
"v_mfma_f32_16x16x32_bf16 v[144:147], v[92:95], a[48:51], v[144:147]\n"
"ds_read_b64_tr_b16 v[84:85], v38 offset:18432\n"
"ds_read_b64_tr_b16 v[86:87], v38 offset:19456\n"
"v_mfma_f32_16x16x32_bf16 v[148:151], v[92:95], a[52:55], v[148:151]\n"
"buffer_load_dwordx4 v0, s[24:27], s80 offen offset:3072 lds\n"
"v_cvt_pk_bf16_f32 v52, v116, v117\n"
"v_mfma_f32_16x16x32_bf16 v[148:151], v[88:91], a[36:39], v[148:151]\n"
"s_xor_b32 s34, s90, 16\n"
"v_cvt_pk_bf16_f32 v53, v118, v119\n"
"v_mfma_f32_16x16x32_bf16 v[152:155], v[88:91], a[40:43], v[152:155]\n"
"s_mul_i32 s34, s34, 8192\n"
"v_exp_f32 v44, v44\n"
"v_mfma_f32_16x16x32_bf16 v[152:155], v[92:95], a[56:59], v[152:155]\n"
"s_sub_u32 s4, s4, s34\n"
"s_subb_u32 s5, s5, 0\n"
"v_mfma_f32_16x16x32_bf16 v[156:159], v[92:95], a[60:63], v[156:159]\n"
"global_load_dword v42, v43, s[4:5]\n"
"v_mfma_f32_16x16x32_bf16 v[156:159], v[88:91], a[44:47], v[156:159]\n"
"s_waitcnt lgkmcnt(8)\n"
"v_exp_f32 v45, v45\n"
"v_mfma_f32_16x16x32_bf16 v[144:147], v[64:67], a[64:67], v[144:147]\n"
"ds_read_b64_tr_b16 v[88:89], v38 offset:20480\n"
"ds_read_b64_tr_b16 v[90:91], v38 offset:21504\n"
"v_mfma_f32_16x16x32_bf16 v[144:147], v[68:71], a[80:83], v[144:147]\n"
"ds_read_b64_tr_b16 v[92:93], v38 offset:22528\n"
"ds_read_b64_tr_b16 v[94:95], v38 offset:23552\n"
"v_mfma_f32_16x16x32_bf16 v[148:151], v[68:71], a[84:87], v[148:151]\n"
"v_exp_f32 v46, v46\n"
"v_mfma_f32_16x16x32_bf16 v[148:151], v[64:67], a[68:71], v[148:151]\n"
"v_exp_f32 v47, v47\n"
"v_mfma_f32_16x16x32_bf16 v[152:155], v[64:67], a[72:75], v[152:155]\n"
"v_fma_f32 v44, v44, s33, s33\n"
"v_fma_f32 v45, v45, s33, s33\n"
"v_mfma_f32_16x16x32_bf16 v[152:155], v[68:71], a[88:91], v[152:155]\n"
"v_fma_f32 v46, v46, s33, s33\n"
"v_fma_f32 v47, v47, s33, s33\n"
"v_mfma_f32_16x16x32_bf16 v[156:159], v[68:71], a[92:95], v[156:159]\n"
"v_rcp_f32 v44, v44\n"
"v_mfma_f32_16x16x32_bf16 v[156:159], v[64:67], a[76:79], v[156:159]\n"
"s_waitcnt lgkmcnt(8)\n"
"v_rcp_f32 v45, v45\n"
"v_mfma_f32_16x16x32_bf16 v[144:147], v[72:75], a[96:99], v[144:147]\n"
"ds_read_b64_tr_b16 v[64:65], v38 offset:24576\n"
"ds_read_b64_tr_b16 v[66:67], v38 offset:25600\n"
"v_mfma_f32_16x16x32_bf16 v[144:147], v[76:79], a[112:115], v[144:147]\n"
"ds_read_b64_tr_b16 v[68:69], v38 offset:26624\n"
"ds_read_b64_tr_b16 v[70:71], v38 offset:27648\n"
"v_mfma_f32_16x16x32_bf16 v[148:151], v[76:79], a[116:119], v[148:151]\n"
"v_rcp_f32 v46, v46\n"
"v_mfma_f32_16x16x32_bf16 v[148:151], v[72:75], a[100:103], v[148:151]\n"
"v_rcp_f32 v47, v47\n"
"v_mfma_f32_16x16x32_bf16 v[152:155], v[72:75], a[104:107], v[152:155]\n"
"v_mul_f32 v104, v104, v44\n"
"v_mul_f32 v105, v105, v45\n"
"v_mfma_f32_16x16x32_bf16 v[152:155], v[76:79], a[120:123], v[152:155]\n"
"v_mul_f32 v106, v106, v46\n"
"v_mul_f32 v107, v107, v47\n"
"v_mfma_f32_16x16x32_bf16 v[156:159], v[76:79], a[124:127], v[156:159]\n"
"v_mul_f32 v120, v120, v104\n"
"v_mul_f32 v121, v121, v105\n"
"v_mfma_f32_16x16x32_bf16 v[156:159], v[72:75], a[108:111], v[156:159]\n"
"s_waitcnt vmcnt(19) lgkmcnt(8)\n"
"v_mul_f32 v122, v122, v106\n"
"v_mul_f32 v123, v123, v107\n"
"v_mfma_f32_16x16x32_bf16 v[144:147], v[80:83], a[128:131], v[144:147]\n"
"s_barrier\n"
"ds_read_b64_tr_b16 v[72:73], v38 offset:28672\n"
"ds_read_b64_tr_b16 v[74:75], v38 offset:29696\n"
"v_mfma_f32_16x16x32_bf16 v[144:147], v[84:87], a[144:147], v[144:147]\n"
"s_sub_u32 s59, s80, 32768\n"
"ds_read_b64_tr_b16 v[76:77], v38 offset:30720\n"
"ds_read_b64_tr_b16 v[78:79], v38 offset:31744\n"
"v_mfma_f32_16x16x32_bf16 v[148:151], v[84:87], a[148:151], v[148:151]\n"
"s_add_u32 m0, s92, 0\n"
"v_cvt_pk_bf16_f32 v56, v120, v121\n"
"v_cvt_pk_bf16_f32 v57, v122, v123\n"
"v_mfma_f32_16x16x32_bf16 v[148:151], v[80:83], a[132:135], v[148:151]\n"
"buffer_load_dwordx4 v0, s[24:27], s59 offen lds\n"
"v_mul_f32 v44, v108, v108\n"
"v_mfma_f32_16x16x32_bf16 v[152:155], v[80:83], a[136:139], v[152:155]\n"
"s_add_u32 m0, m0, 32\n"
"v_mul_f32 v45, v109, v109\n"
"v_mul_f32 v46, v110, v110\n"
"v_mfma_f32_16x16x32_bf16 v[152:155], v[84:87], a[152:155], v[152:155]\n"
"v_mul_f32 v47, v111, v111\n"
"v_fma_f32 v44, v44, s63, v3\n"
"v_mfma_f32_16x16x32_bf16 v[156:159], v[84:87], a[156:159], v[156:159]\n"
"v_fma_f32 v45, v45, s63, v3\n"
"v_fma_f32 v46, v46, s63, v3\n"
"v_mfma_f32_16x16x32_bf16 v[156:159], v[80:83], a[140:143], v[156:159]\n"
"s_waitcnt lgkmcnt(8)\n"
"v_fma_f32 v47, v47, s63, v3\n"
"v_mul_f32 v44, v44, v108\n"
"v_mfma_f32_16x16x32_bf16 v[144:147], v[88:91], a[160:163], v[144:147]\n"
"ds_read_b64_tr_b16 v[80:81], v38 offset:32768\n"
"ds_read_b64_tr_b16 v[82:83], v38 offset:33792\n"
"v_mfma_f32_16x16x32_bf16 v[144:147], v[92:95], a[176:179], v[144:147]\n"
"ds_read_b64_tr_b16 v[84:85], v38 offset:34816\n"
"ds_read_b64_tr_b16 v[86:87], v38 offset:35840\n"
"v_mfma_f32_16x16x32_bf16 v[148:151], v[92:95], a[180:183], v[148:151]\n"
"buffer_load_dwordx4 v0, s[24:27], s59 offen offset:1024 lds\n"
"v_mul_f32 v45, v45, v109\n"
"v_mfma_f32_16x16x32_bf16 v[148:151], v[88:91], a[164:167], v[148:151]\n"
"s_add_u32 m0, m0, 32\n"
"v_mul_f32 v46, v46, v110\n"
"v_mul_f32 v47, v47, v111\n"
"v_mfma_f32_16x16x32_bf16 v[152:155], v[88:91], a[168:171], v[152:155]\n"
"v_exp_f32 v44, v44\n"
"v_mfma_f32_16x16x32_bf16 v[152:155], v[92:95], a[184:187], v[152:155]\n"
"v_exp_f32 v45, v45\n"
"v_mfma_f32_16x16x32_bf16 v[156:159], v[92:95], a[188:191], v[156:159]\n"
"v_exp_f32 v46, v46\n"
"v_mfma_f32_16x16x32_bf16 v[156:159], v[88:91], a[172:175], v[156:159]\n"
"s_waitcnt lgkmcnt(8)\n"
"v_exp_f32 v47, v47\n"
"v_mfma_f32_16x16x32_bf16 v[144:147], v[64:67], a[192:195], v[144:147]\n"
"ds_read_b64_tr_b16 v[88:89], v39 offset:0\n"
"ds_read_b64_tr_b16 v[90:91], v39 offset:1024\n"
"v_mfma_f32_16x16x32_bf16 v[144:147], v[68:71], a[208:211], v[144:147]\n"
"ds_read_b64_tr_b16 v[92:93], v39 offset:2048\n"
"ds_read_b64_tr_b16 v[94:95], v39 offset:3072\n"
"v_mfma_f32_16x16x32_bf16 v[148:151], v[68:71], a[212:215], v[148:151]\n"
"buffer_load_dwordx4 v0, s[24:27], s59 offen offset:2048 lds\n"
"v_fma_f32 v44, v44, s33, s33\n"
"v_mfma_f32_16x16x32_bf16 v[148:151], v[64:67], a[196:199], v[148:151]\n"
"s_add_u32 m0, m0, 32\n"
"v_fma_f32 v45, v45, s33, s33\n"
"v_fma_f32 v46, v46, s33, s33\n"
"v_mfma_f32_16x16x32_bf16 v[152:155], v[64:67], a[200:203], v[152:155]\n"
"v_fma_f32 v47, v47, s33, s33\n"
"v_mfma_f32_16x16x32_bf16 v[152:155], v[68:71], a[216:219], v[152:155]\n"
"v_rcp_f32 v44, v44\n"
"v_mfma_f32_16x16x32_bf16 v[156:159], v[68:71], a[220:223], v[156:159]\n"
"v_rcp_f32 v45, v45\n"
"v_mfma_f32_16x16x32_bf16 v[156:159], v[64:67], a[204:207], v[156:159]\n"
"s_waitcnt lgkmcnt(8)\n"
"v_rcp_f32 v46, v46\n"
"v_mfma_f32_16x16x32_bf16 v[144:147], v[72:75], a[224:227], v[144:147]\n"
"ds_read_b64_tr_b16 v[64:65], v39 offset:4096\n"
"ds_read_b64_tr_b16 v[66:67], v39 offset:5120\n"
"v_mfma_f32_16x16x32_bf16 v[144:147], v[76:79], a[240:243], v[144:147]\n"
"ds_read_b64_tr_b16 v[68:69], v39 offset:6144\n"
"ds_read_b64_tr_b16 v[70:71], v39 offset:7168\n"
"v_mfma_f32_16x16x32_bf16 v[148:151], v[76:79], a[244:247], v[148:151]\n"
"buffer_load_dwordx4 v0, s[24:27], s59 offen offset:3072 lds\n"
"v_mfma_f32_16x16x32_bf16 v[148:151], v[72:75], a[228:231], v[148:151]\n"
"s_mov_b32 m0, s84\n"
"v_rcp_f32 v47, v47\n"
"v_mfma_f32_16x16x32_bf16 v[152:155], v[72:75], a[232:235], v[152:155]\n"
"v_mul_f32 v108, v108, v44\n"
"v_mul_f32 v109, v109, v45\n"
"v_mfma_f32_16x16x32_bf16 v[152:155], v[76:79], a[248:251], v[152:155]\n"
"v_mul_f32 v110, v110, v46\n"
"v_mul_f32 v111, v111, v47\n"
"v_mfma_f32_16x16x32_bf16 v[156:159], v[76:79], a[252:255], v[156:159]\n"
"v_mul_f32 v124, v124, v108\n"
"v_mul_f32 v125, v125, v109\n"
"v_mfma_f32_16x16x32_bf16 v[156:159], v[72:75], a[236:239], v[156:159]\n"
"s_waitcnt lgkmcnt(8)\n"
"v_mul_f32 v126, v126, v110\n"
"v_mul_f32 v127, v127, v111\n"
"v_mfma_f32_16x16x32_bf16 v[144:147], v[80:83], v[224:227], v[144:147]\n"
"ds_read_b64_tr_b16 v[72:73], v39 offset:8192\n"
"ds_read_b64_tr_b16 v[74:75], v39 offset:9216\n"
"v_mfma_f32_16x16x32_bf16 v[144:147], v[84:87], v[240:243], v[144:147]\n"
"ds_read_b64_tr_b16 v[76:77], v39 offset:10240\n"
"ds_read_b64_tr_b16 v[78:79], v39 offset:11264\n"
"v_mfma_f32_16x16x32_bf16 v[148:151], v[84:87], v[244:247], v[148:151]\n"
"buffer_load_dwordx4 v28, s[44:47], s82 offen lds\n"
"v_cvt_pk_bf16_f32 v60, v124, v125\n"
"v_mfma_f32_16x16x32_bf16 v[148:151], v[80:83], v[228:231], v[148:151]\n"
"s_mov_b32 s20, s42\n"
"v_cvt_pk_bf16_f32 v61, v126, v127\n"
"v_mfma_f32_16x16x32_bf16 v[152:155], v[80:83], v[232:235], v[152:155]\n"
"s_and_b32 s21, s43, 0xffff\n"
"v_mfma_f32_16x16x32_bf16 v[152:155], v[84:87], v[248:251], v[152:155]\n"
"s_mov_b32 s22, 0x240000\n"
"v_mfma_f32_16x16x32_bf16 v[156:159], v[84:87], v[252:255], v[156:159]\n"
"s_mov_b32 s23, 0x20000\n"
"v_mfma_f32_16x16x32_bf16 v[156:159], v[80:83], v[236:239], v[156:159]\n"
"s_waitcnt lgkmcnt(8)\n"
"v_mfma_f32_16x16x32_bf16 v[112:115], v[88:91], a[0:3], 0\n"
"s_and_b32 s41, s41, 0xffff\n"
"ds_read_b64_tr_b16 v[80:81], v39 offset:12288\n"
"ds_read_b64_tr_b16 v[82:83], v39 offset:13312\n"
"v_mfma_f32_16x16x32_bf16 v[112:115], v[92:95], a[16:19], v[112:115]\n"
"s_mov_b32 s42, 0x240000\n"
"ds_read_b64_tr_b16 v[84:85], v39 offset:14336\n"
"ds_read_b64_tr_b16 v[86:87], v39 offset:15360\n"
"v_mfma_f32_16x16x32_bf16 v[116:119], v[92:95], a[20:23], 0\n"
"s_mov_b32 s43, 0x20000\n"
"v_mfma_f32_16x16x32_bf16 v[116:119], v[88:91], a[4:7], v[116:119]\n"
"s_lshl2_add_u32 s34, s6, s52\n"
"v_mul_f32 v44, v144, v144\n"
"v_mul_f32 v45, v145, v145\n"
"v_mfma_f32_16x16x32_bf16 v[120:123], v[88:91], a[8:11], 0\n"
"s_lshl_b32 s34, s34, 15\n"
"v_mul_f32 v46, v146, v146\n"
"v_mul_f32 v47, v147, v147\n"
"v_mfma_f32_16x16x32_bf16 v[120:123], v[92:95], a[24:27], v[120:123]\n"
"s_add_u32 s34, s34, 0x3400000\n"
"v_fma_f32 v44, v44, s63, v3\n"
"v_fma_f32 v45, v45, s63, v3\n"
"v_mfma_f32_16x16x32_bf16 v[124:127], v[92:95], a[28:31], 0\n"
"s_add_u32 s16, s8, s34\n"
"s_addc_u32 s17, s9, 0\n"
"v_fma_f32 v46, v46, s63, v3\n"
"v_mfma_f32_16x16x32_bf16 v[124:127], v[88:91], a[12:15], v[124:127]\n"
"s_waitcnt lgkmcnt(8)\n"
"v_fma_f32 v47, v47, s63, v3\n"
"v_mul_f32 v44, v44, v144\n"
"v_mfma_f32_16x16x32_bf16 v[112:115], v[64:67], a[32:35], v[112:115]\n"
"s_mov_b32 s19, 0x20000\n"
"ds_read_b64_tr_b16 v[88:89], v39 offset:16384\n"
"ds_read_b64_tr_b16 v[90:91], v39 offset:17408\n"
"v_mfma_f32_16x16x32_bf16 v[112:115], v[68:71], a[48:51], v[112:115]\n"
"s_bfe_u32 s34, s6, 0x20001\n"
"ds_read_b64_tr_b16 v[92:93], v39 offset:18432\n"
"ds_read_b64_tr_b16 v[94:95], v39 offset:19456\n"
"v_mfma_f32_16x16x32_bf16 v[116:119], v[68:71], a[52:55], v[116:119]\n"
"s_mov_b32 s18, 12288\n"
"v_mul_f32 v45, v45, v145\n"
"v_mul_f32 v46, v46, v146\n"
"v_mfma_f32_16x16x32_bf16 v[116:119], v[64:67], a[36:39], v[116:119]\n"
"s_cmp_eq_u32 s34, s52\n"
"s_cmov_b32 s18, 20480\n"
"v_mul_f32 v47, v47, v147\n"
"v_mfma_f32_16x16x32_bf16 v[120:123], v[64:67], a[40:43], v[120:123]\n"
"v_exp_f32 v44, v44\n"
"v_mfma_f32_16x16x32_bf16 v[120:123], v[68:71], a[56:59], v[120:123]\n"
"v_exp_f32 v45, v45\n"
"v_mfma_f32_16x16x32_bf16 v[124:127], v[68:71], a[60:63], v[124:127]\n"
"v_exp_f32 v46, v46\n"
"v_mfma_f32_16x16x32_bf16 v[124:127], v[64:67], a[44:47], v[124:127]\n"
"s_waitcnt lgkmcnt(8)\n"
"v_exp_f32 v47, v47\n"
"v_mfma_f32_16x16x32_bf16 v[112:115], v[72:75], a[64:67], v[112:115]\n"
"ds_read_b64_tr_b16 v[64:65], v39 offset:20480\n"
"ds_read_b64_tr_b16 v[66:67], v39 offset:21504\n"
"v_mfma_f32_16x16x32_bf16 v[112:115], v[76:79], a[80:83], v[112:115]\n"
"ds_read_b64_tr_b16 v[68:69], v39 offset:22528\n"
"ds_read_b64_tr_b16 v[70:71], v39 offset:23552\n"
"v_mfma_f32_16x16x32_bf16 v[116:119], v[76:79], a[84:87], v[116:119]\n"
"v_fma_f32 v44, v44, s33, s33\n"
"v_fma_f32 v45, v45, s33, s33\n"
"v_mfma_f32_16x16x32_bf16 v[116:119], v[72:75], a[68:71], v[116:119]\n"
"v_fma_f32 v46, v46, s33, s33\n"
"v_fma_f32 v47, v47, s33, s33\n"
"v_mfma_f32_16x16x32_bf16 v[120:123], v[72:75], a[72:75], v[120:123]\n"
"v_rcp_f32 v44, v44\n"
"v_mfma_f32_16x16x32_bf16 v[120:123], v[76:79], a[88:91], v[120:123]\n"
"v_rcp_f32 v45, v45\n"
"v_mfma_f32_16x16x32_bf16 v[124:127], v[76:79], a[92:95], v[124:127]\n"
"v_rcp_f32 v46, v46\n"
"v_mfma_f32_16x16x32_bf16 v[124:127], v[72:75], a[76:79], v[124:127]\n"
"s_waitcnt lgkmcnt(8)\n"
"v_rcp_f32 v47, v47\n"
"v_mfma_f32_16x16x32_bf16 v[112:115], v[80:83], a[96:99], v[112:115]\n"
"ds_read_b64_tr_b16 v[72:73], v39 offset:24576\n"
"ds_read_b64_tr_b16 v[74:75], v39 offset:25600\n"
"v_mfma_f32_16x16x32_bf16 v[112:115], v[84:87], a[112:115], v[112:115]\n"
"ds_read_b64_tr_b16 v[76:77], v39 offset:26624\n"
"ds_read_b64_tr_b16 v[78:79], v39 offset:27648\n"
"v_mfma_f32_16x16x32_bf16 v[116:119], v[84:87], a[116:119], v[116:119]\n"
"v_mul_f32 v96, v144, v44\n"
"v_mul_f32 v97, v145, v45\n"
"v_mfma_f32_16x16x32_bf16 v[116:119], v[80:83], a[100:103], v[116:119]\n"
"v_mul_f32 v98, v146, v46\n"
"v_mul_f32 v99, v147, v47\n"
"v_mfma_f32_16x16x32_bf16 v[120:123], v[80:83], a[104:107], v[120:123]\n"
"v_mul_f32 v44, v148, v148\n"
"v_mul_f32 v45, v149, v149\n"
"v_mfma_f32_16x16x32_bf16 v[120:123], v[84:87], a[120:123], v[120:123]\n"
"v_mul_f32 v46, v150, v150\n"
"v_mul_f32 v47, v151, v151\n"
"v_mfma_f32_16x16x32_bf16 v[124:127], v[84:87], a[124:127], v[124:127]\n"
"v_fma_f32 v44, v44, s63, v3\n"
"v_fma_f32 v45, v45, s63, v3\n"
"v_mfma_f32_16x16x32_bf16 v[124:127], v[80:83], a[108:111], v[124:127]\n"
"s_waitcnt lgkmcnt(8)\n"
"v_fma_f32 v46, v46, s63, v3\n"
"v_fma_f32 v47, v47, s63, v3\n"
"v_mfma_f32_16x16x32_bf16 v[112:115], v[88:91], a[128:131], v[112:115]\n"
"ds_read_b64_tr_b16 v[80:81], v39 offset:28672\n"
"ds_read_b64_tr_b16 v[82:83], v39 offset:29696\n"
"v_mfma_f32_16x16x32_bf16 v[112:115], v[92:95], a[144:147], v[112:115]\n"
"ds_read_b64_tr_b16 v[84:85], v39 offset:30720\n"
"ds_read_b64_tr_b16 v[86:87], v39 offset:31744\n"
"v_mfma_f32_16x16x32_bf16 v[116:119], v[92:95], a[148:151], v[116:119]\n"
"v_mul_f32 v44, v44, v148\n"
"v_mul_f32 v45, v45, v149\n"
"v_mfma_f32_16x16x32_bf16 v[116:119], v[88:91], a[132:135], v[116:119]\n"
"v_mul_f32 v46, v46, v150\n"
"v_mul_f32 v47, v47, v151\n"
"v_mfma_f32_16x16x32_bf16 v[120:123], v[88:91], a[136:139], v[120:123]\n"
"v_exp_f32 v44, v44\n"
"v_mfma_f32_16x16x32_bf16 v[120:123], v[92:95], a[152:155], v[120:123]\n"
"v_exp_f32 v45, v45\n"
"v_mfma_f32_16x16x32_bf16 v[124:127], v[92:95], a[156:159], v[124:127]\n"
"v_exp_f32 v46, v46\n"
"v_mfma_f32_16x16x32_bf16 v[124:127], v[88:91], a[140:143], v[124:127]\n"
"s_waitcnt lgkmcnt(8)\n"
"v_exp_f32 v47, v47\n"
"v_mfma_f32_16x16x32_bf16 v[112:115], v[64:67], a[160:163], v[112:115]\n"
"ds_read_b64_tr_b16 v[88:89], v39 offset:32768\n"
"ds_read_b64_tr_b16 v[90:91], v39 offset:33792\n"
"v_mfma_f32_16x16x32_bf16 v[112:115], v[68:71], a[176:179], v[112:115]\n"
"ds_read_b64_tr_b16 v[92:93], v39 offset:34816\n"
"ds_read_b64_tr_b16 v[94:95], v39 offset:35840\n"
"v_mfma_f32_16x16x32_bf16 v[116:119], v[68:71], a[180:183], v[116:119]\n"
"v_fma_f32 v44, v44, s33, s33\n"
"v_fma_f32 v45, v45, s33, s33\n"
"v_mfma_f32_16x16x32_bf16 v[116:119], v[64:67], a[164:167], v[116:119]\n"
"v_fma_f32 v46, v46, s33, s33\n"
"v_fma_f32 v47, v47, s33, s33\n"
"v_mfma_f32_16x16x32_bf16 v[120:123], v[64:67], a[168:171], v[120:123]\n"
"v_rcp_f32 v44, v44\n"
"v_mfma_f32_16x16x32_bf16 v[120:123], v[68:71], a[184:187], v[120:123]\n"
"v_rcp_f32 v45, v45\n"
"v_mfma_f32_16x16x32_bf16 v[124:127], v[68:71], a[188:191], v[124:127]\n"
"v_rcp_f32 v46, v46\n"
"v_mfma_f32_16x16x32_bf16 v[124:127], v[64:67], a[172:175], v[124:127]\n"
"s_waitcnt lgkmcnt(8)\n"
"v_rcp_f32 v47, v47\n"
"v_mfma_f32_16x16x32_bf16 v[112:115], v[72:75], a[192:195], v[112:115]\n"
"ds_read_b128 a[0:3], v26 offset:0\n"
"v_mul_f32 v100, v148, v44\n"
"v_mfma_f32_16x16x32_bf16 v[112:115], v[76:79], a[208:211], v[112:115]\n"
"ds_read_b128 a[4:7], v26 offset:64\n"
"v_mul_f32 v101, v149, v45\n"
"v_mfma_f32_16x16x32_bf16 v[116:119], v[76:79], a[212:215], v[116:119]\n"
"ds_read_b128 a[8:11], v26 offset:128\n"
"v_mul_f32 v102, v150, v46\n"
"v_mfma_f32_16x16x32_bf16 v[116:119], v[72:75], a[196:199], v[116:119]\n"
"ds_read_b128 a[12:15], v26 offset:192\n"
"v_mul_f32 v103, v151, v47\n"
"v_mfma_f32_16x16x32_bf16 v[120:123], v[72:75], a[200:203], v[120:123]\n"
"ds_read_b128 a[32:35], v26 offset:512\n"
"v_mul_f32 v44, v152, v152\n"
"v_mfma_f32_16x16x32_bf16 v[120:123], v[76:79], a[216:219], v[120:123]\n"
"ds_read_b128 a[36:39], v26 offset:576\n"
"v_mul_f32 v45, v153, v153\n"
"v_mfma_f32_16x16x32_bf16 v[124:127], v[76:79], a[220:223], v[124:127]\n"
"ds_read_b128 a[40:43], v26 offset:640\n"
"v_mul_f32 v46, v154, v154\n"
"v_mfma_f32_16x16x32_bf16 v[124:127], v[72:75], a[204:207], v[124:127]\n"
"s_waitcnt lgkmcnt(11)\n"
"ds_read_b128 a[44:47], v26 offset:704\n"
"v_mul_f32 v47, v155, v155\n"
"v_mfma_f32_16x16x32_bf16 v[112:115], v[80:83], a[224:227], v[112:115]\n"
"ds_read_b128 a[16:19], v26 offset:256\n"
"v_fma_f32 v44, v44, s63, v3\n"
"v_mfma_f32_16x16x32_bf16 v[112:115], v[84:87], a[240:243], v[112:115]\n"
"ds_read_b128 a[20:23], v26 offset:320\n"
"v_fma_f32 v45, v45, s63, v3\n"
"v_mfma_f32_16x16x32_bf16 v[116:119], v[84:87], a[244:247], v[116:119]\n"
"ds_read_b128 a[24:27], v26 offset:384\n"
"v_fma_f32 v46, v46, s63, v3\n"
"v_mfma_f32_16x16x32_bf16 v[116:119], v[80:83], a[228:231], v[116:119]\n"
"ds_read_b128 a[28:31], v26 offset:448\n"
"v_fma_f32 v47, v47, s63, v3\n"
"v_mfma_f32_16x16x32_bf16 v[120:123], v[80:83], a[232:235], v[120:123]\n"
"ds_read_b128 a[48:51], v26 offset:768\n"
"v_mul_f32 v44, v44, v152\n"
"v_mfma_f32_16x16x32_bf16 v[120:123], v[84:87], a[248:251], v[120:123]\n"
"ds_read_b128 a[52:55], v26 offset:832\n"
"v_mul_f32 v45, v45, v153\n"
"v_mfma_f32_16x16x32_bf16 v[124:127], v[84:87], a[252:255], v[124:127]\n"
"ds_read_b128 a[56:59], v26 offset:896\n"
"v_mul_f32 v46, v46, v154\n"
"v_mfma_f32_16x16x32_bf16 v[124:127], v[80:83], a[236:239], v[124:127]\n"
"s_waitcnt lgkmcnt(15)\n"
"ds_read_b128 a[60:63], v26 offset:960\n"
"v_mul_f32 v47, v47, v155\n"
"v_mfma_f32_16x16x32_bf16 v[112:115], v[88:91], v[224:227], v[112:115]\n"
"v_exp_f32 v44, v44\n"
"v_mfma_f32_16x16x32_bf16 v[112:115], v[92:95], v[240:243], v[112:115]\n"
"v_exp_f32 v45, v45\n"
"v_mfma_f32_16x16x32_bf16 v[116:119], v[92:95], v[244:247], v[116:119]\n"
"v_exp_f32 v46, v46\n"
"v_mfma_f32_16x16x32_bf16 v[116:119], v[88:91], v[228:231], v[116:119]\n"
"v_exp_f32 v47, v47\n"
"v_mfma_f32_16x16x32_bf16 v[120:123], v[88:91], v[232:235], v[120:123]\n"
"v_fma_f32 v44, v44, s33, s33\n"
"v_fma_f32 v45, v45, s33, s33\n"
"v_mfma_f32_16x16x32_bf16 v[120:123], v[92:95], v[248:251], v[120:123]\n"
"v_fma_f32 v46, v46, s33, s33\n"
"v_fma_f32 v47, v47, s33, s33\n"
"v_mfma_f32_16x16x32_bf16 v[124:127], v[92:95], v[252:255], v[124:127]\n"
"v_rcp_f32 v44, v44\n"
"v_mfma_f32_16x16x32_bf16 v[124:127], v[88:91], v[236:239], v[124:127]\n"
"s_waitcnt lgkmcnt(12)\n"
"v_rcp_f32 v45, v45\n"
"L_a0:\n"
"v_mfma_f32_16x16x32_bf16 v[128:131], a[0:3], v[160:163], v[8:11]\n"
"v_mov_b32 v80, 0\n"
"v_mov_b32 v81, 0\n"
"v_mfma_f32_16x16x32_bf16 v[128:131], a[4:7], v[176:179], v[128:131]\n"
"v_mov_b32 v82, 0\n"
"v_mov_b32 v83, 0\n"
"v_mfma_f32_16x16x32_bf16 v[128:131], a[8:11], v[192:195], v[128:131]\n"
"v_mfma_f32_16x16x32_bf16 v[128:131], a[12:15], v[208:211], v[128:131]\n"
"v_mul_f32 v112, v112, v96\n"
"v_mul_f32 v113, v113, v97\n"
"v_mfma_f32_16x16x32_bf16 v[132:135], a[12:15], v[212:215], v[12:15]\n"
"v_mul_f32 v114, v114, v98\n"
"v_mul_f32 v115, v115, v99\n"
"v_mfma_f32_16x16x32_bf16 v[132:135], a[8:11], v[196:199], v[132:135]\n"
"v_cvt_pk_bf16_f32 v50, v112, v113\n"
"v_cvt_pk_bf16_f32 v51, v114, v115\n"
"v_mfma_f32_16x16x32_bf16 v[132:135], a[4:7], v[180:183], v[132:135]\n"
"buffer_store_dwordx4 v[48:51], v0, s[36:39], s55 offen sc1\n"
"v_mul_f32 v116, v116, v100\n"
"v_mfma_f32_16x16x32_bf16 v[132:135], a[0:3], v[164:167], v[132:135]\n"
"v_mul_f32 v117, v117, v101\n"
"v_mul_f32 v118, v118, v102\n"
"v_mfma_f32_16x16x32_bf16 v[136:139], a[0:3], v[168:171], v[16:19]\n"
"v_mul_f32 v119, v119, v103\n"
"v_cvt_pk_bf16_f32 v54, v116, v117\n"
"v_mfma_f32_16x16x32_bf16 v[136:139], a[4:7], v[184:187], v[136:139]\n"
"v_cvt_pk_bf16_f32 v55, v118, v119\n"
"v_mfma_f32_16x16x32_bf16 v[136:139], a[8:11], v[200:203], v[136:139]\n"
"buffer_store_dwordx4 v[52:55], v0, s[36:39], s56 offen sc1\n"
"v_mfma_f32_16x16x32_bf16 v[136:139], a[12:15], v[216:219], v[136:139]\n"
"v_rcp_f32 v46, v46\n"
"v_mfma_f32_16x16x32_bf16 v[140:143], a[12:15], v[220:223], v[20:23]\n"
"v_rcp_f32 v47, v47\n"
"v_mfma_f32_16x16x32_bf16 v[140:143], a[8:11], v[204:207], v[140:143]\n"
"s_waitcnt vmcnt(3)\n"
"v_mul_f32 v104, v152, v44\n"
"v_mul_f32 v105, v153, v45\n"
"v_mfma_f32_16x16x32_bf16 v[140:143], a[4:7], v[188:191], v[140:143]\n"
"s_barrier\n"
"v_mul_f32 v106, v154, v46\n"
"v_mul_f32 v107, v155, v47\n"
"v_mfma_f32_16x16x32_bf16 v[140:143], a[0:3], v[172:175], v[140:143]\n"
"s_waitcnt lgkmcnt(8)\n"
"v_mul_f32 v120, v120, v104\n"
"v_mul_f32 v121, v121, v105\n"
"v_mfma_f32_16x16x32_bf16 v[224:227], a[32:35], v[160:163], v[8:11]\n"
"ds_read_b128 a[0:3], v27 offset:0\n"
"v_mul_f32 v122, v122, v106\n"
"v_mfma_f32_16x16x32_bf16 v[224:227], a[36:39], v[176:179], v[224:227]\n"
"ds_read_b128 a[4:7], v27 offset:64\n"
"v_mul_f32 v123, v123, v107\n"
"v_mfma_f32_16x16x32_bf16 v[224:227], a[40:43], v[192:195], v[224:227]\n"
"ds_read_b128 a[8:11], v27 offset:128\n"
"v_cvt_pk_bf16_f32 v58, v120, v121\n"
"v_mfma_f32_16x16x32_bf16 v[224:227], a[44:47], v[208:211], v[224:227]\n"
"ds_read_b128 a[12:15], v27 offset:192\n"
"v_cvt_pk_bf16_f32 v59, v122, v123\n"
"v_mfma_f32_16x16x32_bf16 v[228:231], a[44:47], v[212:215], v[12:15]\n"
"buffer_store_dwordx4 v[56:59], v0, s[36:39], s57 offen sc1\n"
"v_mul_f32 v44, v156, v156\n"
"v_mfma_f32_16x16x32_bf16 v[228:231], a[40:43], v[196:199], v[228:231]\n"
"s_mov_b32 s62, 0\n"
"v_mul_f32 v45, v157, v157\n"
"v_mul_f32 v46, v158, v158\n"
"v_mfma_f32_16x16x32_bf16 v[228:231], a[36:39], v[180:183], v[228:231]\n"
"s_mov_b32 s61, -73728\n"
"v_mul_f32 v47, v159, v159\n"
"v_fma_f32 v44, v44, s63, v3\n"
"v_mfma_f32_16x16x32_bf16 v[228:231], a[32:35], v[164:167], v[228:231]\n"
"s_mov_b32 s60, s92\n"
"v_fma_f32 v45, v45, s63, v3\n"
"v_fma_f32 v46, v46, s63, v3\n"
"v_mfma_f32_16x16x32_bf16 v[232:235], a[32:35], v[168:171], v[16:19]\n"
"v_fma_f32 v47, v47, s63, v3\n"
"v_mul_f32 v44, v44, v156\n"
"v_mfma_f32_16x16x32_bf16 v[232:235], a[36:39], v[184:187], v[232:235]\n"
"v_mul_f32 v45, v45, v157\n"
"v_mul_f32 v46, v46, v158\n"
"v_mfma_f32_16x16x32_bf16 v[232:235], a[40:43], v[200:203], v[232:235]\n"
"v_mul_f32 v47, v47, v159\n"
"v_mfma_f32_16x16x32_bf16 v[232:235], a[44:47], v[216:219], v[232:235]\n"
"v_exp_f32 v44, v44\n"
"v_mfma_f32_16x16x32_bf16 v[236:239], a[44:47], v[220:223], v[20:23]\n"
"v_exp_f32 v45, v45\n"
"v_mfma_f32_16x16x32_bf16 v[236:239], a[40:43], v[204:207], v[236:239]\n"
"v_exp_f32 v46, v46\n"
"v_mfma_f32_16x16x32_bf16 v[236:239], a[36:39], v[188:191], v[236:239]\n"
"v_exp_f32 v47, v47\n"
"v_mfma_f32_16x16x32_bf16 v[236:239], a[32:35], v[172:175], v[236:239]\n"
"s_waitcnt lgkmcnt(8)\n"
"v_fma_f32 v44, v44, s33, s33\n"
"v_fma_f32 v45, v45, s33, s33\n"
"v_mfma_f32_16x16x32_bf16 v[144:147], a[16:19], v[160:163], v[8:11]\n"
"ds_read_b128 a[32:35], v27 offset:512\n"
"v_fma_f32 v46, v46, s33, s33\n"
"v_mfma_f32_16x16x32_bf16 v[144:147], a[20:23], v[176:179], v[144:147]\n"
"ds_read_b128 a[36:39], v27 offset:576\n"
"v_fma_f32 v47, v47, s33, s33\n"
"v_mfma_f32_16x16x32_bf16 v[144:147], a[24:27], v[192:195], v[144:147]\n"
"ds_read_b128 a[40:43], v27 offset:640\n"
"v_mfma_f32_16x16x32_bf16 v[144:147], a[28:31], v[208:211], v[144:147]\n"
"ds_read_b128 a[44:47], v27 offset:704\n"
"v_mfma_f32_16x16x32_bf16 v[148:151], a[28:31], v[212:215], v[12:15]\n"
"v_rcp_f32 v44, v44\n"
"v_mfma_f32_16x16x32_bf16 v[148:151], a[24:27], v[196:199], v[148:151]\n"
"v_rcp_f32 v45, v45\n"
"v_mfma_f32_16x16x32_bf16 v[148:151], a[20:23], v[180:183], v[148:151]\n"
"v_rcp_f32 v46, v46\n"
"v_mfma_f32_16x16x32_bf16 v[148:151], a[16:19], v[164:167], v[148:151]\n"
"v_rcp_f32 v47, v47\n"
"v_mfma_f32_16x16x32_bf16 v[152:155], a[16:19], v[168:171], v[16:19]\n"
"v_mul_f32 v108, v156, v44\n"
"v_mul_f32 v109, v157, v45\n"
"v_mfma_f32_16x16x32_bf16 v[152:155], a[20:23], v[184:187], v[152:155]\n"
"v_mul_f32 v110, v158, v46\n"
"v_mul_f32 v111, v159, v47\n"
"v_mfma_f32_16x16x32_bf16 v[152:155], a[24:27], v[200:203], v[152:155]\n"
"v_mul_f32 v124, v124, v108\n"
"v_mul_f32 v125, v125, v109\n"
"v_mfma_f32_16x16x32_bf16 v[152:155], a[28:31], v[216:219], v[152:155]\n"
"v_mul_f32 v126, v126, v110\n"
"v_mul_f32 v127, v127, v111\n"
"v_mfma_f32_16x16x32_bf16 v[156:159], a[28:31], v[220:223], v[20:23]\n"
"v_cvt_pk_bf16_f32 v62, v124, v125\n"
"v_cvt_pk_bf16_f32 v63, v126, v127\n"
"v_mfma_f32_16x16x32_bf16 v[156:159], a[24:27], v[204:207], v[156:159]\n"
"buffer_store_dwordx4 v[60:63], v0, s[36:39], s58 offen sc1\n"
"v_mfma_f32_16x16x32_bf16 v[156:159], a[20:23], v[188:191], v[156:159]\n"
"v_exp_f32 v128, v128\n"
"v_mfma_f32_16x16x32_bf16 v[156:159], a[16:19], v[172:175], v[156:159]\n"
"s_waitcnt lgkmcnt(8)\n"
"v_exp_f32 v129, v129\n"
"v_mfma_f32_16x16x32_bf16 v[240:243], a[48:51], v[160:163], v[8:11]\n"
"ds_read_b128 a[16:19], v27 offset:256\n"
"v_mfma_f32_16x16x32_bf16 v[240:243], a[52:55], v[176:179], v[240:243]\n"
"ds_read_b128 a[20:23], v27 offset:320\n"
"v_mfma_f32_16x16x32_bf16 v[240:243], a[56:59], v[192:195], v[240:243]\n"
"ds_read_b128 a[24:27], v27 offset:384\n"
"v_mfma_f32_16x16x32_bf16 v[240:243], a[60:63], v[208:211], v[240:243]\n"
"ds_read_b128 a[28:31], v27 offset:448\n"
"v_mfma_f32_16x16x32_bf16 v[244:247], a[60:63], v[212:215], v[12:15]\n"
"v_exp_f32 v130, v130\n"
"v_mfma_f32_16x16x32_bf16 v[244:247], a[56:59], v[196:199], v[244:247]\n"
"v_cvt_pk_bf16_f32 v64, v128, v129\n"
"v_mfma_f32_16x16x32_bf16 v[244:247], a[52:55], v[180:183], v[244:247]\n"
"v_exp_f32 v131, v131\n"
"v_mfma_f32_16x16x32_bf16 v[244:247], a[48:51], v[164:167], v[244:247]\n"
"v_exp_f32 v132, v132\n"
"v_mfma_f32_16x16x32_bf16 v[248:251], a[48:51], v[168:171], v[16:19]\n"
"v_cvt_pk_bf16_f32 v65, v130, v131\n"
"v_mfma_f32_16x16x32_bf16 v[248:251], a[52:55], v[184:187], v[248:251]\n"
"v_exp_f32 v133, v133\n"
"v_mfma_f32_16x16x32_bf16 v[248:251], a[56:59], v[200:203], v[248:251]\n"
"v_exp_f32 v134, v134\n"
"v_mfma_f32_16x16x32_bf16 v[248:251], a[60:63], v[216:219], v[248:251]\n"
"v_cvt_pk_bf16_f32 v68, v132, v133\n"
"v_mfma_f32_16x16x32_bf16 v[252:255], a[60:63], v[220:223], v[20:23]\n"
"v_exp_f32 v135, v135\n"
"v_mfma_f32_16x16x32_bf16 v[252:255], a[56:59], v[204:207], v[252:255]\n"
"v_exp_f32 v136, v136\n"
"v_mfma_f32_16x16x32_bf16 v[252:255], a[52:55], v[188:191], v[252:255]\n"
"v_cvt_pk_bf16_f32 v69, v134, v135\n"
"v_mfma_f32_16x16x32_bf16 v[252:255], a[48:51], v[172:175], v[252:255]\n"
"v_exp_f32 v137, v137\n"
"v_mfma_f32_32x32x16_bf16 a[128:143], v[80:83], v[80:83], 0\n"
"ds_read_b128 a[48:51], v27 offset:768\n"
"v_accvgpr_write_b32 a228, -1\n"
"v_accvgpr_write_b32 a229, -1\n"
"v_accvgpr_write_b32 a230, -1\n"
"v_accvgpr_write_b32 a231, -1\n"
"v_mfma_f32_32x32x16_bf16 a[144:159], v[80:83], v[80:83], 0\n"
"ds_read_b128 a[52:55], v27 offset:832\n"
"v_exp_f32 v138, v138\n"
"v_cvt_pk_bf16_f32 v72, v136, v137\n"
"v_exp_f32 v139, v139\n"
"v_mfma_f32_32x32x16_bf16 a[160:175], v[80:83], v[80:83], 0\n"
"ds_read_b128 a[56:59], v27 offset:896\n"
"v_exp_f32 v140, v140\n"
"v_cvt_pk_bf16_f32 v73, v138, v139\n"
"v_exp_f32 v141, v141\n"
"v_mfma_f32_32x32x16_bf16 a[176:191], v[80:83], v[80:83], 0\n"
"ds_read_b128 a[60:63], v27 offset:960\n"
"v_exp_f32 v142, v142\n"
"v_cvt_pk_bf16_f32 v76, v140, v141\n"
"v_exp_f32 v143, v143\n"
"v_mfma_f32_32x32x16_bf16 a[192:207], v[80:83], v[80:83], 0\n"
"v_exp_f32 v144, v144\n"
"v_cvt_pk_bf16_f32 v77, v142, v143\n"
"v_exp_f32 v145, v145\n"
"v_mfma_f32_32x32x16_bf16 a[208:223], v[80:83], v[80:83], 0\n"
"v_exp_f32 v146, v146\n"
"v_cvt_pk_bf16_f32 v66, v144, v145\n"
"v_exp_f32 v147, v147\n"
"v_mfma_f32_32x32x16_bf16 v[96:111], v[80:83], v[80:83], 0\n"
"v_exp_f32 v148, v148\n"
"v_cvt_pk_bf16_f32 v67, v146, v147\n"
"v_exp_f32 v149, v149\n"
"v_mfma_f32_32x32x16_bf16 v[112:127], v[80:83], v[80:83], 0\n"
"v_exp_f32 v150, v150\n"
"v_cvt_pk_bf16_f32 v70, v148, v149\n"
"v_exp_f32 v151, v151\n"
"v_mfma_f32_32x32x16_bf16 v[32:47], v[80:83], v[80:83], 0\n"
"v_exp_f32 v152, v152\n"
"v_cvt_pk_bf16_f32 v71, v150, v151\n"
"v_exp_f32 v153, v153\n"
"v_exp_f32 v154, v154\n"
"v_cvt_pk_bf16_f32 v74, v152, v153\n"
"v_exp_f32 v155, v155\n"
"v_exp_f32 v156, v156\n"
"v_cvt_pk_bf16_f32 v75, v154, v155\n"
"v_exp_f32 v157, v157\n"
"v_exp_f32 v158, v158\n"
"v_cvt_pk_bf16_f32 v78, v156, v157\n"
"v_exp_f32 v159, v159\n"
"v_exp_f32 v224, v224\n"
"v_cvt_pk_bf16_f32 v79, v158, v159\n"
"v_exp_f32 v225, v225\n"
"v_exp_f32 v226, v226\n"
"v_exp_f32 v227, v227\n"
"v_exp_f32 v228, v228\n"
"v_exp_f32 v229, v229\n"
"v_exp_f32 v230, v230\n"
"v_exp_f32 v231, v231\n"
"s_waitcnt lgkmcnt(0)\n"
"L_att:\n"
"v_mfma_f32_16x16x32_bf16 v[128:131], a[0:3], v[160:163], v[8:11]\n"
"s_waitcnt vmcnt(3)\n"
"s_bitcmp1_b32 0x6800, s62\n"
"v_exp_f32 v232, v232\n"
"v_mfma_f32_16x16x32_bf16 v[128:131], a[4:7], v[176:179], v[128:131]\n"
"s_cbranch_scc1 L_hook\n"
"s_barrier\n"
"L_hret:\n"
"v_exp_f32 v233, v233\n"
"v_mfma_f32_16x16x32_bf16 v[128:131], a[8:11], v[192:195], v[128:131]\n"
"s_add_u32 s81, s81, 16384\n"
"ds_read_b64_tr_b16 a[64:65], v25 offset:0\n"
"ds_read_b64_tr_b16 a[66:67], v25 offset:256\n"
"v_mfma_f32_16x16x32_bf16 v[128:131], a[12:15], v[208:211], v[128:131]\n"
"s_mov_b32 m0, s60\n"
"ds_read_b64_tr_b16 a[68:69], v25 offset:512\n"
"v_cvt_pk_bf16_f32 v48, v224, v225\n"
"v_mfma_f32_16x16x32_bf16 v[132:135], a[12:15], v[212:215], v[12:15]\n"
"v_exp_f32 v234, v234\n"
"v_mfma_f32_16x16x32_bf16 v[132:135], a[8:11], v[196:199], v[132:135]\n"
"ds_read_b64_tr_b16 a[224:225], v29 offset:0\n"
"ds_read_b64_tr_b16 a[226:227], v29 offset:512\n"
"v_mfma_f32_16x16x32_bf16 v[132:135], a[4:7], v[180:183], v[132:135]\n"
"ds_read_b64_tr_b16 a[70:71], v25 offset:768\n"
"v_cvt_pk_bf16_f32 v49, v226, v227\n"
"v_mfma_f32_16x16x32_bf16 v[132:135], a[0:3], v[164:167], v[132:135]\n"
"v_exp_f32 v235, v235\n"
"v_mfma_f32_16x16x32_bf16 v[136:139], a[0:3], v[168:171], v[16:19]\n"
"v_exp_f32 v236, v236\n"
"v_mfma_f32_16x16x32_bf16 v[136:139], a[4:7], v[184:187], v[136:139]\n"
"ds_read_b64_tr_b16 a[72:73], v25 offset:32\n"
"ds_read_b64_tr_b16 a[74:75], v25 offset:288\n"
"v_mfma_f32_16x16x32_bf16 v[136:139], a[8:11], v[200:203], v[136:139]\n"
"ds_read_b64_tr_b16 a[76:77], v25 offset:544\n"
"v_cvt_pk_bf16_f32 v52, v228, v229\n"
"v_mfma_f32_16x16x32_bf16 v[136:139], a[12:15], v[216:219], v[136:139]\n"
"v_exp_f32 v237, v237\n"
"v_mfma_f32_16x16x32_bf16 v[140:143], a[12:15], v[220:223], v[20:23]\n"
"ds_read_b64_tr_b16 a[78:79], v25 offset:800\n"
"v_cvt_pk_bf16_f32 v53, v230, v231\n"
"v_mfma_f32_16x16x32_bf16 v[140:143], a[8:11], v[204:207], v[140:143]\n"
"v_exp_f32 v238, v238\n"
"v_mfma_f32_16x16x32_bf16 v[140:143], a[4:7], v[188:191], v[140:143]\n"
"buffer_load_dwordx4 v0, s[28:31], s81 offen lds\n"
"v_mfma_f32_16x16x32_bf16 v[140:143], a[0:3], v[172:175], v[140:143]\n"
"s_add_u32 m0, m0, 32\n"
"v_exp_f32 v239, v239\n"
"v_mfma_f32_16x16x32_bf16 v[144:147], a[16:19], v[160:163], v[8:11]\n"
"buffer_load_dwordx4 v0, s[28:31], s81 offen offset:1024 lds\n"
"v_mfma_f32_16x16x32_bf16 v[144:147], a[20:23], v[176:179], v[144:147]\n"
"s_add_u32 m0, m0, 32\n"
"ds_read_b64_tr_b16 a[80:81], v25 offset:64\n"
"v_cvt_pk_bf16_f32 v56, v232, v233\n"
"v_mfma_f32_16x16x32_bf16 v[144:147], a[24:27], v[192:195], v[144:147]\n"
"v_exp_f32 v240, v240\n"
"v_mfma_f32_16x16x32_bf16 v[144:147], a[28:31], v[208:211], v[144:147]\n"
"ds_read_b64_tr_b16 a[82:83], v25 offset:320\n"
"v_cvt_pk_bf16_f32 v57, v234, v235\n"
"v_mfma_f32_16x16x32_bf16 v[148:151], a[28:31], v[212:215], v[12:15]\n"
"v_exp_f32 v241, v241\n"
"v_mfma_f32_16x16x32_bf16 v[148:151], a[24:27], v[196:199], v[148:151]\n"
"ds_read_b64_tr_b16 a[84:85], v25 offset:576\n"
"ds_read_b64_tr_b16 a[86:87], v25 offset:832\n"
"v_mfma_f32_16x16x32_bf16 v[148:151], a[20:23], v[180:183], v[148:151]\n"
"v_exp_f32 v242, v242\n"
"v_mfma_f32_16x16x32_bf16 v[148:151], a[16:19], v[164:167], v[148:151]\n"
"buffer_load_dwordx4 v0, s[28:31], s81 offen offset:2048 lds\n"
"v_mfma_f32_16x16x32_bf16 v[152:155], a[16:19], v[168:171], v[16:19]\n"
"s_add_u32 m0, m0, 32\n"
"ds_read_b64_tr_b16 a[88:89], v25 offset:96\n"
"v_cvt_pk_bf16_f32 v60, v236, v237\n"
"v_mfma_f32_16x16x32_bf16 v[152:155], a[20:23], v[184:187], v[152:155]\n"
"v_exp_f32 v243, v243\n"
"v_mfma_f32_16x16x32_bf16 v[152:155], a[24:27], v[200:203], v[152:155]\n"
"ds_read_b64_tr_b16 a[90:91], v25 offset:352\n"
"v_cvt_pk_bf16_f32 v61, v238, v239\n"
"v_mfma_f32_16x16x32_bf16 v[152:155], a[28:31], v[216:219], v[152:155]\n"
"v_exp_f32 v244, v244\n"
"v_mfma_f32_16x16x32_bf16 v[156:159], a[28:31], v[220:223], v[20:23]\n"
"v_mfma_f32_16x16x32_bf16 v[156:159], a[24:27], v[204:207], v[156:159]\n"
"v_exp_f32 v245, v245\n"
"v_mfma_f32_16x16x32_bf16 v[156:159], a[20:23], v[188:191], v[156:159]\n"
"ds_read_b64_tr_b16 a[92:93], v25 offset:608\n"
"v_cvt_pk_bf16_f32 v50, v240, v241\n"
"v_mfma_f32_16x16x32_bf16 v[156:159], a[16:19], v[172:175], v[156:159]\n"
"v_exp_f32 v246, v246\n"
"v_mfma_f32_16x16x32_bf16 v[224:227], a[32:35], v[160:163], v[8:11]\n"
"ds_read_b64_tr_b16 a[94:95], v25 offset:864\n"
"ds_read_b64_tr_b16 a[96:97], v25 offset:128\n"
"v_mfma_f32_16x16x32_bf16 v[224:227], a[36:39], v[176:179], v[224:227]\n"
"ds_read_b64_tr_b16 a[98:99], v25 offset:384\n"
"v_cvt_pk_bf16_f32 v51, v242, v243\n"
"v_mfma_f32_16x16x32_bf16 v[224:227], a[40:43], v[192:195], v[224:227]\n"
"v_exp_f32 v247, v247\n"
"v_mfma_f32_16x16x32_bf16 v[224:227], a[44:47], v[208:211], v[224:227]\n"
"buffer_load_dwordx4 v0, s[28:31], s81 offen offset:3072 lds\n"
"v_mfma_f32_16x16x32_bf16 v[228:231], a[44:47], v[212:215], v[12:15]\n"
"s_add_u32 s81, s81, 16384\n"
"v_exp_f32 v248, v248\n"
"v_mfma_f32_16x16x32_bf16 v[228:231], a[40:43], v[196:199], v[228:231]\n"
"s_add_u32 m0, m0, 16800\n"
"ds_read_b64_tr_b16 a[100:101], v25 offset:640\n"
"v_cvt_pk_bf16_f32 v54, v244, v245\n"
"v_mfma_f32_16x16x32_bf16 v[228:231], a[36:39], v[180:183], v[228:231]\n"
"v_exp_f32 v249, v249\n"
"v_mfma_f32_16x16x32_bf16 v[228:231], a[32:35], v[164:167], v[228:231]\n"
"buffer_load_dwordx4 v0, s[28:31], s81 offen lds\n"
"v_mfma_f32_16x16x32_bf16 v[232:235], a[32:35], v[168:171], v[16:19]\n"
"s_add_u32 m0, m0, 32\n"
"ds_read_b64_tr_b16 a[102:103], v25 offset:896\n"
"v_cvt_pk_bf16_f32 v55, v246, v247\n"
"v_mfma_f32_16x16x32_bf16 v[232:235], a[36:39], v[184:187], v[232:235]\n"
"v_exp_f32 v250, v250\n"
"v_mfma_f32_16x16x32_bf16 v[232:235], a[40:43], v[200:203], v[232:235]\n"
"v_exp_f32 v251, v251\n"
"v_mfma_f32_16x16x32_bf16 v[232:235], a[44:47], v[216:219], v[232:235]\n"
"ds_read_b64_tr_b16 a[104:105], v25 offset:160\n"
"ds_read_b64_tr_b16 a[106:107], v25 offset:416\n"
"v_mfma_f32_16x16x32_bf16 v[236:239], a[44:47], v[220:223], v[20:23]\n"
"ds_read_b64_tr_b16 a[108:109], v25 offset:672\n"
"v_cvt_pk_bf16_f32 v58, v248, v249\n"
"v_mfma_f32_16x16x32_bf16 v[236:239], a[40:43], v[204:207], v[236:239]\n"
"v_exp_f32 v252, v252\n"
"v_mfma_f32_16x16x32_bf16 v[236:239], a[36:39], v[188:191], v[236:239]\n"
"v_mfma_f32_16x16x32_bf16 v[236:239], a[32:35], v[172:175], v[236:239]\n"
"ds_read_b64_tr_b16 a[110:111], v25 offset:928\n"
"v_cvt_pk_bf16_f32 v59, v250, v251\n"
"v_mfma_f32_16x16x32_bf16 v[240:243], a[48:51], v[160:163], v[8:11]\n"
"v_exp_f32 v253, v253\n"
"v_mfma_f32_16x16x32_bf16 v[240:243], a[52:55], v[176:179], v[240:243]\n"
"v_exp_f32 v254, v254\n"
"v_mfma_f32_16x16x32_bf16 v[240:243], a[56:59], v[192:195], v[240:243]\n"
"ds_read_b64_tr_b16 a[112:113], v25 offset:192\n"
"ds_read_b64_tr_b16 a[114:115], v25 offset:448\n"
"v_mfma_f32_16x16x32_bf16 v[240:243], a[60:63], v[208:211], v[240:243]\n"
"ds_read_b64_tr_b16 a[116:117], v25 offset:704\n"
"v_cvt_pk_bf16_f32 v62, v252, v253\n"
"v_mfma_f32_16x16x32_bf16 v[244:247], a[60:63], v[212:215], v[12:15]\n"
"v_exp_f32 v255, v255\n"
"v_mfma_f32_16x16x32_bf16 v[244:247], a[56:59], v[196:199], v[244:247]\n"
"buffer_load_dwordx4 v0, s[28:31], s81 offen offset:1024 lds\n"
"v_mfma_f32_16x16x32_bf16 v[244:247], a[52:55], v[180:183], v[244:247]\n"
"s_add_u32 m0, m0, 32\n"
"ds_read_b64_tr_b16 a[118:119], v25 offset:960\n"
"v_cvt_pk_bf16_f32 v63, v254, v255\n"
"v_mfma_f32_16x16x32_bf16 v[244:247], a[48:51], v[164:167], v[244:247]\n"
"v_mfma_f32_16x16x32_bf16 v[248:251], a[48:51], v[168:171], v[16:19]\n"
"ds_read_b64_tr_b16 a[120:121], v25 offset:224\n"
"ds_read_b64_tr_b16 a[122:123], v25 offset:480\n"
"v_mfma_f32_16x16x32_bf16 v[248:251], a[52:55], v[184:187], v[248:251]\n"
"buffer_load_dwordx4 v0, s[28:31], s81 offen offset:2048 lds\n"
"v_mfma_f32_16x16x32_bf16 v[248:251], a[56:59], v[200:203], v[248:251]\n"
"s_add_u32 m0, m0, 32\n"
"v_mfma_f32_16x16x32_bf16 v[248:251], a[60:63], v[216:219], v[248:251]\n"
"ds_read_b64_tr_b16 a[124:125], v25 offset:736\n"
"ds_read_b64_tr_b16 a[126:127], v25 offset:992\n"
"v_mfma_f32_16x16x32_bf16 v[252:255], a[60:63], v[220:223], v[20:23]\n"
"v_mfma_f32_16x16x32_bf16 v[252:255], a[56:59], v[204:207], v[252:255]\n"
"v_mfma_f32_16x16x32_bf16 v[252:255], a[52:55], v[188:191], v[252:255]\n"
"v_mfma_f32_16x16x32_bf16 v[252:255], a[48:51], v[172:175], v[252:255]\n"
"v_smfmac_f32_16x16x64_bf16 a[128:131], v[4:7], v[64:71], v2\n"
"v_exp_f32 v128, v128\n"
"v_smfmac_f32_16x16x64_bf16 a[132:135], v[4:7], v[72:79], v2\n"
"v_exp_f32 v129, v129\n"
"v_smfmac_f32_16x16x64_bf16 a[128:131], v[4:7], v[48:55], v2\n"
"v_exp_f32 v130, v130\n"
"v_smfmac_f32_16x16x64_bf16 a[132:135], v[4:7], v[56:63], v2\n"
"s_waitcnt lgkmcnt(0)\n"
"buffer_load_dwordx4 v0, s[28:31], s81 offen offset:3072 lds\n"
"v_mfma_f32_16x16x32_bf16 v[96:99], a[64:67], v[64:67], v[96:99]\n"
"s_add_u32 s80, s80, 16384\n"
"ds_read_b128 a[0:3], v24 offset:33792\n"
"v_cvt_pk_bf16_f32 v80, v128, v129\n"
"v_mfma_f32_16x16x32_bf16 v[96:99], a[68:71], v[48:51], v[96:99]\n"
"s_add_u32 m0, m0, 16800\n"
"v_exp_f32 v131, v131\n"
"v_mfma_f32_16x16x32_bf16 v[100:103], a[68:71], v[52:55], v[100:103]\n"
"v_exp_f32 v132, v132\n"
"v_mfma_f32_16x16x32_bf16 v[100:103], a[64:67], v[68:71], v[100:103]\n"
"ds_read_b128 a[4:7], v24 offset:33856\n"
"v_cvt_pk_bf16_f32 v81, v130, v131\n"
"v_mfma_f32_16x16x32_bf16 v[104:107], a[64:67], v[72:75], v[104:107]\n"
"v_exp_f32 v133, v133\n"
"v_mfma_f32_16x16x32_bf16 v[104:107], a[68:71], v[56:59], v[104:107]\n"
"v_exp_f32 v134, v134\n"
"v_mfma_f32_16x16x32_bf16 v[108:111], a[68:71], v[60:63], v[108:111]\n"
"buffer_load_dwordx4 v0, s[24:27], s80 offen lds\n"
"v_mfma_f32_16x16x32_bf16 v[108:111], a[64:67], v[76:79], v[108:111]\n"
"s_add_u32 m0, m0, 32\n"
"ds_read_b128 a[8:11], v24 offset:33920\n"
"v_cvt_pk_bf16_f32 v84, v132, v133\n"
"v_mfma_f32_16x16x32_bf16 v[112:115], a[72:75], v[64:67], v[112:115]\n"
"v_exp_f32 v135, v135\n"
"v_mfma_f32_16x16x32_bf16 v[112:115], a[76:79], v[48:51], v[112:115]\n"
"v_exp_f32 v136, v136\n"
"v_mfma_f32_16x16x32_bf16 v[116:119], a[76:79], v[52:55], v[116:119]\n"
"ds_read_b128 a[12:15], v24 offset:33984\n"
"v_cvt_pk_bf16_f32 v85, v134, v135\n"
"v_mfma_f32_16x16x32_bf16 v[116:119], a[72:75], v[68:71], v[116:119]\n"
"v_exp_f32 v137, v137\n"
"v_mfma_f32_16x16x32_bf16 v[120:123], a[72:75], v[72:75], v[120:123]\n"
"v_exp_f32 v138, v138\n"
"v_mfma_f32_16x16x32_bf16 v[120:123], a[76:79], v[56:59], v[120:123]\n"
"ds_read_b128 a[16:19], v24 offset:34048\n"
"v_cvt_pk_bf16_f32 v88, v136, v137\n"
"v_mfma_f32_16x16x32_bf16 v[124:127], a[76:79], v[60:63], v[124:127]\n"
"v_exp_f32 v139, v139\n"
"v_mfma_f32_16x16x32_bf16 v[124:127], a[72:75], v[76:79], v[124:127]\n"
"buffer_load_dwordx4 v0, s[24:27], s80 offen offset:1024 lds\n"
"v_mfma_f32_16x16x32_bf16 v[32:35], a[80:83], v[64:67], v[32:35]\n"
"s_add_u32 m0, m0, 32\n"
"v_exp_f32 v140, v140\n"
"v_mfma_f32_16x16x32_bf16 v[32:35], a[84:87], v[48:51], v[32:35]\n"
"ds_read_b128 a[20:23], v24 offset:34112\n"
"v_cvt_pk_bf16_f32 v89, v138, v139\n"
"v_mfma_f32_16x16x32_bf16 v[36:39], a[84:87], v[52:55], v[36:39]\n"
"v_exp_f32 v141, v141\n"
"v_mfma_f32_16x16x32_bf16 v[36:39], a[80:83], v[68:71], v[36:39]\n"
"v_exp_f32 v142, v142\n"
"v_mfma_f32_16x16x32_bf16 v[40:43], a[80:83], v[72:75], v[40:43]\n"
"ds_read_b128 a[24:27], v24 offset:34176\n"
"v_cvt_pk_bf16_f32 v92, v140, v141\n"
"v_mfma_f32_16x16x32_bf16 v[40:43], a[84:87], v[56:59], v[40:43]\n"
"v_exp_f32 v143, v143\n"
"v_mfma_f32_16x16x32_bf16 v[44:47], a[84:87], v[60:63], v[44:47]\n"
"v_exp_f32 v144, v144\n"
"v_mfma_f32_16x16x32_bf16 v[44:47], a[80:83], v[76:79], v[44:47]\n"
"v_mfma_f32_16x16x32_bf16 a[144:147], a[88:91], v[64:67], a[144:147]\n"
"ds_read_b128 a[28:31], v24 offset:34240\n"
"v_cvt_pk_bf16_f32 v93, v142, v143\n"
"v_mfma_f32_16x16x32_bf16 a[144:147], a[92:95], v[48:51], a[144:147]\n"
"v_exp_f32 v145, v145\n"
"v_mfma_f32_16x16x32_bf16 a[148:151], a[92:95], v[52:55], a[148:151]\n"
"v_exp_f32 v146, v146\n"
"v_mfma_f32_16x16x32_bf16 a[148:151], a[88:91], v[68:71], a[148:151]\n"
"ds_read_b128 a[32:35], v24 offset:34304\n"
"v_cvt_pk_bf16_f32 v82, v144, v145\n"
"v_mfma_f32_16x16x32_bf16 a[152:155], a[88:91], v[72:75], a[152:155]\n"
"v_exp_f32 v147, v147\n"
"v_mfma_f32_16x16x32_bf16 a[152:155], a[92:95], v[56:59], a[152:155]\n"
"v_exp_f32 v148, v148\n"
"v_mfma_f32_16x16x32_bf16 a[156:159], a[92:95], v[60:63], a[156:159]\n"
"buffer_load_dwordx4 v0, s[24:27], s80 offen offset:2048 lds\n"
"v_mfma_f32_16x16x32_bf16 a[156:159], a[88:91], v[76:79], a[156:159]\n"
"s_add_u32 m0, m0, 32\n"
"ds_read_b128 a[36:39], v24 offset:34368\n"
"v_cvt_pk_bf16_f32 v83, v146, v147\n"
"v_mfma_f32_16x16x32_bf16 a[160:163], a[96:99], v[64:67], a[160:163]\n"
"v_exp_f32 v149, v149\n"
"v_mfma_f32_16x16x32_bf16 a[160:163], a[100:103], v[48:51], a[160:163]\n"
"v_exp_f32 v150, v150\n"
"v_mfma_f32_16x16x32_bf16 a[164:167], a[100:103], v[52:55], a[164:167]\n"
"ds_read_b128 a[40:43], v24 offset:34432\n"
"v_cvt_pk_bf16_f32 v86, v148, v149\n"
"v_mfma_f32_16x16x32_bf16 a[164:167], a[96:99], v[68:71], a[164:167]\n"
"v_exp_f32 v151, v151\n"
"v_mfma_f32_16x16x32_bf16 a[168:171], a[96:99], v[72:75], a[168:171]\n"
"v_exp_f32 v152, v152\n"
"v_mfma_f32_16x16x32_bf16 a[168:171], a[100:103], v[56:59], a[168:171]\n"
"buffer_load_dwordx4 v0, s[24:27], s80 offen offset:3072 lds\n"
"v_mfma_f32_16x16x32_bf16 a[172:175], a[100:103], v[60:63], a[172:175]\n"
"s_add_u32 s80, s80, 16384\n"
"ds_read_b128 a[44:47], v24 offset:34496\n"
"v_cvt_pk_bf16_f32 v87, v150, v151\n"
"v_mfma_f32_16x16x32_bf16 a[172:175], a[96:99], v[76:79], a[172:175]\n"
"s_add_u32 m0, m0, 16800\n"
"v_exp_f32 v153, v153\n"
"v_mfma_f32_16x16x32_bf16 a[176:179], a[104:107], v[64:67], a[176:179]\n"
"v_exp_f32 v154, v154\n"
"v_mfma_f32_16x16x32_bf16 a[176:179], a[108:111], v[48:51], a[176:179]\n"
"ds_read_b128 a[48:51], v24 offset:34560\n"
"v_cvt_pk_bf16_f32 v90, v152, v153\n"
"v_mfma_f32_16x16x32_bf16 a[180:183], a[108:111], v[52:55], a[180:183]\n"
"v_exp_f32 v155, v155\n"
"v_mfma_f32_16x16x32_bf16 a[180:183], a[104:107], v[68:71], a[180:183]\n"
"v_exp_f32 v156, v156\n"
"v_mfma_f32_16x16x32_bf16 a[184:187], a[104:107], v[72:75], a[184:187]\n"
"ds_read_b128 a[52:55], v24 offset:34624\n"
"v_cvt_pk_bf16_f32 v91, v154, v155\n"
"v_mfma_f32_16x16x32_bf16 a[184:187], a[108:111], v[56:59], a[184:187]\n"
"v_exp_f32 v157, v157\n"
"v_mfma_f32_16x16x32_bf16 a[188:191], a[108:111], v[60:63], a[188:191]\n"
"buffer_load_dwordx4 v0, s[24:27], s80 offen lds\n"
"v_mfma_f32_16x16x32_bf16 a[188:191], a[104:107], v[76:79], a[188:191]\n"
"s_add_u32 m0, m0, 32\n"
"v_exp_f32 v158, v158\n"
"v_mfma_f32_16x16x32_bf16 a[192:195], a[112:115], v[64:67], a[192:195]\n"
"ds_read_b128 a[56:59], v24 offset:34688\n"
"v_cvt_pk_bf16_f32 v94, v156, v157\n"
"v_mfma_f32_16x16x32_bf16 a[192:195], a[116:119], v[48:51], a[192:195]\n"
"v_exp_f32 v159, v159\n"
"v_mfma_f32_16x16x32_bf16 a[196:199], a[116:119], v[52:55], a[196:199]\n"
"v_exp_f32 v224, v224\n"
"v_mfma_f32_16x16x32_bf16 a[196:199], a[112:115], v[68:71], a[196:199]\n"
"ds_read_b128 a[60:63], v24 offset:34752\n"
"v_cvt_pk_bf16_f32 v95, v158, v159\n"
"v_mfma_f32_16x16x32_bf16 a[200:203], a[112:115], v[72:75], a[200:203]\n"
"v_exp_f32 v225, v225\n"
"v_mfma_f32_16x16x32_bf16 a[200:203], a[116:119], v[56:59], a[200:203]\n"
"v_exp_f32 v226, v226\n"
"v_mfma_f32_16x16x32_bf16 a[204:207], a[116:119], v[60:63], a[204:207]\n"
"buffer_load_dwordx4 v0, s[24:27], s80 offen offset:1024 lds\n"
"v_mfma_f32_16x16x32_bf16 a[204:207], a[112:115], v[76:79], a[204:207]\n"
"s_add_u32 m0, m0, 32\n"
"v_exp_f32 v227, v227\n"
"v_mfma_f32_16x16x32_bf16 a[208:211], a[120:123], v[64:67], a[208:211]\n"
"v_exp_f32 v228, v228\n"
"v_mfma_f32_16x16x32_bf16 a[208:211], a[124:127], v[48:51], a[208:211]\n"
"v_mfma_f32_16x16x32_bf16 a[212:215], a[124:127], v[52:55], a[212:215]\n"
"v_exp_f32 v229, v229\n"
"v_mfma_f32_16x16x32_bf16 a[212:215], a[120:123], v[68:71], a[212:215]\n"
"v_exp_f32 v230, v230\n"
"v_mfma_f32_16x16x32_bf16 a[216:219], a[120:123], v[72:75], a[216:219]\n"
"v_exp_f32 v231, v231\n"
"v_mfma_f32_16x16x32_bf16 a[216:219], a[124:127], v[56:59], a[216:219]\n"
"buffer_load_dwordx4 v0, s[24:27], s80 offen offset:2048 lds\n"
"v_mfma_f32_16x16x32_bf16 a[220:223], a[124:127], v[60:63], a[220:223]\n"
"s_waitcnt lgkmcnt(0)\n"
"v_mfma_f32_16x16x32_bf16 a[220:223], a[120:123], v[76:79], a[220:223]\n"
"s_add_u32 m0, m0, 32\n"
"v_mfma_f32_16x16x32_bf16 v[128:131], a[0:3], v[160:163], v[8:11]\n"
"v_exp_f32 v232, v232\n"
"v_mfma_f32_16x16x32_bf16 v[128:131], a[4:7], v[176:179], v[128:131]\n"
"v_exp_f32 v233, v233\n"
"v_mfma_f32_16x16x32_bf16 v[128:131], a[8:11], v[192:195], v[128:131]\n"
"ds_read_b64_tr_b16 a[64:65], v25 offset:16896\n"
"ds_read_b64_tr_b16 a[66:67], v25 offset:17152\n"
"v_mfma_f32_16x16x32_bf16 v[128:131], a[12:15], v[208:211], v[128:131]\n"
"ds_read_b64_tr_b16 a[68:69], v25 offset:17408\n"
"v_cvt_pk_bf16_f32 v48, v224, v225\n"
"v_mfma_f32_16x16x32_bf16 v[132:135], a[12:15], v[212:215], v[12:15]\n"
"v_exp_f32 v234, v234\n"
"v_mfma_f32_16x16x32_bf16 v[132:135], a[8:11], v[196:199], v[132:135]\n"
"buffer_load_dwordx4 v0, s[24:27], s80 offen offset:3072 lds\n"
"v_mfma_f32_16x16x32_bf16 v[132:135], a[4:7], v[180:183], v[132:135]\n"
"s_add_u32 s82, s82, 36864\n"
"ds_read_b64_tr_b16 a[70:71], v25 offset:17664\n"
"v_cvt_pk_bf16_f32 v49, v226, v227\n"
"v_mfma_f32_16x16x32_bf16 v[132:135], a[0:3], v[164:167], v[132:135]\n"
"s_mov_b32 m0, s84\n"
"v_exp_f32 v235, v235\n"
"v_mfma_f32_16x16x32_bf16 v[136:139], a[0:3], v[168:171], v[16:19]\n"
"v_exp_f32 v236, v236\n"
"v_mfma_f32_16x16x32_bf16 v[136:139], a[4:7], v[184:187], v[136:139]\n"
"ds_read_b64_tr_b16 a[72:73], v25 offset:16928\n"
"ds_read_b64_tr_b16 a[74:75], v25 offset:17184\n"
"v_mfma_f32_16x16x32_bf16 v[136:139], a[8:11], v[200:203], v[136:139]\n"
"ds_read_b64_tr_b16 a[76:77], v25 offset:17440\n"
"v_cvt_pk_bf16_f32 v52, v228, v229\n"
"v_mfma_f32_16x16x32_bf16 v[136:139], a[12:15], v[216:219], v[136:139]\n"
"v_exp_f32 v237, v237\n"
"v_mfma_f32_16x16x32_bf16 v[140:143], a[12:15], v[220:223], v[20:23]\n"
"ds_read_b64_tr_b16 a[78:79], v25 offset:17696\n"
"v_cvt_pk_bf16_f32 v53, v230, v231\n"
"v_mfma_f32_16x16x32_bf16 v[140:143], a[8:11], v[204:207], v[140:143]\n"
"v_exp_f32 v238, v238\n"
"v_mfma_f32_16x16x32_bf16 v[140:143], a[4:7], v[188:191], v[140:143]\n"
"buffer_load_dwordx4 v28, s[44:47], s82 offen lds\n"
"v_mfma_f32_16x16x32_bf16 v[140:143], a[0:3], v[172:175], v[140:143]\n"
"v_exp_f32 v239, v239\n"
"v_mfma_f32_16x16x32_bf16 v[144:147], a[16:19], v[160:163], v[8:11]\n"
"v_mfma_f32_16x16x32_bf16 v[144:147], a[20:23], v[176:179], v[144:147]\n"
"ds_read_b64_tr_b16 a[80:81], v25 offset:16960\n"
"v_cvt_pk_bf16_f32 v56, v232, v233\n"
"v_mfma_f32_16x16x32_bf16 v[144:147], a[24:27], v[192:195], v[144:147]\n"
"v_exp_f32 v240, v240\n"
"v_mfma_f32_16x16x32_bf16 v[144:147], a[28:31], v[208:211], v[144:147]\n"
"ds_read_b64_tr_b16 a[82:83], v25 offset:17216\n"
"v_cvt_pk_bf16_f32 v57, v234, v235\n"
"v_mfma_f32_16x16x32_bf16 v[148:151], a[28:31], v[212:215], v[12:15]\n"
"v_exp_f32 v241, v241\n"
"v_mfma_f32_16x16x32_bf16 v[148:151], a[24:27], v[196:199], v[148:151]\n"
"ds_read_b64_tr_b16 a[84:85], v25 offset:17472\n"
"ds_read_b64_tr_b16 a[86:87], v25 offset:17728\n"
"v_mfma_f32_16x16x32_bf16 v[148:151], a[20:23], v[180:183], v[148:151]\n"
"v_exp_f32 v242, v242\n"
"v_mfma_f32_16x16x32_bf16 v[148:151], a[16:19], v[164:167], v[148:151]\n"
"buffer_store_dwordx4 a[228:231], v0, s[16:19], s7 offen offset:0 sc1\n"
"v_mfma_f32_16x16x32_bf16 v[152:155], a[16:19], v[168:171], v[16:19]\n"
"ds_read_b64_tr_b16 a[88:89], v25 offset:16992\n"
"v_cvt_pk_bf16_f32 v60, v236, v237\n"
"v_mfma_f32_16x16x32_bf16 v[152:155], a[20:23], v[184:187], v[152:155]\n"
"v_exp_f32 v243, v243\n"
"v_mfma_f32_16x16x32_bf16 v[152:155], a[24:27], v[200:203], v[152:155]\n"
"ds_read_b64_tr_b16 a[90:91], v25 offset:17248\n"
"v_cvt_pk_bf16_f32 v61, v238, v239\n"
"v_mfma_f32_16x16x32_bf16 v[152:155], a[28:31], v[216:219], v[152:155]\n"
"v_exp_f32 v244, v244\n"
"v_mfma_f32_16x16x32_bf16 v[156:159], a[28:31], v[220:223], v[20:23]\n"
"v_mfma_f32_16x16x32_bf16 v[156:159], a[24:27], v[204:207], v[156:159]\n"
"v_exp_f32 v245, v245\n"
"v_mfma_f32_16x16x32_bf16 v[156:159], a[20:23], v[188:191], v[156:159]\n"
"ds_read_b64_tr_b16 a[92:93], v25 offset:17504\n"
"v_cvt_pk_bf16_f32 v50, v240, v241\n"
"v_mfma_f32_16x16x32_bf16 v[156:159], a[16:19], v[172:175], v[156:159]\n"
"v_exp_f32 v246, v246\n"
"v_mfma_f32_16x16x32_bf16 v[224:227], a[32:35], v[160:163], v[8:11]\n"
"ds_read_b64_tr_b16 a[94:95], v25 offset:17760\n"
"ds_read_b64_tr_b16 a[96:97], v25 offset:17024\n"
"v_mfma_f32_16x16x32_bf16 v[224:227], a[36:39], v[176:179], v[224:227]\n"
"ds_read_b64_tr_b16 a[98:99], v25 offset:17280\n"
"v_cvt_pk_bf16_f32 v51, v242, v243\n"
"v_mfma_f32_16x16x32_bf16 v[224:227], a[40:43], v[192:195], v[224:227]\n"
"v_exp_f32 v247, v247\n"
"v_mfma_f32_16x16x32_bf16 v[224:227], a[44:47], v[208:211], v[224:227]\n"
"buffer_store_dwordx4 a[228:231], v0, s[16:19], s7 offen offset:1024 sc1\n"
"v_mfma_f32_16x16x32_bf16 v[228:231], a[44:47], v[212:215], v[12:15]\n"
"v_exp_f32 v248, v248\n"
"v_mfma_f32_16x16x32_bf16 v[228:231], a[40:43], v[196:199], v[228:231]\n"
"ds_read_b64_tr_b16 a[100:101], v25 offset:17536\n"
"v_cvt_pk_bf16_f32 v54, v244, v245\n"
"v_mfma_f32_16x16x32_bf16 v[228:231], a[36:39], v[180:183], v[228:231]\n"
"v_exp_f32 v249, v249\n"
"v_mfma_f32_16x16x32_bf16 v[228:231], a[32:35], v[164:167], v[228:231]\n"
"v_mfma_f32_16x16x32_bf16 v[232:235], a[32:35], v[168:171], v[16:19]\n"
"ds_read_b64_tr_b16 a[102:103], v25 offset:17792\n"
"v_cvt_pk_bf16_f32 v55, v246, v247\n"
"v_mfma_f32_16x16x32_bf16 v[232:235], a[36:39], v[184:187], v[232:235]\n"
"v_exp_f32 v250, v250\n"
"v_mfma_f32_16x16x32_bf16 v[232:235], a[40:43], v[200:203], v[232:235]\n"
"ds_read_b64_tr_b16 a[104:105], v25 offset:17056\n"
"ds_read_b64_tr_b16 a[106:107], v25 offset:17312\n"
"v_mfma_f32_16x16x32_bf16 v[232:235], a[44:47], v[216:219], v[232:235]\n"
"v_exp_f32 v251, v251\n"
"v_mfma_f32_16x16x32_bf16 v[236:239], a[44:47], v[220:223], v[20:23]\n"
"ds_read_b64_tr_b16 a[108:109], v25 offset:17568\n"
"v_cvt_pk_bf16_f32 v58, v248, v249\n"
"v_mfma_f32_16x16x32_bf16 v[236:239], a[40:43], v[204:207], v[236:239]\n"
"v_exp_f32 v252, v252\n"
"v_mfma_f32_16x16x32_bf16 v[236:239], a[36:39], v[188:191], v[236:239]\n"
"buffer_store_dwordx4 a[224:227], v0, s[48:51], s83 offen sc1\n"
"v_mfma_f32_16x16x32_bf16 v[236:239], a[32:35], v[172:175], v[236:239]\n"
"s_bfe_u32 s98, s12, 0x50002\n"
"ds_read_b64_tr_b16 a[110:111], v25 offset:17824\n"
"v_cvt_pk_bf16_f32 v59, v250, v251\n"
"v_mfma_f32_16x16x32_bf16 v[240:243], a[48:51], v[160:163], v[8:11]\n"
"s_mul_i32 s99, s98, 9\n"
"v_exp_f32 v253, v253\n"
"v_mfma_f32_16x16x32_bf16 v[240:243], a[52:55], v[176:179], v[240:243]\n"
"s_mul_i32 s83, s98, 7\n"
"v_exp_f32 v254, v254\n"
"v_mfma_f32_16x16x32_bf16 v[240:243], a[56:59], v[192:195], v[240:243]\n"
"s_add_u32 s83, s83, 24\n"
"ds_read_b64_tr_b16 a[112:113], v25 offset:17088\n"
"ds_read_b64_tr_b16 a[114:115], v25 offset:17344\n"
"v_mfma_f32_16x16x32_bf16 v[240:243], a[60:63], v[208:211], v[240:243]\n"
"s_min_u32 s99, s99, s83\n"
"ds_read_b64_tr_b16 a[116:117], v25 offset:17600\n"
"v_cvt_pk_bf16_f32 v62, v252, v253\n"
"v_mfma_f32_16x16x32_bf16 v[244:247], a[60:63], v[212:215], v[12:15]\n"
"s_add_u32 s99, s99, s14\n"
"v_exp_f32 v255, v255\n"
"v_mfma_f32_16x16x32_bf16 v[244:247], a[56:59], v[196:199], v[244:247]\n"
"s_cmp_lt_u32 s14, 7\n"
"v_mfma_f32_16x16x32_bf16 v[244:247], a[52:55], v[180:183], v[244:247]\n"
"s_cselect_b32 s50, 14, 12\n"
"ds_read_b64_tr_b16 a[118:119], v25 offset:17856\n"
"v_cvt_pk_bf16_f32 v63, v254, v255\n"
"v_mfma_f32_16x16x32_bf16 v[244:247], a[48:51], v[164:167], v[244:247]\n"
"s_add_u32 s83, s98, s15\n"
"v_mfma_f32_16x16x32_bf16 v[248:251], a[48:51], v[168:171], v[16:19]\n"
"s_cmp_lt_u32 s98, s50\n"
"ds_read_b64_tr_b16 a[120:121], v25 offset:17120\n"
"ds_read_b64_tr_b16 a[122:123], v25 offset:17376\n"
"v_mfma_f32_16x16x32_bf16 v[248:251], a[52:55], v[184:187], v[248:251]\n"
"s_cselect_b32 s99, s99, s83\n"
"v_mfma_f32_16x16x32_bf16 v[248:251], a[56:59], v[200:203], v[248:251]\n"
"s_and_b32 s98, s12, 3\n"
"v_mfma_f32_16x16x32_bf16 v[248:251], a[60:63], v[216:219], v[248:251]\n"
"s_lshl2_add_u32 s99, s99, s98\n"
"ds_read_b64_tr_b16 a[124:125], v25 offset:17632\n"
"ds_read_b64_tr_b16 a[126:127], v25 offset:17888\n"
"v_mfma_f32_16x16x32_bf16 v[252:255], a[60:63], v[220:223], v[20:23]\n"
"s_lshl_b32 s83, s99, 10\n"
"v_mfma_f32_16x16x32_bf16 v[252:255], a[56:59], v[204:207], v[252:255]\n"
"s_cmp_lt_u32 s12, s13\n"
"v_mfma_f32_16x16x32_bf16 v[252:255], a[52:55], v[188:191], v[252:255]\n"
"s_cselect_b32 s50, 626688, 0\n"
"v_mfma_f32_16x16x32_bf16 v[252:255], a[48:51], v[172:175], v[252:255]\n"
"s_add_u32 s12, s12, 1\n"
"v_smfmac_f32_16x16x64_bf16 a[128:131], v[4:7], v[80:87], v2\n"
"s_add_u32 s7, s7, 2048\n"
"v_exp_f32 v128, v128\n"
"v_smfmac_f32_16x16x64_bf16 a[132:135], v[4:7], v[88:95], v2\n"
"s_add_u32 s62, s62, 1\n"
"v_exp_f32 v129, v129\n"
"v_smfmac_f32_16x16x64_bf16 a[128:131], v[4:7], v[48:55], v2\n"
"v_exp_f32 v130, v130\n"
"v_smfmac_f32_16x16x64_bf16 a[132:135], v[4:7], v[56:63], v2\n"
"s_waitcnt lgkmcnt(0)\n"
"v_add_u32 v25, s61, v25\n"
"v_mfma_f32_16x16x32_bf16 v[96:99], a[64:67], v[80:83], v[96:99]\n"
"ds_read_b128 a[0:3], v24 offset:50688\n"
"v_cvt_pk_bf16_f32 v64, v128, v129\n"
"v_mfma_f32_16x16x32_bf16 v[96:99], a[68:71], v[48:51], v[96:99]\n"
"v_exp_f32 v131, v131\n"
"v_mfma_f32_16x16x32_bf16 v[100:103], a[68:71], v[52:55], v[100:103]\n"
"v_exp_f32 v132, v132\n"
"v_mfma_f32_16x16x32_bf16 v[100:103], a[64:67], v[84:87], v[100:103]\n"
"ds_read_b128 a[4:7], v24 offset:50752\n"
"v_cvt_pk_bf16_f32 v65, v130, v131\n"
"v_mfma_f32_16x16x32_bf16 v[104:107], a[64:67], v[88:91], v[104:107]\n"
"v_exp_f32 v133, v133\n"
"v_mfma_f32_16x16x32_bf16 v[104:107], a[68:71], v[56:59], v[104:107]\n"
"v_exp_f32 v134, v134\n"
"v_mfma_f32_16x16x32_bf16 v[108:111], a[68:71], v[60:63], v[108:111]\n"
"v_mfma_f32_16x16x32_bf16 v[108:111], a[64:67], v[92:95], v[108:111]\n"
"ds_read_b128 a[8:11], v24 offset:50816\n"
"v_cvt_pk_bf16_f32 v68, v132, v133\n"
"v_mfma_f32_16x16x32_bf16 v[112:115], a[72:75], v[80:83], v[112:115]\n"
"v_exp_f32 v135, v135\n"
"v_mfma_f32_16x16x32_bf16 v[112:115], a[76:79], v[48:51], v[112:115]\n"
"v_exp_f32 v136, v136\n"
"v_mfma_f32_16x16x32_bf16 v[116:119], a[76:79], v[52:55], v[116:119]\n"
"ds_read_b128 a[12:15], v24 offset:50880\n"
"v_cvt_pk_bf16_f32 v69, v134, v135\n"
"v_mfma_f32_16x16x32_bf16 v[116:119], a[72:75], v[84:87], v[116:119]\n"
"v_exp_f32 v137, v137\n"
"v_mfma_f32_16x16x32_bf16 v[120:123], a[72:75], v[88:91], v[120:123]\n"
"v_exp_f32 v138, v138\n"
"v_mfma_f32_16x16x32_bf16 v[120:123], a[76:79], v[56:59], v[120:123]\n"
"ds_read_b128 a[16:19], v24 offset:50944\n"
"v_cvt_pk_bf16_f32 v72, v136, v137\n"
"v_mfma_f32_16x16x32_bf16 v[124:127], a[76:79], v[60:63], v[124:127]\n"
"v_exp_f32 v139, v139\n"
"v_mfma_f32_16x16x32_bf16 v[124:127], a[72:75], v[92:95], v[124:127]\n"
"v_mfma_f32_16x16x32_bf16 v[32:35], a[80:83], v[80:83], v[32:35]\n"
"v_exp_f32 v140, v140\n"
"v_mfma_f32_16x16x32_bf16 v[32:35], a[84:87], v[48:51], v[32:35]\n"
"ds_read_b128 a[20:23], v24 offset:51008\n"
"v_cvt_pk_bf16_f32 v73, v138, v139\n"
"v_mfma_f32_16x16x32_bf16 v[36:39], a[84:87], v[52:55], v[36:39]\n"
"v_exp_f32 v141, v141\n"
"v_mfma_f32_16x16x32_bf16 v[36:39], a[80:83], v[84:87], v[36:39]\n"
"v_exp_f32 v142, v142\n"
"v_mfma_f32_16x16x32_bf16 v[40:43], a[80:83], v[88:91], v[40:43]\n"
"ds_read_b128 a[24:27], v24 offset:51072\n"
"v_cvt_pk_bf16_f32 v76, v140, v141\n"
"v_mfma_f32_16x16x32_bf16 v[40:43], a[84:87], v[56:59], v[40:43]\n"
"v_exp_f32 v143, v143\n"
"v_mfma_f32_16x16x32_bf16 v[44:47], a[84:87], v[60:63], v[44:47]\n"
"v_exp_f32 v144, v144\n"
"v_mfma_f32_16x16x32_bf16 v[44:47], a[80:83], v[92:95], v[44:47]\n"
"v_mfma_f32_16x16x32_bf16 a[144:147], a[88:91], v[80:83], a[144:147]\n"
"ds_read_b128 a[28:31], v24 offset:51136\n"
"v_cvt_pk_bf16_f32 v77, v142, v143\n"
"v_mfma_f32_16x16x32_bf16 a[144:147], a[92:95], v[48:51], a[144:147]\n"
"v_exp_f32 v145, v145\n"
"v_mfma_f32_16x16x32_bf16 a[148:151], a[92:95], v[52:55], a[148:151]\n"
"v_exp_f32 v146, v146\n"
"v_mfma_f32_16x16x32_bf16 a[148:151], a[88:91], v[84:87], a[148:151]\n"
"ds_read_b128 a[32:35], v24 offset:51200\n"
"v_cvt_pk_bf16_f32 v66, v144, v145\n"
"v_mfma_f32_16x16x32_bf16 a[152:155], a[88:91], v[88:91], a[152:155]\n"
"v_exp_f32 v147, v147\n"
"v_mfma_f32_16x16x32_bf16 a[152:155], a[92:95], v[56:59], a[152:155]\n"
"v_exp_f32 v148, v148\n"
"v_mfma_f32_16x16x32_bf16 a[156:159], a[92:95], v[60:63], a[156:159]\n"
"v_mfma_f32_16x16x32_bf16 a[156:159], a[88:91], v[92:95], a[156:159]\n"
"ds_read_b128 a[36:39], v24 offset:51264\n"
"v_cvt_pk_bf16_f32 v67, v146, v147\n"
"v_mfma_f32_16x16x32_bf16 a[160:163], a[96:99], v[80:83], a[160:163]\n"
"v_exp_f32 v149, v149\n"
"v_mfma_f32_16x16x32_bf16 a[160:163], a[100:103], v[48:51], a[160:163]\n"
"v_exp_f32 v150, v150\n"
"v_mfma_f32_16x16x32_bf16 a[164:167], a[100:103], v[52:55], a[164:167]\n"
"ds_read_b128 a[40:43], v24 offset:51328\n"
"v_cvt_pk_bf16_f32 v70, v148, v149\n"
"v_mfma_f32_16x16x32_bf16 a[164:167], a[96:99], v[84:87], a[164:167]\n"
"v_exp_f32 v151, v151\n"
"v_mfma_f32_16x16x32_bf16 a[168:171], a[96:99], v[88:91], a[168:171]\n"
"v_exp_f32 v152, v152\n"
"v_mfma_f32_16x16x32_bf16 a[168:171], a[100:103], v[56:59], a[168:171]\n"
"v_mfma_f32_16x16x32_bf16 a[172:175], a[100:103], v[60:63], a[172:175]\n"
"ds_read_b128 a[44:47], v24 offset:51392\n"
"v_cvt_pk_bf16_f32 v71, v150, v151\n"
"v_mfma_f32_16x16x32_bf16 a[172:175], a[96:99], v[92:95], a[172:175]\n"
"v_exp_f32 v153, v153\n"
"v_mfma_f32_16x16x32_bf16 a[176:179], a[104:107], v[80:83], a[176:179]\n"
"v_exp_f32 v154, v154\n"
"v_mfma_f32_16x16x32_bf16 a[176:179], a[108:111], v[48:51], a[176:179]\n"
"ds_read_b128 a[48:51], v24 offset:51456\n"
"v_cvt_pk_bf16_f32 v74, v152, v153\n"
"v_mfma_f32_16x16x32_bf16 a[180:183], a[108:111], v[52:55], a[180:183]\n"
"v_exp_f32 v155, v155\n"
"v_mfma_f32_16x16x32_bf16 a[180:183], a[104:107], v[84:87], a[180:183]\n"
"v_exp_f32 v156, v156\n"
"v_mfma_f32_16x16x32_bf16 a[184:187], a[104:107], v[88:91], a[184:187]\n"
"ds_read_b128 a[52:55], v24 offset:51520\n"
"v_cvt_pk_bf16_f32 v75, v154, v155\n"
"v_mfma_f32_16x16x32_bf16 a[184:187], a[108:111], v[56:59], a[184:187]\n"
"v_exp_f32 v157, v157\n"
"v_mfma_f32_16x16x32_bf16 a[188:191], a[108:111], v[60:63], a[188:191]\n"
"v_mfma_f32_16x16x32_bf16 a[188:191], a[104:107], v[92:95], a[188:191]\n"
"v_exp_f32 v158, v158\n"
"v_mfma_f32_16x16x32_bf16 a[192:195], a[112:115], v[80:83], a[192:195]\n"
"ds_read_b128 a[56:59], v24 offset:51584\n"
"v_cvt_pk_bf16_f32 v78, v156, v157\n"
"v_mfma_f32_16x16x32_bf16 a[192:195], a[116:119], v[48:51], a[192:195]\n"
"v_exp_f32 v159, v159\n"
"v_mfma_f32_16x16x32_bf16 a[196:199], a[116:119], v[52:55], a[196:199]\n"
"v_exp_f32 v224, v224\n"
"v_mfma_f32_16x16x32_bf16 a[196:199], a[112:115], v[84:87], a[196:199]\n"
"ds_read_b128 a[60:63], v24 offset:51648\n"
"v_cvt_pk_bf16_f32 v79, v158, v159\n"
"v_mfma_f32_16x16x32_bf16 a[200:203], a[112:115], v[88:91], a[200:203]\n"
"v_exp_f32 v225, v225\n"
"v_mfma_f32_16x16x32_bf16 a[200:203], a[116:119], v[56:59], a[200:203]\n"
"v_exp_f32 v226, v226\n"
"v_mfma_f32_16x16x32_bf16 a[204:207], a[116:119], v[60:63], a[204:207]\n"
"v_add_u32 v24, s61, v24\n"
"v_mfma_f32_16x16x32_bf16 a[204:207], a[112:115], v[92:95], a[204:207]\n"
"s_sub_u32 s60, s60, s61\n"
"v_exp_f32 v227, v227\n"
"v_mfma_f32_16x16x32_bf16 a[208:211], a[120:123], v[80:83], a[208:211]\n"
"s_sub_u32 s61, 0, s61\n"
"v_exp_f32 v228, v228\n"
"v_mfma_f32_16x16x32_bf16 a[208:211], a[124:127], v[48:51], a[208:211]\n"
"v_mfma_f32_16x16x32_bf16 a[212:215], a[124:127], v[52:55], a[212:215]\n"
"v_exp_f32 v229, v229\n"
"v_mfma_f32_16x16x32_bf16 a[212:215], a[120:123], v[84:87], a[212:215]\n"
"v_exp_f32 v230, v230\n"
"v_mfma_f32_16x16x32_bf16 a[216:219], a[120:123], v[88:91], a[216:219]\n"
"v_exp_f32 v231, v231\n"
"v_mfma_f32_16x16x32_bf16 a[216:219], a[124:127], v[56:59], a[216:219]\n"
"s_cmp_lt_u32 s62, 14\n"
"v_mfma_f32_16x16x32_bf16 a[220:223], a[124:127], v[60:63], a[220:223]\n"
"s_waitcnt lgkmcnt(0)\n"
"v_mfma_f32_16x16x32_bf16 a[220:223], a[120:123], v[92:95], a[220:223]\n"
"s_cbranch_scc1 L_att\n"
"v_mfma_f32_16x16x32_bf16 v[128:131], a[0:3], v[160:163], v[8:11]\n"
"s_waitcnt vmcnt(3)\n"
"v_exp_f32 v232, v232\n"
"v_mfma_f32_16x16x32_bf16 v[128:131], a[4:7], v[176:179], v[128:131]\n"
"s_branch L_h14\n"
"L_hr2:\n"
"v_exp_f32 v233, v233\n"
"v_mfma_f32_16x16x32_bf16 v[128:131], a[8:11], v[192:195], v[128:131]\n"
"s_add_u32 s81, s81, 16384\n"
"ds_read_b64_tr_b16 a[64:65], v25 offset:0\n"
"ds_read_b64_tr_b16 a[66:67], v25 offset:256\n"
"v_mfma_f32_16x16x32_bf16 v[128:131], a[12:15], v[208:211], v[128:131]\n"
"s_mov_b32 m0, s60\n"
"ds_read_b64_tr_b16 a[68:69], v25 offset:512\n"
"v_cvt_pk_bf16_f32 v48, v224, v225\n"
"v_mfma_f32_16x16x32_bf16 v[132:135], a[12:15], v[212:215], v[12:15]\n"
"s_bfe_u32 s64, s6, 0x20001\n"
"v_exp_f32 v234, v234\n"
"v_mfma_f32_16x16x32_bf16 v[132:135], a[8:11], v[196:199], v[132:135]\n"
"s_bfe_u32 s65, s6, 0x20003\n"
"buffer_load_dwordx4 v0, s[28:31], s81 offen lds\n"
"v_lshlrev_b32 v1, 2, v0\n"
"v_mfma_f32_16x16x32_bf16 v[132:135], a[4:7], v[180:183], v[132:135]\n"
"s_add_u32 m0, m0, 32\n"
"ds_read_b64_tr_b16 a[70:71], v25 offset:768\n"
"v_cvt_pk_bf16_f32 v49, v226, v227\n"
"v_mfma_f32_16x16x32_bf16 v[132:135], a[0:3], v[164:167], v[132:135]\n"
"s_lshl2_add_u32 s34, s65, s64\n"
"v_exp_f32 v235, v235\n"
"v_mfma_f32_16x16x32_bf16 v[136:139], a[0:3], v[168:171], v[16:19]\n"
"s_mul_i32 s34, s34, 626688\n"
"v_exp_f32 v236, v236\n"
"v_mfma_f32_16x16x32_bf16 v[136:139], a[4:7], v[184:187], v[136:139]\n"
"s_lshl_b32 s55, s52, 12\n"
"ds_read_b64_tr_b16 a[72:73], v25 offset:32\n"
"ds_read_b64_tr_b16 a[74:75], v25 offset:288\n"
"v_mfma_f32_16x16x32_bf16 v[136:139], a[8:11], v[200:203], v[136:139]\n"
"s_add_u32 s34, s34, s55\n"
"ds_read_b64_tr_b16 a[76:77], v25 offset:544\n"
"v_cvt_pk_bf16_f32 v52, v228, v229\n"
"v_mfma_f32_16x16x32_bf16 v[136:139], a[12:15], v[216:219], v[136:139]\n"
"s_add_u32 s59, s34, 0x2800000\n"
"v_exp_f32 v237, v237\n"
"v_mfma_f32_16x16x32_bf16 v[140:143], a[12:15], v[220:223], v[20:23]\n"
"s_lshr_b32 s34, s6, 5\n"
"ds_read_b64_tr_b16 a[78:79], v25 offset:800\n"
"v_cvt_pk_bf16_f32 v53, v230, v231\n"
"v_mfma_f32_16x16x32_bf16 v[140:143], a[8:11], v[204:207], v[140:143]\n"
"s_and_b32 s35, s6, 1\n"
"v_exp_f32 v238, v238\n"
"v_mfma_f32_16x16x32_bf16 v[140:143], a[4:7], v[188:191], v[140:143]\n"
"s_lshl1_add_u32 s34, s34, s35\n"
"buffer_load_dwordx4 v0, s[28:31], s81 offen offset:1024 lds\n"
"v_mfma_f32_16x16x32_bf16 v[140:143], a[0:3], v[172:175], v[140:143]\n"
"s_add_u32 m0, m0, 32\n"
"v_exp_f32 v239, v239\n"
"v_mfma_f32_16x16x32_bf16 v[144:147], a[16:19], v[160:163], v[8:11]\n"
"s_lshl2_add_u32 s34, s34, s52\n"
"buffer_load_dwordx4 v0, s[28:31], s81 offen offset:2048 lds\n"
"v_mfma_f32_16x16x32_bf16 v[144:147], a[20:23], v[176:179], v[144:147]\n"
"s_add_u32 m0, m0, 32\n"
"ds_read_b64_tr_b16 a[80:81], v25 offset:64\n"
"v_cvt_pk_bf16_f32 v56, v232, v233\n"
"v_mfma_f32_16x16x32_bf16 v[144:147], a[24:27], v[192:195], v[144:147]\n"
"s_mul_i32 s35, s34, 8\n"
"v_exp_f32 v240, v240\n"
"v_mfma_f32_16x16x32_bf16 v[144:147], a[28:31], v[208:211], v[144:147]\n"
"s_add_u32 s35, s35, s64\n"
"ds_read_b64_tr_b16 a[82:83], v25 offset:320\n"
"v_cvt_pk_bf16_f32 v57, v234, v235\n"
"v_mfma_f32_16x16x32_bf16 v[148:151], a[28:31], v[212:215], v[12:15]\n"
"s_mul_i32 s56, s35, 36864\n"
"v_exp_f32 v241, v241\n"
"v_mfma_f32_16x16x32_bf16 v[148:151], a[24:27], v[196:199], v[148:151]\n"
"s_add_u32 s56, s56, 0x1400000\n"
"ds_read_b64_tr_b16 a[84:85], v25 offset:576\n"
"ds_read_b64_tr_b16 a[86:87], v25 offset:832\n"
"v_mfma_f32_16x16x32_bf16 v[148:151], a[20:23], v[180:183], v[148:151]\n"
"s_add_u32 s57, s56, 147456\n"
"v_exp_f32 v242, v242\n"
"v_mfma_f32_16x16x32_bf16 v[148:151], a[16:19], v[164:167], v[148:151]\n"
"s_lshr_b32 s91, s6, 5\n"
"buffer_load_dwordx4 v0, s[28:31], s81 offen offset:3072 lds\n"
"v_mfma_f32_16x16x32_bf16 v[152:155], a[16:19], v[168:171], v[16:19]\n"
"s_add_u32 s81, s81, 16384\n"
"ds_read_b64_tr_b16 a[88:89], v25 offset:96\n"
"v_cvt_pk_bf16_f32 v60, v236, v237\n"
"v_mfma_f32_16x16x32_bf16 v[152:155], a[20:23], v[184:187], v[152:155]\n"
"s_add_u32 m0, m0, 16800\n"
"v_exp_f32 v243, v243\n"
"v_mfma_f32_16x16x32_bf16 v[152:155], a[24:27], v[200:203], v[152:155]\n"
"s_lshl_b32 s91, s91, 14\n"
"ds_read_b64_tr_b16 a[90:91], v25 offset:352\n"
"v_cvt_pk_bf16_f32 v61, v238, v239\n"
"v_mfma_f32_16x16x32_bf16 v[152:155], a[28:31], v[216:219], v[152:155]\n"
"s_add_u32 s91, s91, s59\n"
"v_exp_f32 v244, v244\n"
"v_mfma_f32_16x16x32_bf16 v[156:159], a[28:31], v[220:223], v[20:23]\n"
"s_lshr_b32 s90, s65, 1\n"
"v_mfma_f32_16x16x32_bf16 v[156:159], a[24:27], v[204:207], v[156:159]\n"
"s_lshl_b32 s90, s90, 12\n"
"v_exp_f32 v245, v245\n"
"v_mfma_f32_16x16x32_bf16 v[156:159], a[20:23], v[188:191], v[156:159]\n"
"s_and_b32 s33, s65, 1\n"
"ds_read_b64_tr_b16 a[92:93], v25 offset:608\n"
"v_cvt_pk_bf16_f32 v50, v240, v241\n"
"v_mfma_f32_16x16x32_bf16 v[156:159], a[16:19], v[172:175], v[156:159]\n"
"s_mul_i32 s97, s33, 147456\n"
"v_exp_f32 v246, v246\n"
"v_mfma_f32_16x16x32_bf16 v[224:227], a[32:35], v[160:163], v[8:11]\n"
"s_add_u32 s97, s97, s90\n"
"ds_read_b64_tr_b16 a[94:95], v25 offset:864\n"
"ds_read_b64_tr_b16 a[96:97], v25 offset:128\n"
"v_mfma_f32_16x16x32_bf16 v[224:227], a[36:39], v[176:179], v[224:227]\n"
"s_add_u32 s97, s97, s56\n"
"ds_read_b64_tr_b16 a[98:99], v25 offset:384\n"
"v_cvt_pk_bf16_f32 v51, v242, v243\n"
"v_mfma_f32_16x16x32_bf16 v[224:227], a[40:43], v[192:195], v[224:227]\n"
"v_exp_f32 v247, v247\n"
"v_mfma_f32_16x16x32_bf16 v[224:227], a[44:47], v[208:211], v[224:227]\n"
"buffer_load_dwordx4 v0, s[28:31], s81 offen lds\n"
"v_mfma_f32_16x16x32_bf16 v[228:231], a[44:47], v[212:215], v[12:15]\n"
"s_add_u32 m0, m0, 32\n"
"v_exp_f32 v248, v248\n"
"v_mfma_f32_16x16x32_bf16 v[228:231], a[40:43], v[196:199], v[228:231]\n"
"ds_read_b64_tr_b16 a[100:101], v25 offset:640\n"
"v_cvt_pk_bf16_f32 v54, v244, v245\n"
"v_mfma_f32_16x16x32_bf16 v[228:231], a[36:39], v[180:183], v[228:231]\n"
"v_exp_f32 v249, v249\n"
"v_mfma_f32_16x16x32_bf16 v[228:231], a[32:35], v[164:167], v[228:231]\n"
"buffer_load_dwordx4 v0, s[28:31], s81 offen offset:1024 lds\n"
"v_mfma_f32_16x16x32_bf16 v[232:235], a[32:35], v[168:171], v[16:19]\n"
"s_add_u32 m0, m0, 32\n"
"ds_read_b64_tr_b16 a[102:103], v25 offset:896\n"
"v_cvt_pk_bf16_f32 v55, v246, v247\n"
"v_mfma_f32_16x16x32_bf16 v[232:235], a[36:39], v[184:187], v[232:235]\n"
"v_exp_f32 v250, v250\n"
"v_mfma_f32_16x16x32_bf16 v[232:235], a[40:43], v[200:203], v[232:235]\n"
"v_exp_f32 v251, v251\n"
"v_mfma_f32_16x16x32_bf16 v[232:235], a[44:47], v[216:219], v[232:235]\n"
"ds_read_b64_tr_b16 a[104:105], v25 offset:160\n"
"ds_read_b64_tr_b16 a[106:107], v25 offset:416\n"
"v_mfma_f32_16x16x32_bf16 v[236:239], a[44:47], v[220:223], v[20:23]\n"
"ds_read_b64_tr_b16 a[108:109], v25 offset:672\n"
"v_cvt_pk_bf16_f32 v58, v248, v249\n"
"v_mfma_f32_16x16x32_bf16 v[236:239], a[40:43], v[204:207], v[236:239]\n"
"v_exp_f32 v252, v252\n"
"v_mfma_f32_16x16x32_bf16 v[236:239], a[36:39], v[188:191], v[236:239]\n"
"buffer_load_dwordx4 v0, s[28:31], s81 offen offset:2048 lds\n"
"v_mfma_f32_16x16x32_bf16 v[236:239], a[32:35], v[172:175], v[236:239]\n"
"s_add_u32 m0, m0, 32\n"
"ds_read_b64_tr_b16 a[110:111], v25 offset:928\n"
"v_cvt_pk_bf16_f32 v59, v250, v251\n"
"v_mfma_f32_16x16x32_bf16 v[240:243], a[48:51], v[160:163], v[8:11]\n"
"v_exp_f32 v253, v253\n"
"v_mfma_f32_16x16x32_bf16 v[240:243], a[52:55], v[176:179], v[240:243]\n"
"v_exp_f32 v254, v254\n"
"v_mfma_f32_16x16x32_bf16 v[240:243], a[56:59], v[192:195], v[240:243]\n"
"ds_read_b64_tr_b16 a[112:113], v25 offset:192\n"
"ds_read_b64_tr_b16 a[114:115], v25 offset:448\n"
"v_mfma_f32_16x16x32_bf16 v[240:243], a[60:63], v[208:211], v[240:243]\n"
"ds_read_b64_tr_b16 a[116:117], v25 offset:704\n"
"v_cvt_pk_bf16_f32 v62, v252, v253\n"
"v_mfma_f32_16x16x32_bf16 v[244:247], a[60:63], v[212:215], v[12:15]\n"
"v_exp_f32 v255, v255\n"
"v_mfma_f32_16x16x32_bf16 v[244:247], a[56:59], v[196:199], v[244:247]\n"
"buffer_load_dwordx4 v0, s[28:31], s81 offen offset:3072 lds\n"
"v_mfma_f32_16x16x32_bf16 v[244:247], a[52:55], v[180:183], v[244:247]\n"
"ds_read_b64_tr_b16 a[118:119], v25 offset:960\n"
"v_cvt_pk_bf16_f32 v63, v254, v255\n"
"v_mfma_f32_16x16x32_bf16 v[244:247], a[48:51], v[164:167], v[244:247]\n"
"v_mfma_f32_16x16x32_bf16 v[248:251], a[48:51], v[168:171], v[16:19]\n"
"ds_read_b64_tr_b16 a[120:121], v25 offset:224\n"
"ds_read_b64_tr_b16 a[122:123], v25 offset:480\n"
"v_mfma_f32_16x16x32_bf16 v[248:251], a[52:55], v[184:187], v[248:251]\n"
"v_mfma_f32_16x16x32_bf16 v[248:251], a[56:59], v[200:203], v[248:251]\n"
"v_mfma_f32_16x16x32_bf16 v[248:251], a[60:63], v[216:219], v[248:251]\n"
"ds_read_b64_tr_b16 a[124:125], v25 offset:736\n"
"ds_read_b64_tr_b16 a[126:127], v25 offset:992\n"
"v_mfma_f32_16x16x32_bf16 v[252:255], a[60:63], v[220:223], v[20:23]\n"
"s_add_u32 m0, s55, 33792\n"
"v_mfma_f32_16x16x32_bf16 v[252:255], a[56:59], v[204:207], v[252:255]\n"
"buffer_load_dwordx4 v0, s[8:11], s59 offen sc1 lds\n"
"v_mfma_f32_16x16x32_bf16 v[252:255], a[52:55], v[188:191], v[252:255]\n"
"v_mfma_f32_16x16x32_bf16 v[252:255], a[48:51], v[172:175], v[252:255]\n"
"v_smfmac_f32_16x16x64_bf16 a[128:131], v[4:7], v[64:71], v2\n"
"v_exp_f32 v128, v128\n"
"v_smfmac_f32_16x16x64_bf16 a[132:135], v[4:7], v[72:79], v2\n"
"v_exp_f32 v129, v129\n"
"v_smfmac_f32_16x16x64_bf16 a[128:131], v[4:7], v[48:55], v2\n"
"v_exp_f32 v130, v130\n"
"v_smfmac_f32_16x16x64_bf16 a[132:135], v[4:7], v[56:63], v2\n"
"s_waitcnt lgkmcnt(0)\n"
"buffer_load_dwordx4 v0, s[8:11], s59 offen offset:1024 sc1 lds\n"
"v_mfma_f32_16x16x32_bf16 v[96:99], a[64:67], v[64:67], v[96:99]\n"
"ds_read_b128 a[0:3], v24 offset:33792\n"
"v_cvt_pk_bf16_f32 v80, v128, v129\n"
"v_mfma_f32_16x16x32_bf16 v[96:99], a[68:71], v[48:51], v[96:99]\n"
"v_exp_f32 v131, v131\n"
"v_mfma_f32_16x16x32_bf16 v[100:103], a[68:71], v[52:55], v[100:103]\n"
"v_exp_f32 v132, v132\n"
"v_mfma_f32_16x16x32_bf16 v[100:103], a[64:67], v[68:71], v[100:103]\n"
"ds_read_b128 a[4:7], v24 offset:33856\n"
"v_cvt_pk_bf16_f32 v81, v130, v131\n"
"v_mfma_f32_16x16x32_bf16 v[104:107], a[64:67], v[72:75], v[104:107]\n"
"v_exp_f32 v133, v133\n"
"v_mfma_f32_16x16x32_bf16 v[104:107], a[68:71], v[56:59], v[104:107]\n"
"v_exp_f32 v134, v134\n"
"v_mfma_f32_16x16x32_bf16 v[108:111], a[68:71], v[60:63], v[108:111]\n"
"buffer_load_dwordx4 v0, s[8:11], s59 offen offset:2048 sc1 lds\n"
"v_mfma_f32_16x16x32_bf16 v[108:111], a[64:67], v[76:79], v[108:111]\n"
"ds_read_b128 a[8:11], v24 offset:33920\n"
"v_cvt_pk_bf16_f32 v84, v132, v133\n"
"v_mfma_f32_16x16x32_bf16 v[112:115], a[72:75], v[64:67], v[112:115]\n"
"v_exp_f32 v135, v135\n"
"v_mfma_f32_16x16x32_bf16 v[112:115], a[76:79], v[48:51], v[112:115]\n"
"v_exp_f32 v136, v136\n"
"v_mfma_f32_16x16x32_bf16 v[116:119], a[76:79], v[52:55], v[116:119]\n"
"ds_read_b128 a[12:15], v24 offset:33984\n"
"v_cvt_pk_bf16_f32 v85, v134, v135\n"
"v_mfma_f32_16x16x32_bf16 v[116:119], a[72:75], v[68:71], v[116:119]\n"
"v_exp_f32 v137, v137\n"
"v_mfma_f32_16x16x32_bf16 v[120:123], a[72:75], v[72:75], v[120:123]\n"
"v_exp_f32 v138, v138\n"
"v_mfma_f32_16x16x32_bf16 v[120:123], a[76:79], v[56:59], v[120:123]\n"
"ds_read_b128 a[16:19], v24 offset:34048\n"
"v_cvt_pk_bf16_f32 v88, v136, v137\n"
"v_mfma_f32_16x16x32_bf16 v[124:127], a[76:79], v[60:63], v[124:127]\n"
"v_exp_f32 v139, v139\n"
"v_mfma_f32_16x16x32_bf16 v[124:127], a[72:75], v[76:79], v[124:127]\n"
"buffer_load_dwordx4 v0, s[8:11], s59 offen offset:3072 sc1 lds\n"
"v_mfma_f32_16x16x32_bf16 v[32:35], a[80:83], v[64:67], v[32:35]\n"
"v_exp_f32 v140, v140\n"
"v_mfma_f32_16x16x32_bf16 v[32:35], a[84:87], v[48:51], v[32:35]\n"
"ds_read_b128 a[20:23], v24 offset:34112\n"
"v_cvt_pk_bf16_f32 v89, v138, v139\n"
"v_mfma_f32_16x16x32_bf16 v[36:39], a[84:87], v[52:55], v[36:39]\n"
"v_exp_f32 v141, v141\n"
"v_mfma_f32_16x16x32_bf16 v[36:39], a[80:83], v[68:71], v[36:39]\n"
"v_exp_f32 v142, v142\n"
"v_mfma_f32_16x16x32_bf16 v[40:43], a[80:83], v[72:75], v[40:43]\n"
"ds_read_b128 a[24:27], v24 offset:34176\n"
"v_cvt_pk_bf16_f32 v92, v140, v141\n"
"v_mfma_f32_16x16x32_bf16 v[40:43], a[84:87], v[56:59], v[40:43]\n"
"v_exp_f32 v143, v143\n"
"v_mfma_f32_16x16x32_bf16 v[44:47], a[84:87], v[60:63], v[44:47]\n"
"v_exp_f32 v144, v144\n"
"v_mfma_f32_16x16x32_bf16 v[44:47], a[80:83], v[76:79], v[44:47]\n"
"buffer_load_dwordx4 a[224:227], v0, s[8:11], s56 offen offset:0 sc1\n"
"v_mfma_f32_16x16x32_bf16 a[144:147], a[88:91], v[64:67], a[144:147]\n"
"ds_read_b128 a[28:31], v24 offset:34240\n"
"v_cvt_pk_bf16_f32 v93, v142, v143\n"
"v_mfma_f32_16x16x32_bf16 a[144:147], a[92:95], v[48:51], a[144:147]\n"
"v_exp_f32 v145, v145\n"
"v_mfma_f32_16x16x32_bf16 a[148:151], a[92:95], v[52:55], a[148:151]\n"
"v_exp_f32 v146, v146\n"
"v_mfma_f32_16x16x32_bf16 a[148:151], a[88:91], v[68:71], a[148:151]\n"
"ds_read_b128 a[32:35], v24 offset:34304\n"
"v_cvt_pk_bf16_f32 v82, v144, v145\n"
"v_mfma_f32_16x16x32_bf16 a[152:155], a[88:91], v[72:75], a[152:155]\n"
"v_exp_f32 v147, v147\n"
"v_mfma_f32_16x16x32_bf16 a[152:155], a[92:95], v[56:59], a[152:155]\n"
"v_exp_f32 v148, v148\n"
"v_mfma_f32_16x16x32_bf16 a[156:159], a[92:95], v[60:63], a[156:159]\n"
"buffer_load_dwordx4 a[232:235], v0, s[8:11], s56 offen offset:1024 sc1\n"
"v_mfma_f32_16x16x32_bf16 a[156:159], a[88:91], v[76:79], a[156:159]\n"
"ds_read_b128 a[36:39], v24 offset:34368\n"
"v_cvt_pk_bf16_f32 v83, v146, v147\n"
"v_mfma_f32_16x16x32_bf16 a[160:163], a[96:99], v[64:67], a[160:163]\n"
"v_exp_f32 v149, v149\n"
"v_mfma_f32_16x16x32_bf16 a[160:163], a[100:103], v[48:51], a[160:163]\n"
"s_add_u32 s33, s91, 147456\n"
"v_exp_f32 v150, v150\n"
"v_mfma_f32_16x16x32_bf16 a[164:167], a[100:103], v[52:55], a[164:167]\n"
"ds_read_b128 a[40:43], v24 offset:34432\n"
"v_cvt_pk_bf16_f32 v86, v148, v149\n"
"v_mfma_f32_16x16x32_bf16 a[164:167], a[96:99], v[68:71], a[164:167]\n"
"v_exp_f32 v151, v151\n"
"v_mfma_f32_16x16x32_bf16 a[168:171], a[96:99], v[72:75], a[168:171]\n"
"v_exp_f32 v152, v152\n"
"v_mfma_f32_16x16x32_bf16 a[168:171], a[100:103], v[56:59], a[168:171]\n"
"buffer_load_dwordx4 a[240:243], v0, s[8:11], s56 offen offset:2048 sc1\n"
"buffer_load_dword v3, v1, s[8:11], s33 offen sc1\n"
"v_mfma_f32_16x16x32_bf16 a[172:175], a[100:103], v[60:63], a[172:175]\n"
"ds_read_b128 a[44:47], v24 offset:34496\n"
"v_cvt_pk_bf16_f32 v87, v150, v151\n"
"v_mfma_f32_16x16x32_bf16 a[172:175], a[96:99], v[76:79], a[172:175]\n"
"v_exp_f32 v153, v153\n"
"v_mfma_f32_16x16x32_bf16 a[176:179], a[104:107], v[64:67], a[176:179]\n"
"v_exp_f32 v154, v154\n"
"v_mfma_f32_16x16x32_bf16 a[176:179], a[108:111], v[48:51], a[176:179]\n"
"ds_read_b128 a[48:51], v24 offset:34560\n"
"v_cvt_pk_bf16_f32 v90, v152, v153\n"
"v_mfma_f32_16x16x32_bf16 a[180:183], a[108:111], v[52:55], a[180:183]\n"
"v_exp_f32 v155, v155\n"
"v_mfma_f32_16x16x32_bf16 a[180:183], a[104:107], v[68:71], a[180:183]\n"
"v_exp_f32 v156, v156\n"
"v_mfma_f32_16x16x32_bf16 a[184:187], a[104:107], v[72:75], a[184:187]\n"
"ds_read_b128 a[52:55], v24 offset:34624\n"
"v_cvt_pk_bf16_f32 v91, v154, v155\n"
"v_mfma_f32_16x16x32_bf16 a[184:187], a[108:111], v[56:59], a[184:187]\n"
"v_exp_f32 v157, v157\n"
"v_mfma_f32_16x16x32_bf16 a[188:191], a[108:111], v[60:63], a[188:191]\n"
"buffer_load_dwordx4 a[248:251], v0, s[8:11], s56 offen offset:3072 sc1\n"
"v_mfma_f32_16x16x32_bf16 a[188:191], a[104:107], v[76:79], a[188:191]\n"
"v_exp_f32 v158, v158\n"
"v_mfma_f32_16x16x32_bf16 a[192:195], a[112:115], v[64:67], a[192:195]\n"
"ds_read_b128 a[56:59], v24 offset:34688\n"
"v_cvt_pk_bf16_f32 v94, v156, v157\n"
"v_mfma_f32_16x16x32_bf16 a[192:195], a[116:119], v[48:51], a[192:195]\n"
"v_exp_f32 v159, v159\n"
"v_mfma_f32_16x16x32_bf16 a[196:199], a[116:119], v[52:55], a[196:199]\n"
"v_exp_f32 v224, v224\n"
"v_mfma_f32_16x16x32_bf16 a[196:199], a[112:115], v[68:71], a[196:199]\n"
"ds_read_b128 a[60:63], v24 offset:34752\n"
"v_cvt_pk_bf16_f32 v95, v158, v159\n"
"v_mfma_f32_16x16x32_bf16 a[200:203], a[112:115], v[72:75], a[200:203]\n"
"v_exp_f32 v225, v225\n"
"v_mfma_f32_16x16x32_bf16 a[200:203], a[116:119], v[56:59], a[200:203]\n"
"v_exp_f32 v226, v226\n"
"v_mfma_f32_16x16x32_bf16 a[204:207], a[116:119], v[60:63], a[204:207]\n"
"buffer_load_dwordx4 a[228:231], v0, s[8:11], s57 offen offset:0 sc1\n"
"v_mfma_f32_16x16x32_bf16 a[204:207], a[112:115], v[76:79], a[204:207]\n"
"v_exp_f32 v227, v227\n"
"v_mfma_f32_16x16x32_bf16 a[208:211], a[120:123], v[64:67], a[208:211]\n"
"v_exp_f32 v228, v228\n"
"v_mfma_f32_16x16x32_bf16 a[208:211], a[124:127], v[48:51], a[208:211]\n"
"buffer_load_dwordx4 a[236:239], v0, s[8:11], s57 offen offset:1024 sc1\n"
"v_mfma_f32_16x16x32_bf16 a[212:215], a[124:127], v[52:55], a[212:215]\n"
"v_exp_f32 v229, v229\n"
"v_mfma_f32_16x16x32_bf16 a[212:215], a[120:123], v[68:71], a[212:215]\n"
"v_exp_f32 v230, v230\n"
"v_mfma_f32_16x16x32_bf16 a[216:219], a[120:123], v[72:75], a[216:219]\n"
"v_exp_f32 v231, v231\n"
"v_mfma_f32_16x16x32_bf16 a[216:219], a[124:127], v[56:59], a[216:219]\n"
"v_mfma_f32_16x16x32_bf16 a[220:223], a[124:127], v[60:63], a[220:223]\n"
"s_waitcnt lgkmcnt(0)\n"
"buffer_load_dwordx4 a[244:247], v0, s[8:11], s57 offen offset:2048 sc1\n"
"v_mfma_f32_16x16x32_bf16 a[220:223], a[120:123], v[76:79], a[220:223]\n"
"s_barrier\n"
"v_mfma_f32_16x16x32_bf16 v[128:131], a[0:3], v[160:163], v[8:11]\n"
"v_exp_f32 v232, v232\n"
"v_mfma_f32_16x16x32_bf16 v[128:131], a[4:7], v[176:179], v[128:131]\n"
"v_exp_f32 v233, v233\n"
"v_mfma_f32_16x16x32_bf16 v[128:131], a[8:11], v[192:195], v[128:131]\n"
"ds_read_b64_tr_b16 a[64:65], v25 offset:16896\n"
"ds_read_b64_tr_b16 a[66:67], v25 offset:17152\n"
"v_mfma_f32_16x16x32_bf16 v[128:131], a[12:15], v[208:211], v[128:131]\n"
"ds_read_b64_tr_b16 a[68:69], v25 offset:17408\n"
"v_cvt_pk_bf16_f32 v48, v224, v225\n"
"v_mfma_f32_16x16x32_bf16 v[132:135], a[12:15], v[212:215], v[12:15]\n"
"v_exp_f32 v234, v234\n"
"v_mfma_f32_16x16x32_bf16 v[132:135], a[8:11], v[196:199], v[132:135]\n"
"buffer_load_dwordx4 a[252:255], v0, s[8:11], s57 offen offset:3072 sc1\n"
"v_mfma_f32_16x16x32_bf16 v[132:135], a[4:7], v[180:183], v[132:135]\n"
"s_add_u32 m0, s55, 50176\n"
"ds_read_b64_tr_b16 a[70:71], v25 offset:17664\n"
"v_cvt_pk_bf16_f32 v49, v226, v227\n"
"v_mfma_f32_16x16x32_bf16 v[132:135], a[0:3], v[164:167], v[132:135]\n"
"s_add_u32 s59, s59, 16384\n"
"v_exp_f32 v235, v235\n"
"v_mfma_f32_16x16x32_bf16 v[136:139], a[0:3], v[168:171], v[16:19]\n"
"v_exp_f32 v236, v236\n"
"v_mfma_f32_16x16x32_bf16 v[136:139], a[4:7], v[184:187], v[136:139]\n"
"ds_read_b64_tr_b16 a[72:73], v25 offset:16928\n"
"ds_read_b64_tr_b16 a[74:75], v25 offset:17184\n"
"v_mfma_f32_16x16x32_bf16 v[136:139], a[8:11], v[200:203], v[136:139]\n"
"ds_read_b64_tr_b16 a[76:77], v25 offset:17440\n"
"v_cvt_pk_bf16_f32 v52, v228, v229\n"
"v_mfma_f32_16x16x32_bf16 v[136:139], a[12:15], v[216:219], v[136:139]\n"
"v_exp_f32 v237, v237\n"
"v_mfma_f32_16x16x32_bf16 v[140:143], a[12:15], v[220:223], v[20:23]\n"
"ds_read_b64_tr_b16 a[78:79], v25 offset:17696\n"
"v_cvt_pk_bf16_f32 v53, v230, v231\n"
"v_mfma_f32_16x16x32_bf16 v[140:143], a[8:11], v[204:207], v[140:143]\n"
"v_exp_f32 v238, v238\n"
"v_mfma_f32_16x16x32_bf16 v[140:143], a[4:7], v[188:191], v[140:143]\n"
"buffer_load_dwordx4 v0, s[8:11], s59 offen sc1 lds\n"
"v_mfma_f32_16x16x32_bf16 v[140:143], a[0:3], v[172:175], v[140:143]\n"
"v_exp_f32 v239, v239\n"
"v_mfma_f32_16x16x32_bf16 v[144:147], a[16:19], v[160:163], v[8:11]\n"
"v_mfma_f32_16x16x32_bf16 v[144:147], a[20:23], v[176:179], v[144:147]\n"
"ds_read_b64_tr_b16 a[80:81], v25 offset:16960\n"
"v_cvt_pk_bf16_f32 v56, v232, v233\n"
"v_mfma_f32_16x16x32_bf16 v[144:147], a[24:27], v[192:195], v[144:147]\n"
"v_exp_f32 v240, v240\n"
"v_mfma_f32_16x16x32_bf16 v[144:147], a[28:31], v[208:211], v[144:147]\n"
"ds_read_b64_tr_b16 a[82:83], v25 offset:17216\n"
"v_cvt_pk_bf16_f32 v57, v234, v235\n"
"v_mfma_f32_16x16x32_bf16 v[148:151], a[28:31], v[212:215], v[12:15]\n"
"v_exp_f32 v241, v241\n"
"v_mfma_f32_16x16x32_bf16 v[148:151], a[24:27], v[196:199], v[148:151]\n"
"ds_read_b64_tr_b16 a[84:85], v25 offset:17472\n"
"ds_read_b64_tr_b16 a[86:87], v25 offset:17728\n"
"v_mfma_f32_16x16x32_bf16 v[148:151], a[20:23], v[180:183], v[148:151]\n"
"v_exp_f32 v242, v242\n"
"v_mfma_f32_16x16x32_bf16 v[148:151], a[16:19], v[164:167], v[148:151]\n"
"buffer_load_dwordx4 v0, s[8:11], s59 offen offset:1024 sc1 lds\n"
"v_mfma_f32_16x16x32_bf16 v[152:155], a[16:19], v[168:171], v[16:19]\n"
"ds_read_b64_tr_b16 a[88:89], v25 offset:16992\n"
"v_cvt_pk_bf16_f32 v60, v236, v237\n"
"v_mfma_f32_16x16x32_bf16 v[152:155], a[20:23], v[184:187], v[152:155]\n"
"v_exp_f32 v243, v243\n"
"v_mfma_f32_16x16x32_bf16 v[152:155], a[24:27], v[200:203], v[152:155]\n"
"ds_read_b64_tr_b16 a[90:91], v25 offset:17248\n"
"v_cvt_pk_bf16_f32 v61, v238, v239\n"
"v_mfma_f32_16x16x32_bf16 v[152:155], a[28:31], v[216:219], v[152:155]\n"
"v_exp_f32 v244, v244\n"
"v_mfma_f32_16x16x32_bf16 v[156:159], a[28:31], v[220:223], v[20:23]\n"
"buffer_load_dwordx4 v0, s[8:11], s59 offen offset:2048 sc1 lds\n"
"v_mfma_f32_16x16x32_bf16 v[156:159], a[24:27], v[204:207], v[156:159]\n"
"v_exp_f32 v245, v245\n"
"v_mfma_f32_16x16x32_bf16 v[156:159], a[20:23], v[188:191], v[156:159]\n"
"ds_read_b64_tr_b16 a[92:93], v25 offset:17504\n"
"v_cvt_pk_bf16_f32 v50, v240, v241\n"
"v_mfma_f32_16x16x32_bf16 v[156:159], a[16:19], v[172:175], v[156:159]\n"
"v_exp_f32 v246, v246\n"
"v_mfma_f32_16x16x32_bf16 v[224:227], a[32:35], v[160:163], v[8:11]\n"
"ds_read_b64_tr_b16 a[94:95], v25 offset:17760\n"
"ds_read_b64_tr_b16 a[96:97], v25 offset:17024\n"
"v_mfma_f32_16x16x32_bf16 v[224:227], a[36:39], v[176:179], v[224:227]\n"
"ds_read_b64_tr_b16 a[98:99], v25 offset:17280\n"
"v_cvt_pk_bf16_f32 v51, v242, v243\n"
"v_mfma_f32_16x16x32_bf16 v[224:227], a[40:43], v[192:195], v[224:227]\n"
"v_exp_f32 v247, v247\n"
"v_mfma_f32_16x16x32_bf16 v[224:227], a[44:47], v[208:211], v[224:227]\n"
"buffer_load_dwordx4 v0, s[8:11], s59 offen offset:3072 sc1 lds\n"
"v_mfma_f32_16x16x32_bf16 v[228:231], a[44:47], v[212:215], v[12:15]\n"
"s_add_u32 m0, s55, 141312\n"
"v_exp_f32 v248, v248\n"
"v_mfma_f32_16x16x32_bf16 v[228:231], a[40:43], v[196:199], v[228:231]\n"
"s_add_u32 s59, s59, 16384\n"
"ds_read_b64_tr_b16 a[100:101], v25 offset:17536\n"
"v_cvt_pk_bf16_f32 v54, v244, v245\n"
"v_mfma_f32_16x16x32_bf16 v[228:231], a[36:39], v[180:183], v[228:231]\n"
"v_exp_f32 v249, v249\n"
"v_mfma_f32_16x16x32_bf16 v[228:231], a[32:35], v[164:167], v[228:231]\n"
"v_mfma_f32_16x16x32_bf16 v[232:235], a[32:35], v[168:171], v[16:19]\n"
"ds_read_b64_tr_b16 a[102:103], v25 offset:17792\n"
"v_cvt_pk_bf16_f32 v55, v246, v247\n"
"v_mfma_f32_16x16x32_bf16 v[232:235], a[36:39], v[184:187], v[232:235]\n"
"v_exp_f32 v250, v250\n"
"v_mfma_f32_16x16x32_bf16 v[232:235], a[40:43], v[200:203], v[232:235]\n"
"ds_read_b64_tr_b16 a[104:105], v25 offset:17056\n"
"ds_read_b64_tr_b16 a[106:107], v25 offset:17312\n"
"v_mfma_f32_16x16x32_bf16 v[232:235], a[44:47], v[216:219], v[232:235]\n"
"v_exp_f32 v251, v251\n"
"v_mfma_f32_16x16x32_bf16 v[236:239], a[44:47], v[220:223], v[20:23]\n"
"ds_read_b64_tr_b16 a[108:109], v25 offset:17568\n"
"v_cvt_pk_bf16_f32 v58, v248, v249\n"
"v_mfma_f32_16x16x32_bf16 v[236:239], a[40:43], v[204:207], v[236:239]\n"
"v_exp_f32 v252, v252\n"
"v_mfma_f32_16x16x32_bf16 v[236:239], a[36:39], v[188:191], v[236:239]\n"
"buffer_load_dwordx4 v0, s[8:11], s59 offen sc1 lds\n"
"v_mfma_f32_16x16x32_bf16 v[236:239], a[32:35], v[172:175], v[236:239]\n"
"ds_read_b64_tr_b16 a[110:111], v25 offset:17824\n"
"v_cvt_pk_bf16_f32 v59, v250, v251\n"
"v_mfma_f32_16x16x32_bf16 v[240:243], a[48:51], v[160:163], v[8:11]\n"
"v_exp_f32 v253, v253\n"
"v_mfma_f32_16x16x32_bf16 v[240:243], a[52:55], v[176:179], v[240:243]\n"
"v_exp_f32 v254, v254\n"
"v_mfma_f32_16x16x32_bf16 v[240:243], a[56:59], v[192:195], v[240:243]\n"
"ds_read_b64_tr_b16 a[112:113], v25 offset:17088\n"
"ds_read_b64_tr_b16 a[114:115], v25 offset:17344\n"
"v_mfma_f32_16x16x32_bf16 v[240:243], a[60:63], v[208:211], v[240:243]\n"
"ds_read_b64_tr_b16 a[116:117], v25 offset:17600\n"
"v_cvt_pk_bf16_f32 v62, v252, v253\n"
"v_mfma_f32_16x16x32_bf16 v[244:247], a[60:63], v[212:215], v[12:15]\n"
"v_exp_f32 v255, v255\n"
"v_mfma_f32_16x16x32_bf16 v[244:247], a[56:59], v[196:199], v[244:247]\n"
"buffer_load_dwordx4 v0, s[8:11], s59 offen offset:1024 sc1 lds\n"
"v_mfma_f32_16x16x32_bf16 v[244:247], a[52:55], v[180:183], v[244:247]\n"
"ds_read_b64_tr_b16 a[118:119], v25 offset:17856\n"
"v_cvt_pk_bf16_f32 v63, v254, v255\n"
"v_mfma_f32_16x16x32_bf16 v[244:247], a[48:51], v[164:167], v[244:247]\n"
"buffer_load_dwordx4 v0, s[8:11], s59 offen offset:2048 sc1 lds\n"
"v_mfma_f32_16x16x32_bf16 v[248:251], a[48:51], v[168:171], v[16:19]\n"
"ds_read_b64_tr_b16 a[120:121], v25 offset:17120\n"
"ds_read_b64_tr_b16 a[122:123], v25 offset:17376\n"
"v_mfma_f32_16x16x32_bf16 v[248:251], a[52:55], v[184:187], v[248:251]\n"
"v_mfma_f32_16x16x32_bf16 v[248:251], a[56:59], v[200:203], v[248:251]\n"
"v_mfma_f32_16x16x32_bf16 v[248:251], a[60:63], v[216:219], v[248:251]\n"
"ds_read_b64_tr_b16 a[124:125], v25 offset:17632\n"
"ds_read_b64_tr_b16 a[126:127], v25 offset:17888\n"
"v_mfma_f32_16x16x32_bf16 v[252:255], a[60:63], v[220:223], v[20:23]\n"
"v_mfma_f32_16x16x32_bf16 v[252:255], a[56:59], v[204:207], v[252:255]\n"
"buffer_load_dwordx4 v0, s[8:11], s59 offen offset:3072 sc1 lds\n"
"v_mfma_f32_16x16x32_bf16 v[252:255], a[52:55], v[188:191], v[252:255]\n"
"v_mfma_f32_16x16x32_bf16 v[252:255], a[48:51], v[172:175], v[252:255]\n"
"v_smfmac_f32_16x16x64_bf16 a[128:131], v[4:7], v[80:87], v2\n"
"v_exp_f32 v128, v128\n"
"v_smfmac_f32_16x16x64_bf16 a[132:135], v[4:7], v[88:95], v2\n"
"v_exp_f32 v129, v129\n"
"v_smfmac_f32_16x16x64_bf16 a[128:131], v[4:7], v[48:55], v2\n"
"v_exp_f32 v130, v130\n"
"v_smfmac_f32_16x16x64_bf16 a[132:135], v[4:7], v[56:63], v2\n"
"s_waitcnt lgkmcnt(0)\n"
"v_add_u32 v25, s61, v25\n"
"v_mfma_f32_16x16x32_bf16 v[96:99], a[64:67], v[80:83], v[96:99]\n"
"s_add_u32 m0, s55, 66560\n"
"ds_read_b128 a[0:3], v24 offset:50688\n"
"v_cvt_pk_bf16_f32 v64, v128, v129\n"
"v_mfma_f32_16x16x32_bf16 v[96:99], a[68:71], v[48:51], v[96:99]\n"
"s_add_u32 s59, s59, 16384\n"
"v_exp_f32 v131, v131\n"
"v_mfma_f32_16x16x32_bf16 v[100:103], a[68:71], v[52:55], v[100:103]\n"
"v_exp_f32 v132, v132\n"
"v_mfma_f32_16x16x32_bf16 v[100:103], a[64:67], v[84:87], v[100:103]\n"
"ds_read_b128 a[4:7], v24 offset:50752\n"
"v_cvt_pk_bf16_f32 v65, v130, v131\n"
"v_mfma_f32_16x16x32_bf16 v[104:107], a[64:67], v[88:91], v[104:107]\n"
"v_exp_f32 v133, v133\n"
"v_mfma_f32_16x16x32_bf16 v[104:107], a[68:71], v[56:59], v[104:107]\n"
"v_exp_f32 v134, v134\n"
"v_mfma_f32_16x16x32_bf16 v[108:111], a[68:71], v[60:63], v[108:111]\n"
"buffer_load_dwordx4 v0, s[8:11], s59 offen sc1 lds\n"
"v_mfma_f32_16x16x32_bf16 v[108:111], a[64:67], v[92:95], v[108:111]\n"
"ds_read_b128 a[8:11], v24 offset:50816\n"
"v_cvt_pk_bf16_f32 v68, v132, v133\n"
"v_mfma_f32_16x16x32_bf16 v[112:115], a[72:75], v[80:83], v[112:115]\n"
"v_exp_f32 v135, v135\n"
"v_mfma_f32_16x16x32_bf16 v[112:115], a[76:79], v[48:51], v[112:115]\n"
"v_exp_f32 v136, v136\n"
"v_mfma_f32_16x16x32_bf16 v[116:119], a[76:79], v[52:55], v[116:119]\n"
"ds_read_b128 a[12:15], v24 offset:50880\n"
"v_cvt_pk_bf16_f32 v69, v134, v135\n"
"v_mfma_f32_16x16x32_bf16 v[116:119], a[72:75], v[84:87], v[116:119]\n"
"v_exp_f32 v137, v137\n"
"v_mfma_f32_16x16x32_bf16 v[120:123], a[72:75], v[88:91], v[120:123]\n"
"v_exp_f32 v138, v138\n"
"v_mfma_f32_16x16x32_bf16 v[120:123], a[76:79], v[56:59], v[120:123]\n"
"ds_read_b128 a[16:19], v24 offset:50944\n"
"v_cvt_pk_bf16_f32 v72, v136, v137\n"
"v_mfma_f32_16x16x32_bf16 v[124:127], a[76:79], v[60:63], v[124:127]\n"
"v_exp_f32 v139, v139\n"
"v_mfma_f32_16x16x32_bf16 v[124:127], a[72:75], v[92:95], v[124:127]\n"
"buffer_load_dwordx4 v0, s[8:11], s59 offen offset:1024 sc1 lds\n"
"v_mfma_f32_16x16x32_bf16 v[32:35], a[80:83], v[80:83], v[32:35]\n"
"v_exp_f32 v140, v140\n"
"v_mfma_f32_16x16x32_bf16 v[32:35], a[84:87], v[48:51], v[32:35]\n"
"ds_read_b128 a[20:23], v24 offset:51008\n"
"v_cvt_pk_bf16_f32 v73, v138, v139\n"
"v_mfma_f32_16x16x32_bf16 v[36:39], a[84:87], v[52:55], v[36:39]\n"
"v_exp_f32 v141, v141\n"
"v_mfma_f32_16x16x32_bf16 v[36:39], a[80:83], v[84:87], v[36:39]\n"
"v_exp_f32 v142, v142\n"
"v_mfma_f32_16x16x32_bf16 v[40:43], a[80:83], v[88:91], v[40:43]\n"
"ds_read_b128 a[24:27], v24 offset:51072\n"
"v_cvt_pk_bf16_f32 v76, v140, v141\n"
"v_mfma_f32_16x16x32_bf16 v[40:43], a[84:87], v[56:59], v[40:43]\n"
"v_exp_f32 v143, v143\n"
"v_mfma_f32_16x16x32_bf16 v[44:47], a[84:87], v[60:63], v[44:47]\n"
"v_exp_f32 v144, v144\n"
"v_mfma_f32_16x16x32_bf16 v[44:47], a[80:83], v[92:95], v[44:47]\n"
"buffer_load_dwordx4 v0, s[8:11], s59 offen offset:2048 sc1 lds\n"
"v_mfma_f32_16x16x32_bf16 a[144:147], a[88:91], v[80:83], a[144:147]\n"
"ds_read_b128 a[28:31], v24 offset:51136\n"
"v_cvt_pk_bf16_f32 v77, v142, v143\n"
"v_mfma_f32_16x16x32_bf16 a[144:147], a[92:95], v[48:51], a[144:147]\n"
"v_exp_f32 v145, v145\n"
"v_mfma_f32_16x16x32_bf16 a[148:151], a[92:95], v[52:55], a[148:151]\n"
"v_exp_f32 v146, v146\n"
"v_mfma_f32_16x16x32_bf16 a[148:151], a[88:91], v[84:87], a[148:151]\n"
"ds_read_b128 a[32:35], v24 offset:51200\n"
"v_cvt_pk_bf16_f32 v66, v144, v145\n"
"v_mfma_f32_16x16x32_bf16 a[152:155], a[88:91], v[88:91], a[152:155]\n"
"v_exp_f32 v147, v147\n"
"v_mfma_f32_16x16x32_bf16 a[152:155], a[92:95], v[56:59], a[152:155]\n"
"v_exp_f32 v148, v148\n"
"v_mfma_f32_16x16x32_bf16 a[156:159], a[92:95], v[60:63], a[156:159]\n"
"buffer_load_dwordx4 v0, s[8:11], s59 offen offset:3072 sc1 lds\n"
"v_mfma_f32_16x16x32_bf16 a[156:159], a[88:91], v[92:95], a[156:159]\n"
"s_add_u32 m0, s55, 107520\n"
"ds_read_b128 a[36:39], v24 offset:51264\n"
"v_cvt_pk_bf16_f32 v67, v146, v147\n"
"v_mfma_f32_16x16x32_bf16 a[160:163], a[96:99], v[80:83], a[160:163]\n"
"s_add_u32 s59, s59, 16384\n"
"v_exp_f32 v149, v149\n"
"v_mfma_f32_16x16x32_bf16 a[160:163], a[100:103], v[48:51], a[160:163]\n"
"v_exp_f32 v150, v150\n"
"v_mfma_f32_16x16x32_bf16 a[164:167], a[100:103], v[52:55], a[164:167]\n"
"ds_read_b128 a[40:43], v24 offset:51328\n"
"v_cvt_pk_bf16_f32 v70, v148, v149\n"
"v_mfma_f32_16x16x32_bf16 a[164:167], a[96:99], v[84:87], a[164:167]\n"
"v_exp_f32 v151, v151\n"
"v_mfma_f32_16x16x32_bf16 a[168:171], a[96:99], v[88:91], a[168:171]\n"
"v_exp_f32 v152, v152\n"
"v_mfma_f32_16x16x32_bf16 a[168:171], a[100:103], v[56:59], a[168:171]\n"
"buffer_load_dwordx4 v0, s[8:11], s59 offen sc1 lds\n"
"v_mfma_f32_16x16x32_bf16 a[172:175], a[100:103], v[60:63], a[172:175]\n"
"ds_read_b128 a[44:47], v24 offset:51392\n"
"v_cvt_pk_bf16_f32 v71, v150, v151\n"
"v_mfma_f32_16x16x32_bf16 a[172:175], a[96:99], v[92:95], a[172:175]\n"
"v_exp_f32 v153, v153\n"
"v_mfma_f32_16x16x32_bf16 a[176:179], a[104:107], v[80:83], a[176:179]\n"
"v_exp_f32 v154, v154\n"
"v_mfma_f32_16x16x32_bf16 a[176:179], a[108:111], v[48:51], a[176:179]\n"
"s_add_u32 s33, s91, 278528\n"
"ds_read_b128 a[48:51], v24 offset:51456\n"
"v_cvt_pk_bf16_f32 v74, v152, v153\n"
"v_mfma_f32_16x16x32_bf16 a[180:183], a[108:111], v[52:55], a[180:183]\n"
"v_exp_f32 v155, v155\n"
"v_mfma_f32_16x16x32_bf16 a[180:183], a[104:107], v[84:87], a[180:183]\n"
"v_exp_f32 v156, v156\n"
"v_mfma_f32_16x16x32_bf16 a[184:187], a[104:107], v[88:91], a[184:187]\n"
"ds_read_b128 a[52:55], v24 offset:51520\n"
"v_cvt_pk_bf16_f32 v75, v154, v155\n"
"v_mfma_f32_16x16x32_bf16 a[184:187], a[108:111], v[56:59], a[184:187]\n"
"v_exp_f32 v157, v157\n"
"v_mfma_f32_16x16x32_bf16 a[188:191], a[108:111], v[60:63], a[188:191]\n"
"buffer_load_dwordx4 v0, s[8:11], s59 offen offset:1024 sc1 lds\n"
"buffer_load_dword v3, v1, s[8:11], s33 offen sc1\n"
"v_mfma_f32_16x16x32_bf16 a[188:191], a[104:107], v[92:95], a[188:191]\n"
"v_exp_f32 v158, v158\n"
"v_mfma_f32_16x16x32_bf16 a[192:195], a[112:115], v[80:83], a[192:195]\n"
"ds_read_b128 a[56:59], v24 offset:51584\n"
"v_cvt_pk_bf16_f32 v78, v156, v157\n"
"v_mfma_f32_16x16x32_bf16 a[192:195], a[116:119], v[48:51], a[192:195]\n"
"v_exp_f32 v159, v159\n"
"v_mfma_f32_16x16x32_bf16 a[196:199], a[116:119], v[52:55], a[196:199]\n"
"v_exp_f32 v224, v224\n"
"v_mfma_f32_16x16x32_bf16 a[196:199], a[112:115], v[84:87], a[196:199]\n"
"ds_read_b128 a[60:63], v24 offset:51648\n"
"v_cvt_pk_bf16_f32 v79, v158, v159\n"
"v_mfma_f32_16x16x32_bf16 a[200:203], a[112:115], v[88:91], a[200:203]\n"
"v_exp_f32 v225, v225\n"
"v_mfma_f32_16x16x32_bf16 a[200:203], a[116:119], v[56:59], a[200:203]\n"
"v_exp_f32 v226, v226\n"
"v_mfma_f32_16x16x32_bf16 a[204:207], a[116:119], v[60:63], a[204:207]\n"
"buffer_load_dwordx4 v0, s[8:11], s59 offen offset:2048 sc1 lds\n"
"v_add_u32 v24, s61, v24\n"
"v_mfma_f32_16x16x32_bf16 a[204:207], a[112:115], v[92:95], a[204:207]\n"
"v_exp_f32 v227, v227\n"
"v_mfma_f32_16x16x32_bf16 a[208:211], a[120:123], v[80:83], a[208:211]\n"
"v_exp_f32 v228, v228\n"
"v_mfma_f32_16x16x32_bf16 a[208:211], a[124:127], v[48:51], a[208:211]\n"
"buffer_load_dwordx4 v0, s[8:11], s59 offen offset:3072 sc1 lds\n"
"v_mfma_f32_16x16x32_bf16 a[212:215], a[124:127], v[52:55], a[212:215]\n"
"v_exp_f32 v229, v229\n"
"v_mfma_f32_16x16x32_bf16 a[212:215], a[120:123], v[84:87], a[212:215]\n"
"v_exp_f32 v230, v230\n"
"v_mfma_f32_16x16x32_bf16 a[216:219], a[120:123], v[88:91], a[216:219]\n"
"v_exp_f32 v231, v231\n"
"v_mfma_f32_16x16x32_bf16 a[216:219], a[124:127], v[56:59], a[216:219]\n"
"v_mfma_f32_16x16x32_bf16 a[220:223], a[124:127], v[60:63], a[220:223]\n"
"s_waitcnt lgkmcnt(0)\n"
"v_mfma_f32_16x16x32_bf16 a[220:223], a[120:123], v[92:95], a[220:223]\n"
"L_z:\n"
"v_mfma_f32_16x16x32_bf16 v[128:131], a[0:3], v[160:163], v[8:11]\n"
"s_waitcnt vmcnt(30)\n"
"v_exp_f32 v232, v232\n"
"v_mfma_f32_16x16x32_bf16 v[128:131], a[4:7], v[176:179], v[128:131]\n"
"s_barrier\n"
"v_exp_f32 v233, v233\n"
"v_mfma_f32_16x16x32_bf16 v[128:131], a[8:11], v[192:195], v[128:131]\n"
"s_add_u32 m0, s55, 90624\n"
"ds_read_b64_tr_b16 a[64:65], v25 offset:0\n"
"ds_read_b64_tr_b16 a[66:67], v25 offset:256\n"
"v_mfma_f32_16x16x32_bf16 v[128:131], a[12:15], v[208:211], v[128:131]\n"
"s_add_u32 s59, s59, 16384\n"
"ds_read_b64_tr_b16 a[68:69], v25 offset:512\n"
"v_cvt_pk_bf16_f32 v48, v224, v225\n"
"v_mfma_f32_16x16x32_bf16 v[132:135], a[12:15], v[212:215], v[12:15]\n"
"s_lshl_b32 s35, s35, 15\n"
"buffer_load_dwordx4 v0, s[8:11], s59 offen sc1 lds\n"
"v_exp_f32 v234, v234\n"
"v_mfma_f32_16x16x32_bf16 v[132:135], a[8:11], v[196:199], v[132:135]\n"
"s_add_u32 s58, s35, 0x400000\n"
"buffer_load_dwordx4 v0, s[8:11], s59 offen offset:1024 sc1 lds\n"
"v_mfma_f32_16x16x32_bf16 v[132:135], a[4:7], v[180:183], v[132:135]\n"
"s_add_u32 s63, s58, 131072\n"
"ds_read_b64_tr_b16 a[70:71], v25 offset:768\n"
"v_cvt_pk_bf16_f32 v49, v226, v227\n"
"v_mfma_f32_16x16x32_bf16 v[132:135], a[0:3], v[164:167], v[132:135]\n"
"s_mul_i32 s34, s34, 36864\n"
"v_exp_f32 v235, v235\n"
"v_mfma_f32_16x16x32_bf16 v[136:139], a[0:3], v[168:171], v[16:19]\n"
"s_mul_i32 s35, s65, 288\n"
"v_exp_f32 v236, v236\n"
"v_mfma_f32_16x16x32_bf16 v[136:139], a[4:7], v[184:187], v[136:139]\n"
"s_add_u32 s34, s34, s35\n"
"ds_read_b64_tr_b16 a[72:73], v25 offset:32\n"
"ds_read_b64_tr_b16 a[74:75], v25 offset:288\n"
"v_mfma_f32_16x16x32_bf16 v[136:139], a[8:11], v[200:203], v[136:139]\n"
"s_lshl_b32 s35, s64, 6\n"
"ds_read_b64_tr_b16 a[76:77], v25 offset:544\n"
"v_cvt_pk_bf16_f32 v52, v228, v229\n"
"v_mfma_f32_16x16x32_bf16 v[136:139], a[12:15], v[216:219], v[136:139]\n"
"s_add_u32 s98, s34, s35\n"
"v_exp_f32 v237, v237\n"
"v_mfma_f32_16x16x32_bf16 v[140:143], a[12:15], v[220:223], v[20:23]\n"
"s_add_u32 s99, s98, 18432\n"
"ds_read_b64_tr_b16 a[78:79], v25 offset:800\n"
"v_cvt_pk_bf16_f32 v53, v230, v231\n"
"v_mfma_f32_16x16x32_bf16 v[140:143], a[8:11], v[204:207], v[140:143]\n"
"s_xor_b32 s35, s64, s52\n"
"v_exp_f32 v238, v238\n"
"v_mfma_f32_16x16x32_bf16 v[140:143], a[4:7], v[188:191], v[140:143]\n"
"s_lshl_b32 s35, s35, 28\n"
"buffer_load_dwordx4 v0, s[8:11], s59 offen offset:2048 sc1 lds\n"
"v_mfma_f32_16x16x32_bf16 v[140:143], a[0:3], v[172:175], v[140:143]\n"
"s_add_u32 s34, s34, s35\n"
"v_exp_f32 v239, v239\n"
"v_mfma_f32_16x16x32_bf16 v[144:147], a[16:19], v[160:163], v[8:11]\n"
"s_add_u32 s62, s34, 256\n"
"buffer_load_dwordx4 v0, s[8:11], s59 offen offset:3072 sc1 lds\n"
"v_mfma_f32_16x16x32_bf16 v[144:147], a[20:23], v[176:179], v[144:147]\n"
"s_add_u32 s65, s34, 18688\n"
"ds_read_b64_tr_b16 a[80:81], v25 offset:64\n"
"v_cvt_pk_bf16_f32 v56, v232, v233\n"
"v_mfma_f32_16x16x32_bf16 v[144:147], a[24:27], v[192:195], v[144:147]\n"
"s_andn2_b32 s34, s6, 6\n"
"v_exp_f32 v240, v240\n"
"v_mfma_f32_16x16x32_bf16 v[144:147], a[28:31], v[208:211], v[144:147]\n"
"s_lshl2_add_u32 s34, s34, s52\n"
"ds_read_b64_tr_b16 a[82:83], v25 offset:320\n"
"v_cvt_pk_bf16_f32 v57, v234, v235\n"
"v_mfma_f32_16x16x32_bf16 v[148:151], a[28:31], v[212:215], v[12:15]\n"
"s_lshl_b32 s34, s34, 15\n"
"v_exp_f32 v241, v241\n"
"v_mfma_f32_16x16x32_bf16 v[148:151], a[24:27], v[196:199], v[148:151]\n"
"s_add_u32 m0, s55, 124416\n"
"ds_read_b64_tr_b16 a[84:85], v25 offset:576\n"
"ds_read_b64_tr_b16 a[86:87], v25 offset:832\n"
"v_mfma_f32_16x16x32_bf16 v[148:151], a[20:23], v[180:183], v[148:151]\n"
"s_add_u32 s59, s59, 16384\n"
"v_exp_f32 v242, v242\n"
"v_mfma_f32_16x16x32_bf16 v[148:151], a[16:19], v[164:167], v[148:151]\n"
"s_add_u32 s34, s34, 0x3400000\n"
"buffer_load_dwordx4 v0, s[8:11], s59 offen sc1 lds\n"
"v_mfma_f32_16x16x32_bf16 v[152:155], a[16:19], v[168:171], v[16:19]\n"
"s_mov_b32 s60, 4096\n"
"ds_read_b64_tr_b16 a[88:89], v25 offset:96\n"
"v_cvt_pk_bf16_f32 v60, v236, v237\n"
"v_mfma_f32_16x16x32_bf16 v[152:155], a[20:23], v[184:187], v[152:155]\n"
"s_mov_b32 s61, 8192\n"
"v_exp_f32 v243, v243\n"
"v_mfma_f32_16x16x32_bf16 v[152:155], a[24:27], v[200:203], v[152:155]\n"
"s_mov_b32 s80, 12288\n"
"ds_read_b64_tr_b16 a[90:91], v25 offset:352\n"
"v_cvt_pk_bf16_f32 v61, v238, v239\n"
"v_mfma_f32_16x16x32_bf16 v[152:155], a[28:31], v[216:219], v[152:155]\n"
"s_mov_b32 s81, 16384\n"
"buffer_load_dwordx4 v0, s[8:11], s59 offen offset:1024 sc1 lds\n"
"v_exp_f32 v244, v244\n"
"v_mfma_f32_16x16x32_bf16 v[156:159], a[28:31], v[220:223], v[20:23]\n"
"s_add_u32 s35, s64, 1\n"
"buffer_load_dwordx4 v0, s[8:11], s59 offen offset:2048 sc1 lds\n"
"v_mfma_f32_16x16x32_bf16 v[156:159], a[24:27], v[204:207], v[156:159]\n"
"s_and_b32 s35, s35, 3\n"
"v_exp_f32 v245, v245\n"
"v_mfma_f32_16x16x32_bf16 v[156:159], a[20:23], v[188:191], v[156:159]\n"
"s_lshl_b32 s35, s35, 18\n"
"ds_read_b64_tr_b16 a[92:93], v25 offset:608\n"
"v_cvt_pk_bf16_f32 v50, v240, v241\n"
"v_mfma_f32_16x16x32_bf16 v[156:159], a[16:19], v[172:175], v[156:159]\n"
"s_add_u32 s35, s35, s34\n"
"v_exp_f32 v246, v246\n"
"v_mfma_f32_16x16x32_bf16 v[224:227], a[32:35], v[160:163], v[8:11]\n"
"s_add_u32 s86, s35, 0\n"
"ds_read_b64_tr_b16 a[94:95], v25 offset:864\n"
"ds_read_b64_tr_b16 a[96:97], v25 offset:128\n"
"v_mfma_f32_16x16x32_bf16 v[224:227], a[36:39], v[176:179], v[224:227]\n"
"s_add_u32 s35, s64, 2\n"
"ds_read_b64_tr_b16 a[98:99], v25 offset:384\n"
"v_cvt_pk_bf16_f32 v51, v242, v243\n"
"v_mfma_f32_16x16x32_bf16 v[224:227], a[40:43], v[192:195], v[224:227]\n"
"s_add_u32 s56, s56, 4096\n"
"v_exp_f32 v247, v247\n"
"v_mfma_f32_16x16x32_bf16 v[224:227], a[44:47], v[208:211], v[224:227]\n"
"s_and_b32 s35, s35, 3\n"
"buffer_load_dwordx4 a[0:3], v0, s[8:11], s56 offen offset:0 sc1\n"
"v_mfma_f32_16x16x32_bf16 v[228:231], a[44:47], v[212:215], v[12:15]\n"
"s_lshl_b32 s35, s35, 18\n"
"v_exp_f32 v248, v248\n"
"v_mfma_f32_16x16x32_bf16 v[228:231], a[40:43], v[196:199], v[228:231]\n"
"s_add_u32 s35, s35, s34\n"
"ds_read_b64_tr_b16 a[100:101], v25 offset:640\n"
"v_cvt_pk_bf16_f32 v54, v244, v245\n"
"v_mfma_f32_16x16x32_bf16 v[228:231], a[36:39], v[180:183], v[228:231]\n"
"s_add_u32 s87, s35, 2048\n"
"v_exp_f32 v249, v249\n"
"v_mfma_f32_16x16x32_bf16 v[228:231], a[32:35], v[164:167], v[228:231]\n"
"s_add_u32 s35, s64, 3\n"
"buffer_load_dwordx4 a[8:11], v0, s[8:11], s56 offen offset:1024 sc1\n"
"v_mfma_f32_16x16x32_bf16 v[232:235], a[32:35], v[168:171], v[16:19]\n"
"s_and_b32 s35, s35, 3\n"
"ds_read_b64_tr_b16 a[102:103], v25 offset:896\n"
"v_cvt_pk_bf16_f32 v55, v246, v247\n"
"v_mfma_f32_16x16x32_bf16 v[232:235], a[36:39], v[184:187], v[232:235]\n"
"s_lshl_b32 s35, s35, 18\n"
"v_exp_f32 v250, v250\n"
"v_mfma_f32_16x16x32_bf16 v[232:235], a[40:43], v[200:203], v[232:235]\n"
"s_add_u32 s35, s35, s34\n"
"v_exp_f32 v251, v251\n"
"v_mfma_f32_16x16x32_bf16 v[232:235], a[44:47], v[216:219], v[232:235]\n"
"s_add_u32 s88, s35, 4096\n"
"ds_read_b64_tr_b16 a[104:105], v25 offset:160\n"
"ds_read_b64_tr_b16 a[106:107], v25 offset:416\n"
"v_mfma_f32_16x16x32_bf16 v[236:239], a[44:47], v[220:223], v[20:23]\n"
"s_lshl_b32 s35, s64, 10\n"
"ds_read_b64_tr_b16 a[108:109], v25 offset:672\n"
"v_cvt_pk_bf16_f32 v58, v248, v249\n"
"v_mfma_f32_16x16x32_bf16 v[236:239], a[40:43], v[204:207], v[236:239]\n"
"s_add_u32 s34, s34, s35\n"
"v_exp_f32 v252, v252\n"
"v_mfma_f32_16x16x32_bf16 v[236:239], a[36:39], v[188:191], v[236:239]\n"
"s_lshl_b32 s35, s52, 18\n"
"buffer_load_dwordx4 a[16:19], v0, s[8:11], s56 offen offset:2048 sc1\n"
"v_mfma_f32_16x16x32_bf16 v[236:239], a[32:35], v[172:175], v[236:239]\n"
"s_add_u32 s34, s34, s35\n"
"ds_read_b64_tr_b16 a[110:111], v25 offset:928\n"
"v_cvt_pk_bf16_f32 v59, v250, v251\n"
"v_mfma_f32_16x16x32_bf16 v[240:243], a[48:51], v[160:163], v[8:11]\n"
"s_add_u32 s89, s34, 12288\n"
"v_exp_f32 v253, v253\n"
"v_mfma_f32_16x16x32_bf16 v[240:243], a[52:55], v[176:179], v[240:243]\n"
"s_lshr_b32 s34, s6, 5\n"
"v_exp_f32 v254, v254\n"
"v_mfma_f32_16x16x32_bf16 v[240:243], a[56:59], v[192:195], v[240:243]\n"
"s_lshl2_add_u32 s34, s34, s64\n"
"ds_read_b64_tr_b16 a[112:113], v25 offset:192\n"
"ds_read_b64_tr_b16 a[114:115], v25 offset:448\n"
"v_mfma_f32_16x16x32_bf16 v[240:243], a[60:63], v[208:211], v[240:243]\n"
"s_lshl_b32 s34, s34, 6\n"
"ds_read_b64_tr_b16 a[116:117], v25 offset:704\n"
"v_cvt_pk_bf16_f32 v62, v252, v253\n"
"v_mfma_f32_16x16x32_bf16 v[244:247], a[60:63], v[212:215], v[12:15]\n"
"s_add_u32 s64, s34, 8192\n"
"buffer_load_dwordx4 a[24:27], v0, s[8:11], s56 offen offset:3072 sc1\n"
"v_exp_f32 v255, v255\n"
"v_mfma_f32_16x16x32_bf16 v[244:247], a[56:59], v[196:199], v[244:247]\n"
"buffer_load_dwordx4 v0, s[8:11], s59 offen offset:3072 sc1 lds\n"
"v_mfma_f32_16x16x32_bf16 v[244:247], a[52:55], v[180:183], v[244:247]\n"
"ds_read_b64_tr_b16 a[118:119], v25 offset:960\n"
"v_cvt_pk_bf16_f32 v63, v254, v255\n"
"v_mfma_f32_16x16x32_bf16 v[244:247], a[48:51], v[164:167], v[244:247]\n"
"v_mfma_f32_16x16x32_bf16 v[248:251], a[48:51], v[168:171], v[16:19]\n"
"s_add_u32 s57, s57, 4096\n"
"ds_read_b64_tr_b16 a[120:121], v25 offset:224\n"
"ds_read_b64_tr_b16 a[122:123], v25 offset:480\n"
"v_mfma_f32_16x16x32_bf16 v[248:251], a[52:55], v[184:187], v[248:251]\n"
"buffer_load_dwordx4 a[4:7], v0, s[8:11], s57 offen offset:0 sc1\n"
"v_mfma_f32_16x16x32_bf16 v[248:251], a[56:59], v[200:203], v[248:251]\n"
"v_mfma_f32_16x16x32_bf16 v[248:251], a[60:63], v[216:219], v[248:251]\n"
"ds_read_b64_tr_b16 a[124:125], v25 offset:736\n"
"ds_read_b64_tr_b16 a[126:127], v25 offset:992\n"
"v_mfma_f32_16x16x32_bf16 v[252:255], a[60:63], v[220:223], v[20:23]\n"
"buffer_load_dwordx4 a[12:15], v0, s[8:11], s57 offen offset:1024 sc1\n"
"v_mfma_f32_16x16x32_bf16 v[252:255], a[56:59], v[204:207], v[252:255]\n"
"v_mfma_f32_16x16x32_bf16 v[252:255], a[52:55], v[188:191], v[252:255]\n"
"v_mfma_f32_16x16x32_bf16 v[252:255], a[48:51], v[172:175], v[252:255]\n"
"v_smfmac_f32_16x16x64_bf16 a[128:131], v[4:7], v[64:71], v2\n"
"v_exp_f32 v128, v128\n"
"v_smfmac_f32_16x16x64_bf16 a[132:135], v[4:7], v[72:79], v2\n"
"v_exp_f32 v129, v129\n"
"v_smfmac_f32_16x16x64_bf16 a[128:131], v[4:7], v[48:55], v2\n"
"v_exp_f32 v130, v130\n"
"v_smfmac_f32_16x16x64_bf16 a[132:135], v[4:7], v[56:63], v2\n"
"s_waitcnt lgkmcnt(0)\n"
"buffer_load_dwordx4 a[20:23], v0, s[8:11], s57 offen offset:2048 sc1\n"
"v_add_u32 v1, 0x10000, v0\n"
"v_mfma_f32_16x16x32_bf16 v[96:99], a[64:67], v[64:67], v[96:99]\n"
"v_cvt_pk_bf16_f32 v80, v128, v129\n"
"v_mfma_f32_16x16x32_bf16 v[96:99], a[68:71], v[48:51], v[96:99]\n"
"v_exp_f32 v131, v131\n"
"v_mfma_f32_16x16x32_bf16 v[100:103], a[68:71], v[52:55], v[100:103]\n"
"v_exp_f32 v132, v132\n"
"v_mfma_f32_16x16x32_bf16 v[100:103], a[64:67], v[68:71], v[100:103]\n"
"buffer_load_dwordx4 a[28:31], v0, s[8:11], s57 offen offset:3072 sc1\n"
"v_cvt_pk_bf16_f32 v81, v130, v131\n"
"v_mfma_f32_16x16x32_bf16 v[104:107], a[64:67], v[72:75], v[104:107]\n"
"v_exp_f32 v133, v133\n"
"v_mfma_f32_16x16x32_bf16 v[104:107], a[68:71], v[56:59], v[104:107]\n"
"s_add_u32 s56, s56, 4096\n"
"v_exp_f32 v134, v134\n"
"v_mfma_f32_16x16x32_bf16 v[108:111], a[68:71], v[60:63], v[108:111]\n"
"buffer_load_dwordx4 a[32:35], v0, s[8:11], s56 offen offset:0 sc1\n"
"v_mfma_f32_16x16x32_bf16 v[108:111], a[64:67], v[76:79], v[108:111]\n"
"v_cvt_pk_bf16_f32 v84, v132, v133\n"
"v_mfma_f32_16x16x32_bf16 v[112:115], a[72:75], v[64:67], v[112:115]\n"
"v_exp_f32 v135, v135\n"
"v_mfma_f32_16x16x32_bf16 v[112:115], a[76:79], v[48:51], v[112:115]\n"
"v_exp_f32 v136, v136\n"
"v_mfma_f32_16x16x32_bf16 v[116:119], a[76:79], v[52:55], v[116:119]\n"
"v_cvt_pk_bf16_f32 v85, v134, v135\n"
"v_mfma_f32_16x16x32_bf16 v[116:119], a[72:75], v[68:71], v[116:119]\n"
"v_exp_f32 v137, v137\n"
"v_mfma_f32_16x16x32_bf16 v[120:123], a[72:75], v[72:75], v[120:123]\n"
"v_exp_f32 v138, v138\n"
"v_mfma_f32_16x16x32_bf16 v[120:123], a[76:79], v[56:59], v[120:123]\n"
"buffer_load_dwordx4 a[40:43], v0, s[8:11], s56 offen offset:1024 sc1\n"
"v_cvt_pk_bf16_f32 v88, v136, v137\n"
"v_mfma_f32_16x16x32_bf16 v[124:127], a[76:79], v[60:63], v[124:127]\n"
"v_exp_f32 v139, v139\n"
"v_mfma_f32_16x16x32_bf16 v[124:127], a[72:75], v[76:79], v[124:127]\n"
"buffer_load_dwordx4 a[48:51], v0, s[8:11], s56 offen offset:2048 sc1\n"
"v_mfma_f32_16x16x32_bf16 v[32:35], a[80:83], v[64:67], v[32:35]\n"
"v_exp_f32 v140, v140\n"
"v_mfma_f32_16x16x32_bf16 v[32:35], a[84:87], v[48:51], v[32:35]\n"
"v_cvt_pk_bf16_f32 v89, v138, v139\n"
"v_mfma_f32_16x16x32_bf16 v[36:39], a[84:87], v[52:55], v[36:39]\n"
"v_exp_f32 v141, v141\n"
"v_mfma_f32_16x16x32_bf16 v[36:39], a[80:83], v[68:71], v[36:39]\n"
"v_exp_f32 v142, v142\n"
"v_mfma_f32_16x16x32_bf16 v[40:43], a[80:83], v[72:75], v[40:43]\n"
"buffer_load_dwordx4 a[56:59], v0, s[8:11], s56 offen offset:3072 sc1\n"
"v_cvt_pk_bf16_f32 v92, v140, v141\n"
"v_mfma_f32_16x16x32_bf16 v[40:43], a[84:87], v[56:59], v[40:43]\n"
"v_exp_f32 v143, v143\n"
"v_mfma_f32_16x16x32_bf16 v[44:47], a[84:87], v[60:63], v[44:47]\n"
"v_exp_f32 v144, v144\n"
"v_mfma_f32_16x16x32_bf16 v[44:47], a[80:83], v[76:79], v[44:47]\n"
"s_add_u32 s57, s57, 4096\n"
"v_mfma_f32_16x16x32_bf16 a[144:147], a[88:91], v[64:67], a[144:147]\n"
"buffer_load_dwordx4 a[36:39], v0, s[8:11], s57 offen offset:0 sc1\n"
"v_cvt_pk_bf16_f32 v93, v142, v143\n"
"v_mfma_f32_16x16x32_bf16 a[144:147], a[92:95], v[48:51], a[144:147]\n"
"v_exp_f32 v145, v145\n"
"v_mfma_f32_16x16x32_bf16 a[148:151], a[92:95], v[52:55], a[148:151]\n"
"v_exp_f32 v146, v146\n"
"v_mfma_f32_16x16x32_bf16 a[148:151], a[88:91], v[68:71], a[148:151]\n"
"v_cvt_pk_bf16_f32 v82, v144, v145\n"
"v_mfma_f32_16x16x32_bf16 a[152:155], a[88:91], v[72:75], a[152:155]\n"
"v_exp_f32 v147, v147\n"
"v_mfma_f32_16x16x32_bf16 a[152:155], a[92:95], v[56:59], a[152:155]\n"
"v_exp_f32 v148, v148\n"
"v_mfma_f32_16x16x32_bf16 a[156:159], a[92:95], v[60:63], a[156:159]\n"
"buffer_load_dwordx4 a[44:47], v0, s[8:11], s57 offen offset:1024 sc1\n"
"v_mfma_f32_16x16x32_bf16 a[156:159], a[88:91], v[76:79], a[156:159]\n"
"v_cvt_pk_bf16_f32 v83, v146, v147\n"
"v_mfma_f32_16x16x32_bf16 a[160:163], a[96:99], v[64:67], a[160:163]\n"
"v_exp_f32 v149, v149\n"
"v_mfma_f32_16x16x32_bf16 a[160:163], a[100:103], v[48:51], a[160:163]\n"
"v_exp_f32 v150, v150\n"
"v_mfma_f32_16x16x32_bf16 a[164:167], a[100:103], v[52:55], a[164:167]\n"
"buffer_load_dwordx4 a[52:55], v0, s[8:11], s57 offen offset:2048 sc1\n"
"v_cvt_pk_bf16_f32 v86, v148, v149\n"
"v_mfma_f32_16x16x32_bf16 a[164:167], a[96:99], v[68:71], a[164:167]\n"
"v_exp_f32 v151, v151\n"
"v_mfma_f32_16x16x32_bf16 a[168:171], a[96:99], v[72:75], a[168:171]\n"
"v_exp_f32 v152, v152\n"
"v_mfma_f32_16x16x32_bf16 a[168:171], a[100:103], v[56:59], a[168:171]\n"
"v_mfma_f32_16x16x32_bf16 a[172:175], a[100:103], v[60:63], a[172:175]\n"
"buffer_load_dwordx4 a[60:63], v0, s[8:11], s57 offen offset:3072 sc1\n"
"v_cvt_pk_bf16_f32 v87, v150, v151\n"
"v_mfma_f32_16x16x32_bf16 a[172:175], a[96:99], v[76:79], a[172:175]\n"
"v_exp_f32 v153, v153\n"
"v_mfma_f32_16x16x32_bf16 a[176:179], a[104:107], v[64:67], a[176:179]\n"
"v_exp_f32 v154, v154\n"
"v_mfma_f32_16x16x32_bf16 a[176:179], a[108:111], v[48:51], a[176:179]\n"
"ds_read_b64_tr_b16 a[96:97], v25 offset:17024\n"
"v_cvt_pk_bf16_f32 v90, v152, v153\n"
"v_mfma_f32_16x16x32_bf16 a[180:183], a[108:111], v[52:55], a[180:183]\n"
"v_exp_f32 v155, v155\n"
"v_mfma_f32_16x16x32_bf16 a[180:183], a[104:107], v[68:71], a[180:183]\n"
"v_exp_f32 v156, v156\n"
"v_mfma_f32_16x16x32_bf16 a[184:187], a[104:107], v[72:75], a[184:187]\n"
"ds_read_b64_tr_b16 a[98:99], v25 offset:17280\n"
"v_cvt_pk_bf16_f32 v91, v154, v155\n"
"v_mfma_f32_16x16x32_bf16 a[184:187], a[108:111], v[56:59], a[184:187]\n"
"v_exp_f32 v157, v157\n"
"v_mfma_f32_16x16x32_bf16 a[188:191], a[108:111], v[60:63], a[188:191]\n"
"s_add_u32 s56, s56, 4096\n"
"ds_read_b64_tr_b16 a[80:81], v25 offset:16960\n"
"ds_read_b64_tr_b16 a[82:83], v25 offset:17216\n"
"v_mfma_f32_16x16x32_bf16 a[188:191], a[104:107], v[76:79], a[188:191]\n"
"buffer_load_dwordx4 v[160:163], v0, s[8:11], s56 offen offset:0 sc1\n"
"v_exp_f32 v158, v158\n"
"v_mfma_f32_16x16x32_bf16 a[192:195], a[112:115], v[64:67], a[192:195]\n"
"ds_read_b64_tr_b16 a[88:89], v25 offset:16992\n"
"v_cvt_pk_bf16_f32 v94, v156, v157\n"
"v_mfma_f32_16x16x32_bf16 a[192:195], a[116:119], v[48:51], a[192:195]\n"
"v_exp_f32 v159, v159\n"
"v_mfma_f32_16x16x32_bf16 a[196:199], a[116:119], v[52:55], a[196:199]\n"
"v_exp_f32 v224, v224\n"
"v_mfma_f32_16x16x32_bf16 a[196:199], a[112:115], v[68:71], a[196:199]\n"
"ds_read_b64_tr_b16 a[90:91], v25 offset:17248\n"
"v_cvt_pk_bf16_f32 v95, v158, v159\n"
"v_mfma_f32_16x16x32_bf16 a[200:203], a[112:115], v[72:75], a[200:203]\n"
"buffer_load_dwordx4 v[168:171], v0, s[8:11], s56 offen offset:1024 sc1\n"
"v_exp_f32 v225, v225\n"
"v_mfma_f32_16x16x32_bf16 a[200:203], a[116:119], v[56:59], a[200:203]\n"
"v_exp_f32 v226, v226\n"
"v_mfma_f32_16x16x32_bf16 a[204:207], a[116:119], v[60:63], a[204:207]\n"
"ds_read_b64_tr_b16 a[104:105], v25 offset:17056\n"
"ds_read_b64_tr_b16 a[106:107], v25 offset:17312\n"
"v_mfma_f32_16x16x32_bf16 a[204:207], a[112:115], v[76:79], a[204:207]\n"
"v_exp_f32 v227, v227\n"
"v_mfma_f32_16x16x32_bf16 a[208:211], a[120:123], v[64:67], a[208:211]\n"
"buffer_load_dwordx4 v[176:179], v0, s[8:11], s56 offen offset:2048 sc1\n"
"v_exp_f32 v228, v228\n"
"v_mfma_f32_16x16x32_bf16 a[208:211], a[124:127], v[48:51], a[208:211]\n"
"ds_read_b64_tr_b16 a[64:65], v25 offset:16896\n"
"ds_read_b64_tr_b16 a[66:67], v25 offset:17152\n"
"v_mfma_f32_16x16x32_bf16 a[212:215], a[124:127], v[52:55], a[212:215]\n"
"v_exp_f32 v229, v229\n"
"v_mfma_f32_16x16x32_bf16 a[212:215], a[120:123], v[68:71], a[212:215]\n"
"v_exp_f32 v230, v230\n"
"v_mfma_f32_16x16x32_bf16 a[216:219], a[120:123], v[72:75], a[216:219]\n"
"v_exp_f32 v231, v231\n"
"v_mfma_f32_16x16x32_bf16 a[216:219], a[124:127], v[56:59], a[216:219]\n"
"ds_read_b64_tr_b16 a[72:73], v25 offset:16928\n"
"ds_read_b64_tr_b16 a[74:75], v25 offset:17184\n"
"v_mfma_f32_16x16x32_bf16 a[220:223], a[124:127], v[60:63], a[220:223]\n"
"ds_read_b64_tr_b16 a[112:113], v25 offset:17088\n"
"ds_read_b64_tr_b16 a[114:115], v25 offset:17344\n"
"v_mfma_f32_16x16x32_bf16 a[220:223], a[120:123], v[76:79], a[220:223]\n"
"ds_read_b64_tr_b16 a[100:101], v25 offset:17536\n"
"ds_read_b64_tr_b16 a[102:103], v25 offset:17792\n"
"v_smfmac_f32_16x16x64_bf16 a[128:131], v[4:7], v[80:87], v2\n"
"ds_read_b64_tr_b16 a[120:121], v25 offset:17120\n"
"ds_read_b64_tr_b16 a[122:123], v25 offset:17376\n"
"ds_read_b64_tr_b16 a[108:109], v25 offset:17568\n"
"v_smfmac_f32_16x16x64_bf16 a[132:135], v[4:7], v[88:95], v2\n"
"s_waitcnt lgkmcnt(15)\n"
"ds_read_b64_tr_b16 a[110:111], v25 offset:17824\n"
"v_exp_f32 v232, v232\n"
"v_mfma_f32_16x16x32_bf16 a[160:163], a[96:99], v[80:83], a[160:163]\n"
"v_exp_f32 v233, v233\n"
"v_cvt_pk_bf16_f32 v48, v224, v225\n"
"v_mfma_f32_16x16x32_bf16 a[164:167], a[96:99], v[84:87], a[164:167]\n"
"v_exp_f32 v234, v234\n"
"v_cvt_pk_bf16_f32 v49, v226, v227\n"
"v_mfma_f32_16x16x32_bf16 a[168:171], a[96:99], v[88:91], a[168:171]\n"
"ds_read_b64_tr_b16 a[116:117], v25 offset:17600\n"
"ds_read_b64_tr_b16 a[118:119], v25 offset:17856\n"
"buffer_load_dwordx4 v[184:187], v0, s[8:11], s56 offen offset:3072 sc1\n"
"v_mfma_f32_16x16x32_bf16 a[172:175], a[96:99], v[92:95], a[172:175]\n"
"s_waitcnt lgkmcnt(14)\n"
"v_exp_f32 v235, v235\n"
"v_mfma_f32_16x16x32_bf16 a[176:179], a[104:107], v[80:83], a[176:179]\n"
"v_exp_f32 v236, v236\n"
"v_cvt_pk_bf16_f32 v52, v228, v229\n"
"v_mfma_f32_16x16x32_bf16 a[180:183], a[104:107], v[84:87], a[180:183]\n"
"s_add_u32 s57, s57, 4096\n"
"v_exp_f32 v237, v237\n"
"v_cvt_pk_bf16_f32 v53, v230, v231\n"
"v_mfma_f32_16x16x32_bf16 a[184:187], a[104:107], v[88:91], a[184:187]\n"
"ds_read_b64_tr_b16 a[124:125], v25 offset:17632\n"
"ds_read_b64_tr_b16 a[126:127], v25 offset:17888\n"
"buffer_load_dwordx4 v[164:167], v0, s[8:11], s57 offen offset:0 sc1\n"
"v_mfma_f32_16x16x32_bf16 a[188:191], a[104:107], v[92:95], a[188:191]\n"
"s_waitcnt lgkmcnt(10)\n"
"buffer_load_dwordx4 v[172:175], v0, s[8:11], s57 offen offset:1024 sc1\n"
"v_exp_f32 v238, v238\n"
"v_mfma_f32_16x16x32_bf16 a[192:195], a[112:115], v[80:83], a[192:195]\n"
"v_exp_f32 v239, v239\n"
"v_cvt_pk_bf16_f32 v56, v232, v233\n"
"v_mfma_f32_16x16x32_bf16 a[196:199], a[112:115], v[84:87], a[196:199]\n"
"v_exp_f32 v240, v240\n"
"v_cvt_pk_bf16_f32 v57, v234, v235\n"
"v_mfma_f32_16x16x32_bf16 a[200:203], a[112:115], v[88:91], a[200:203]\n"
"ds_read_b64_tr_b16 a[84:85], v25 offset:17472\n"
"ds_read_b64_tr_b16 a[86:87], v25 offset:17728\n"
"buffer_load_dwordx4 v[180:183], v0, s[8:11], s57 offen offset:2048 sc1\n"
"v_mfma_f32_16x16x32_bf16 a[204:207], a[112:115], v[92:95], a[204:207]\n"
"s_waitcnt lgkmcnt(8)\n"
"v_exp_f32 v241, v241\n"
"v_mfma_f32_16x16x32_bf16 a[208:211], a[120:123], v[80:83], a[208:211]\n"
"v_exp_f32 v242, v242\n"
"v_cvt_pk_bf16_f32 v60, v236, v237\n"
"v_mfma_f32_16x16x32_bf16 a[212:215], a[120:123], v[84:87], a[212:215]\n"
"v_exp_f32 v243, v243\n"
"v_cvt_pk_bf16_f32 v61, v238, v239\n"
"v_mfma_f32_16x16x32_bf16 a[216:219], a[120:123], v[88:91], a[216:219]\n"
"ds_read_b64_tr_b16 a[92:93], v25 offset:17504\n"
"ds_read_b64_tr_b16 a[94:95], v25 offset:17760\n"
"buffer_load_dwordx4 v[188:191], v0, s[8:11], s57 offen offset:3072 sc1\n"
"v_mfma_f32_16x16x32_bf16 a[220:223], a[120:123], v[92:95], a[220:223]\n"
"s_waitcnt lgkmcnt(15)\n"
"v_exp_f32 v244, v244\n"
"v_mfma_f32_16x16x32_bf16 v[32:35], a[80:83], v[80:83], v[32:35]\n"
"v_exp_f32 v245, v245\n"
"v_cvt_pk_bf16_f32 v50, v240, v241\n"
"v_mfma_f32_16x16x32_bf16 v[36:39], a[80:83], v[84:87], v[36:39]\n"
"s_add_u32 s56, s56, 4096\n"
"v_exp_f32 v246, v246\n"
"v_cvt_pk_bf16_f32 v51, v242, v243\n"
"v_mfma_f32_16x16x32_bf16 v[40:43], a[80:83], v[88:91], v[40:43]\n"
"ds_read_b64_tr_b16 a[68:69], v25 offset:17408\n"
"ds_read_b64_tr_b16 a[70:71], v25 offset:17664\n"
"buffer_load_dwordx4 v[192:195], v0, s[8:11], s56 offen offset:0 sc1\n"
"v_mfma_f32_16x16x32_bf16 v[44:47], a[80:83], v[92:95], v[44:47]\n"
"s_waitcnt lgkmcnt(15)\n"
"v_exp_f32 v247, v247\n"
"v_mfma_f32_16x16x32_bf16 a[144:147], a[88:91], v[80:83], a[144:147]\n"
"v_exp_f32 v248, v248\n"
"v_cvt_pk_bf16_f32 v54, v244, v245\n"
"v_mfma_f32_16x16x32_bf16 a[148:151], a[88:91], v[84:87], a[148:151]\n"
"v_exp_f32 v249, v249\n"
"v_cvt_pk_bf16_f32 v55, v246, v247\n"
"v_mfma_f32_16x16x32_bf16 a[152:155], a[88:91], v[88:91], a[152:155]\n"
"ds_read_b64_tr_b16 a[76:77], v25 offset:17440\n"
"ds_read_b64_tr_b16 a[78:79], v25 offset:17696\n"
"buffer_load_dwordx4 v[200:203], v0, s[8:11], s56 offen offset:1024 sc1\n"
"v_mfma_f32_16x16x32_bf16 a[156:159], a[88:91], v[92:95], a[156:159]\n"
"s_waitcnt lgkmcnt(15)\n"
"v_exp_f32 v250, v250\n"
"v_mfma_f32_16x16x32_bf16 v[96:99], a[64:67], v[80:83], v[96:99]\n"
"v_exp_f32 v251, v251\n"
"v_cvt_pk_bf16_f32 v58, v248, v249\n"
"v_mfma_f32_16x16x32_bf16 v[100:103], a[64:67], v[84:87], v[100:103]\n"
"v_exp_f32 v252, v252\n"
"v_cvt_pk_bf16_f32 v59, v250, v251\n"
"v_mfma_f32_16x16x32_bf16 v[104:107], a[64:67], v[88:91], v[104:107]\n"
"v_exp_f32 v253, v253\n"
"v_mfma_f32_16x16x32_bf16 v[108:111], a[64:67], v[92:95], v[108:111]\n"
"s_waitcnt lgkmcnt(15)\n"
"v_exp_f32 v254, v254\n"
"v_cvt_pk_bf16_f32 v62, v252, v253\n"
"v_mfma_f32_16x16x32_bf16 v[112:115], a[72:75], v[80:83], v[112:115]\n"
"buffer_load_dwordx4 v[208:211], v0, s[8:11], s56 offen offset:2048 sc1\n"
"v_exp_f32 v255, v255\n"
"v_mfma_f32_16x16x32_bf16 v[116:119], a[72:75], v[84:87], v[116:119]\n"
"v_cvt_pk_bf16_f32 v63, v254, v255\n"
"v_mov_b32 v132, 0\n"
"v_mov_b32 v133, 0\n"
"v_mfma_f32_16x16x32_bf16 v[120:123], a[72:75], v[88:91], v[120:123]\n"
"v_mov_b32 v134, 0\n"
"v_mov_b32 v135, 0\n"
"v_mfma_f32_16x16x32_bf16 v[124:127], a[72:75], v[92:95], v[124:127]\n"
"buffer_load_dwordx4 v[216:219], v0, s[8:11], s56 offen offset:3072 sc1\n"
"v_mov_b32 v30, 0x7b800000\n"
"v_smfmac_f32_16x16x64_bf16 a[128:131], v[4:7], v[48:55], v2\n"
"v_smfmac_f32_16x16x64_bf16 a[132:135], v[4:7], v[56:63], v2\n"
"s_waitcnt lgkmcnt(15)\n"
"v_mfma_f32_16x16x32_bf16 a[160:163], a[100:103], v[48:51], a[160:163]\n"
"v_mfma_f32_16x16x32_bf16 a[164:167], a[100:103], v[52:55], a[164:167]\n"
"v_accvgpr_read_b32 v224, a128\n"
"v_accvgpr_read_b32 v225, a129\n"
"v_mfma_f32_16x16x32_bf16 a[168:171], a[100:103], v[56:59], a[168:171]\n"
"s_add_u32 s57, s57, 4096\n"
"v_add_u32 v2, 0x20000, v0\n"
"v_accvgpr_read_b32 v226, a130\n"
"v_mfma_f32_16x16x32_bf16 a[172:175], a[100:103], v[60:63], a[172:175]\n"
"s_waitcnt lgkmcnt(12)\n"
"v_accvgpr_read_b32 v227, a131\n"
"v_accvgpr_read_b32 v228, a132\n"
"v_mfma_f32_16x16x32_bf16 a[176:179], a[108:111], v[48:51], a[176:179]\n"
"buffer_load_dwordx4 v[196:199], v0, s[8:11], s57 offen offset:0 sc1\n"
"v_accvgpr_read_b32 v229, a133\n"
"v_mfma_f32_16x16x32_bf16 a[180:183], a[108:111], v[52:55], a[180:183]\n"
"v_accvgpr_read_b32 v230, a134\n"
"v_accvgpr_read_b32 v231, a135\n"
"v_mfma_f32_16x16x32_bf16 a[184:187], a[108:111], v[56:59], a[184:187]\n"
"s_add_u32 m0, s55, 0\n"
"v_add_f32 v128, v224, v225\n"
"v_add_f32 v129, v226, v227\n"
"v_mfma_f32_16x16x32_bf16 a[188:191], a[108:111], v[60:63], a[188:191]\n"
"s_waitcnt vmcnt(50) lgkmcnt(0)\n"
"v_add_f32 v130, v228, v229\n"
"v_add_f32 v131, v230, v231\n"
"v_mfma_f32_16x16x32_bf16 a[192:195], a[116:119], v[48:51], a[192:195]\n"
"s_barrier\n"
"buffer_load_dwordx4 v[204:207], v0, s[8:11], s57 offen offset:1024 sc1\n"
"v_add_f32 v31, v128, v129\n"
"v_mfma_f32_16x16x32_bf16 a[196:199], a[116:119], v[52:55], a[196:199]\n"
"s_add_u32 s59, s59, 16384\n"
"v_add_f32 v31, v31, v130\n"
"v_add_f32 v31, v31, v131\n"
"v_mfma_f32_16x16x32_bf16 a[200:203], a[116:119], v[56:59], a[200:203]\n"
"buffer_load_dwordx4 v0, s[8:11], s59 offen sc1 lds\n"
"v_cmp_nle_f32 vcc, v31, v30\n"
"v_mfma_f32_16x16x32_bf16 a[204:207], a[116:119], v[60:63], a[204:207]\n"
"s_waitcnt lgkmcnt(8)\n"
"v_rcp_f32 v128, v128\n"
"v_mfma_f32_16x16x32_bf16 a[208:211], a[124:127], v[48:51], a[208:211]\n"
"v_mfma_f32_16x16x32_bf16 a[212:215], a[124:127], v[52:55], a[212:215]\n"
"v_rcp_f32 v129, v129\n"
"v_mfma_f32_16x16x32_bf16 a[216:219], a[124:127], v[56:59], a[216:219]\n"
"v_rcp_f32 v130, v130\n"
"v_mfma_f32_16x16x32_bf16 a[220:223], a[124:127], v[60:63], a[220:223]\n"
"s_waitcnt lgkmcnt(6)\n"
"v_rcp_f32 v131, v131\n"
"v_mfma_f32_16x16x32_bf16 v[32:35], a[84:87], v[48:51], v[32:35]\n"
"ds_read_b128 v[64:67], v0 offset:33792\n"
"buffer_load_dwordx4 v0, s[8:11], s59 offen offset:1024 sc1 lds\n"
"v_mfma_f32_16x16x32_bf16 v[36:39], a[84:87], v[52:55], v[36:39]\n"
"ds_read_b128 v[68:71], v0 offset:34816\n"
"v_cndmask_b32 v31, 0, v30, vcc\n"
"v_mfma_f32_16x16x32_bf16 v[40:43], a[84:87], v[56:59], v[40:43]\n"
"ds_read_b128 v[72:75], v0 offset:35840\n"
"v_accvgpr_read_b32 v224, a160\n"
"v_mfma_f32_16x16x32_bf16 v[44:47], a[84:87], v[60:63], v[44:47]\n"
"s_waitcnt lgkmcnt(7)\n"
"ds_read_b128 v[76:79], v0 offset:36864\n"
"v_accvgpr_read_b32 v225, a161\n"
"v_mfma_f32_16x16x32_bf16 a[144:147], a[92:95], v[48:51], a[144:147]\n"
"buffer_load_dwordx4 v0, s[8:11], s59 offen offset:2048 sc1 lds\n"
"v_accvgpr_read_b32 v226, a162\n"
"v_mfma_f32_16x16x32_bf16 a[148:151], a[92:95], v[52:55], a[148:151]\n"
"v_accvgpr_read_b32 v227, a163\n"
"v_mul_f32 v224, v224, v128\n"
"v_mfma_f32_16x16x32_bf16 a[152:155], a[92:95], v[56:59], a[152:155]\n"
"v_mul_f32 v225, v225, v128\n"
"v_mul_f32 v226, v226, v128\n"
"v_mfma_f32_16x16x32_bf16 a[156:159], a[92:95], v[60:63], a[156:159]\n"
"s_waitcnt lgkmcnt(6)\n"
"v_mul_f32 v227, v227, v128\n"
"v_cvt_pk_bf16_f32 v140, v224, v225\n"
"v_mfma_f32_16x16x32_bf16 v[96:99], a[68:71], v[48:51], v[96:99]\n"
"ds_read_b128 v[80:83], v0 offset:37888\n"
"buffer_load_dwordx4 v0, s[8:11], s59 offen offset:3072 sc1 lds\n"
"v_mfma_f32_16x16x32_bf16 v[100:103], a[68:71], v[52:55], v[100:103]\n"
"s_add_u32 m0, s55, 16384\n"
"ds_read_b128 v[84:87], v0 offset:38912\n"
"v_cvt_pk_bf16_f32 v141, v226, v227\n"
"v_mfma_f32_16x16x32_bf16 v[104:107], a[68:71], v[56:59], v[104:107]\n"
"s_add_u32 s59, s59, 16384\n"
"ds_read_b128 v[88:91], v0 offset:39936\n"
"v_accvgpr_read_b32 v228, a176\n"
"v_mfma_f32_16x16x32_bf16 v[108:111], a[68:71], v[60:63], v[108:111]\n"
"s_waitcnt lgkmcnt(7)\n"
"ds_read_b128 v[92:95], v0 offset:40960\n"
"v_accvgpr_read_b32 v229, a177\n"
"v_mfma_f32_16x16x32_bf16 v[112:115], a[76:79], v[48:51], v[112:115]\n"
"buffer_load_dwordx4 v0, s[8:11], s59 offen sc1 lds\n"
"v_accvgpr_read_b32 v230, a178\n"
"v_mfma_f32_16x16x32_bf16 v[116:119], a[76:79], v[52:55], v[116:119]\n"
"v_accvgpr_read_b32 v231, a179\n"
"v_mul_f32 v228, v228, v128\n"
"v_mfma_f32_16x16x32_bf16 v[120:123], a[76:79], v[56:59], v[120:123]\n"
"v_mul_f32 v229, v229, v128\n"
"v_mul_f32 v230, v230, v128\n"
"v_mfma_f32_16x16x32_bf16 v[124:127], a[76:79], v[60:63], v[124:127]\n"
"s_waitcnt vmcnt(60) lgkmcnt(4)\n"
"v_mul_f32 v231, v231, v128\n"
"v_cvt_pk_bf16_f32 v142, v228, v229\n"
"v_mfma_f32_16x16x32_bf16 a[64:67], v[64:67], a[224:227], 0\n"
"ds_read_b128 v[48:51], v0 offset:41984\n"
"buffer_load_dwordx4 v0, s[8:11], s59 offen offset:1024 sc1 lds\n"
"v_mfma_f32_16x16x32_bf16 a[64:67], v[68:71], a[232:235], a[64:67]\n"
"ds_read_b128 v[52:55], v0 offset:43008\n"
"v_cvt_pk_bf16_f32 v143, v230, v231\n"
"v_mfma_f32_16x16x32_bf16 a[64:67], v[72:75], a[240:243], a[64:67]\n"
"ds_read_b128 v[56:59], v0 offset:44032\n"
"buffer_store_dwordx4 v[140:143], v0, s[8:11], s93 offen offset:2048 sc1\n"
"v_mfma_f32_16x16x32_bf16 a[64:67], v[76:79], a[248:251], a[64:67]\n"
"v_mfma_f32_16x16x32_bf16 v[136:139], v[132:135], v[140:143], 0\n"
"ds_read_b128 v[60:63], v0 offset:45056\n"
"v_accvgpr_read_b32 v232, a164\n"
"v_mfma_f32_16x16x32_bf16 a[68:71], v[76:79], a[252:255], 0\n"
"v_accvgpr_read_b32 v233, a165\n"
"v_mfma_f32_16x16x32_bf16 a[68:71], v[72:75], a[244:247], a[68:71]\n"
"v_accvgpr_read_b32 v234, a166\n"
"v_accvgpr_read_b32 v235, a167\n"
"v_mfma_f32_16x16x32_bf16 a[68:71], v[68:71], a[236:239], a[68:71]\n"
"v_mul_f32 v232, v232, v129\n"
"v_mul_f32 v233, v233, v129\n"
"v_mfma_f32_16x16x32_bf16 a[68:71], v[64:67], a[228:231], a[68:71]\n"
"s_waitcnt lgkmcnt(4)\n"
"v_mul_f32 v234, v234, v129\n"
"v_mul_f32 v235, v235, v129\n"
"v_mfma_f32_16x16x32_bf16 a[72:75], v[80:83], a[224:227], 0\n"
"ds_read_b128 v[64:67], v0 offset:46080\n"
"v_mfma_f32_16x16x32_bf16 a[72:75], v[84:87], a[232:235], a[72:75]\n"
"ds_read_b128 v[68:71], v0 offset:47104\n"
"v_cvt_pk_bf16_f32 v144, v232, v233\n"
"v_mfma_f32_16x16x32_bf16 a[72:75], v[88:91], a[240:243], a[72:75]\n"
"ds_read_b128 v[72:75], v0 offset:48128\n"
"v_cvt_pk_bf16_f32 v145, v234, v235\n"
"v_mfma_f32_16x16x32_bf16 a[72:75], v[92:95], a[248:251], a[72:75]\n"
"ds_read_b128 v[76:79], v0 offset:49152\n"
"v_accvgpr_read_b32 v236, a180\n"
"v_mfma_f32_16x16x32_bf16 a[76:79], v[92:95], a[252:255], 0\n"
"buffer_load_dwordx4 v0, s[8:11], s59 offen offset:2048 sc1 lds\n"
"v_accvgpr_read_b32 v237, a181\n"
"v_mfma_f32_16x16x32_bf16 a[76:79], v[88:91], a[244:247], a[76:79]\n"
"v_accvgpr_read_b32 v238, a182\n"
"v_accvgpr_read_b32 v239, a183\n"
"v_mfma_f32_16x16x32_bf16 a[76:79], v[84:87], a[236:239], a[76:79]\n"
"v_mul_f32 v236, v236, v129\n"
"v_mul_f32 v237, v237, v129\n"
"v_mfma_f32_16x16x32_bf16 a[76:79], v[80:83], a[228:231], a[76:79]\n"
"s_waitcnt lgkmcnt(4)\n"
"v_mul_f32 v238, v238, v129\n"
"v_mul_f32 v239, v239, v129\n"
"v_mfma_f32_16x16x32_bf16 a[80:83], v[48:51], a[224:227], 0\n"
"ds_read_b128 v[80:83], v0 offset:50176\n"
"buffer_load_dwordx4 v0, s[8:11], s59 offen offset:3072 sc1 lds\n"
"v_mfma_f32_16x16x32_bf16 a[80:83], v[52:55], a[232:235], a[80:83]\n"
"ds_read_b128 v[84:87], v0 offset:51200\n"
"v_cvt_pk_bf16_f32 v146, v236, v237\n"
"v_mfma_f32_16x16x32_bf16 a[80:83], v[56:59], a[240:243], a[80:83]\n"
"ds_read_b128 v[88:91], v0 offset:52224\n"
"v_cvt_pk_bf16_f32 v147, v238, v239\n"
"v_mfma_f32_16x16x32_bf16 a[80:83], v[60:63], a[248:251], a[80:83]\n"
"ds_read_b128 v[92:95], v0 offset:53248\n"
"buffer_store_dwordx4 v[144:147], v0, s[8:11], s94 offen offset:2048 sc1\n"
"v_mfma_f32_16x16x32_bf16 a[84:87], v[60:63], a[252:255], 0\n"
"v_mfma_f32_16x16x32_bf16 v[136:139], v[132:135], v[144:147], v[136:139]\n"
"v_accvgpr_read_b32 v224, a168\n"
"v_mfma_f32_16x16x32_bf16 a[84:87], v[56:59], a[244:247], a[84:87]\n"
"v_accvgpr_read_b32 v225, a169\n"
"v_accvgpr_read_b32 v226, a170\n"
"v_mfma_f32_16x16x32_bf16 a[84:87], v[52:55], a[236:239], a[84:87]\n"
"v_accvgpr_read_b32 v227, a171\n"
"v_mul_f32 v224, v224, v130\n"
"v_mfma_f32_16x16x32_bf16 a[84:87], v[48:51], a[228:231], a[84:87]\n"
"s_waitcnt lgkmcnt(4)\n"
"v_mul_f32 v225, v225, v130\n"
"v_mul_f32 v226, v226, v130\n"
"v_mfma_f32_16x16x32_bf16 a[88:91], v[64:67], a[224:227], 0\n"
"ds_read_b128 v[48:51], v0 offset:54272\n"
"buffer_load_dwordx4 v[212:215], v0, s[8:11], s57 offen offset:2048 sc1\n"
"v_mfma_f32_16x16x32_bf16 a[88:91], v[68:71], a[232:235], a[88:91]\n"
"ds_read_b128 v[52:55], v0 offset:55296\n"
"v_mul_f32 v227, v227, v130\n"
"v_mfma_f32_16x16x32_bf16 a[88:91], v[72:75], a[240:243], a[88:91]\n"
"ds_read_b128 v[56:59], v0 offset:56320\n"
"v_cvt_pk_bf16_f32 v148, v224, v225\n"
"v_mfma_f32_16x16x32_bf16 a[88:91], v[76:79], a[248:251], a[88:91]\n"
"ds_read_b128 v[60:63], v0 offset:57344\n"
"v_cvt_pk_bf16_f32 v149, v226, v227\n"
"v_mfma_f32_16x16x32_bf16 a[92:95], v[76:79], a[252:255], 0\n"
"buffer_load_dwordx4 v[220:223], v0, s[8:11], s57 offen offset:3072 sc1\n"
"v_accvgpr_read_b32 v228, a184\n"
"v_mfma_f32_16x16x32_bf16 a[92:95], v[72:75], a[244:247], a[92:95]\n"
"v_accvgpr_read_b32 v229, a185\n"
"v_accvgpr_read_b32 v230, a186\n"
"v_mfma_f32_16x16x32_bf16 a[92:95], v[68:71], a[236:239], a[92:95]\n"
"v_accvgpr_read_b32 v231, a187\n"
"v_mul_f32 v228, v228, v130\n"
"v_mfma_f32_16x16x32_bf16 a[92:95], v[64:67], a[228:231], a[92:95]\n"
"s_waitcnt lgkmcnt(4)\n"
"v_mul_f32 v229, v229, v130\n"
"v_mul_f32 v230, v230, v130\n"
"v_mfma_f32_16x16x32_bf16 a[96:99], v[80:83], a[224:227], 0\n"
"ds_read_b128 v[64:67], v0 offset:58368\n"
"v_mfma_f32_16x16x32_bf16 a[96:99], v[84:87], a[232:235], a[96:99]\n"
"ds_read_b128 v[68:71], v0 offset:59392\n"
"v_mul_f32 v231, v231, v130\n"
"v_mfma_f32_16x16x32_bf16 a[96:99], v[88:91], a[240:243], a[96:99]\n"
"ds_read_b128 v[72:75], v0 offset:60416\n"
"v_cvt_pk_bf16_f32 v150, v228, v229\n"
"v_mfma_f32_16x16x32_bf16 a[96:99], v[92:95], a[248:251], a[96:99]\n"
"ds_read_b128 v[76:79], v0 offset:61440\n"
"v_cvt_pk_bf16_f32 v151, v230, v231\n"
"v_mfma_f32_16x16x32_bf16 a[100:103], v[92:95], a[252:255], 0\n"
"buffer_store_dwordx4 v[148:151], v0, s[8:11], s95 offen offset:2048 sc1\n"
"v_mfma_f32_16x16x32_bf16 a[100:103], v[88:91], a[244:247], a[100:103]\n"
"v_mfma_f32_16x16x32_bf16 v[136:139], v[132:135], v[148:151], v[136:139]\n"
"v_accvgpr_read_b32 v232, a172\n"
"v_accvgpr_read_b32 v233, a173\n"
"v_mfma_f32_16x16x32_bf16 a[100:103], v[84:87], a[236:239], a[100:103]\n"
"v_accvgpr_read_b32 v234, a174\n"
"v_accvgpr_read_b32 v235, a175\n"
"v_mfma_f32_16x16x32_bf16 a[100:103], v[80:83], a[228:231], a[100:103]\n"
"s_waitcnt lgkmcnt(4)\n"
"v_mul_f32 v232, v232, v131\n"
"v_mul_f32 v233, v233, v131\n"
"v_mfma_f32_16x16x32_bf16 a[104:107], v[48:51], a[224:227], 0\n"
"ds_read_b128 v[80:83], v0 offset:62464\n"
"ds_read_b128 v[84:87], v0 offset:63488\n"
"v_mfma_f32_16x16x32_bf16 a[104:107], v[52:55], a[232:235], a[104:107]\n"
"ds_read_b128 v[88:91], v0 offset:64512\n"
"ds_read_b128 v[92:95], v1 offset:0\n"
"v_mfma_f32_16x16x32_bf16 a[104:107], v[56:59], a[240:243], a[104:107]\n"
"v_mul_f32 v234, v234, v131\n"
"v_mfma_f32_16x16x32_bf16 a[104:107], v[60:63], a[248:251], a[104:107]\n"
"v_mul_f32 v235, v235, v131\n"
"v_cvt_pk_bf16_f32 v152, v232, v233\n"
"v_mfma_f32_16x16x32_bf16 a[108:111], v[60:63], a[252:255], 0\n"
"v_cvt_pk_bf16_f32 v153, v234, v235\n"
"v_mfma_f32_16x16x32_bf16 a[108:111], v[56:59], a[244:247], a[108:111]\n"
"v_accvgpr_read_b32 v236, a188\n"
"v_accvgpr_read_b32 v237, a189\n"
"v_mfma_f32_16x16x32_bf16 a[108:111], v[52:55], a[236:239], a[108:111]\n"
"v_accvgpr_read_b32 v238, a190\n"
"v_accvgpr_read_b32 v239, a191\n"
"v_mfma_f32_16x16x32_bf16 a[108:111], v[48:51], a[228:231], a[108:111]\n"
"s_waitcnt vmcnt(56) lgkmcnt(0)\n"
"v_mul_f32 v236, v236, v131\n"
"v_mul_f32 v237, v237, v131\n"
"v_mfma_f32_16x16x32_bf16 a[112:115], v[64:67], a[224:227], 0\n"
"s_barrier\n"
"ds_read_b128 v[48:51], v2 offset:10240\n"
"v_mfma_f32_16x16x32_bf16 a[112:115], v[68:71], a[232:235], a[112:115]\n"
"ds_read_b128 v[52:55], v2 offset:11264\n"
"v_mul_f32 v238, v238, v131\n"
"v_mfma_f32_16x16x32_bf16 a[112:115], v[72:75], a[240:243], a[112:115]\n"
"s_add_u32 m0, s55, 33792\n"
"ds_read_b128 v[56:59], v2 offset:12288\n"
"v_mul_f32 v239, v239, v131\n"
"v_mfma_f32_16x16x32_bf16 a[112:115], v[76:79], a[248:251], a[112:115]\n"
"s_add_u32 s59, s59, 16384\n"
"ds_read_b128 v[60:63], v2 offset:13312\n"
"v_cvt_pk_bf16_f32 v154, v236, v237\n"
"v_mfma_f32_16x16x32_bf16 a[116:119], v[76:79], a[252:255], 0\n"
"buffer_load_dwordx4 v0, s[8:11], s59 offen sc1 lds\n"
"v_cvt_pk_bf16_f32 v155, v238, v239\n"
"v_mfma_f32_16x16x32_bf16 a[116:119], v[72:75], a[244:247], a[116:119]\n"
"buffer_store_dwordx4 v[152:155], v0, s[8:11], s96 offen offset:2048 sc1\n"
"v_accvgpr_read_b32 v224, a192\n"
"v_mfma_f32_16x16x32_bf16 v[136:139], v[132:135], v[152:155], v[136:139]\n"
"v_mfma_f32_16x16x32_bf16 a[116:119], v[68:71], a[236:239], a[116:119]\n"
"v_accvgpr_read_b32 v225, a193\n"
"v_accvgpr_read_b32 v226, a194\n"
"v_mfma_f32_16x16x32_bf16 a[116:119], v[64:67], a[228:231], a[116:119]\n"
"s_waitcnt lgkmcnt(4)\n"
"v_accvgpr_read_b32 v227, a195\n"
"v_mul_f32 v224, v224, v128\n"
"v_mfma_f32_16x16x32_bf16 v[8:11], v[80:83], a[224:227], 0\n"
"ds_read_b128 v[64:67], v2 offset:14336\n"
"buffer_load_dwordx4 v0, s[8:11], s59 offen offset:1024 sc1 lds\n"
"v_mfma_f32_16x16x32_bf16 v[8:11], v[84:87], a[232:235], v[8:11]\n"
"ds_read_b128 v[68:71], v2 offset:15360\n"
"v_mul_f32 v225, v225, v128\n"
"v_mfma_f32_16x16x32_bf16 v[8:11], v[88:91], a[240:243], v[8:11]\n"
"ds_read_b128 v[72:75], v2 offset:16384\n"
"v_mul_f32 v226, v226, v128\n"
"v_mfma_f32_16x16x32_bf16 v[8:11], v[92:95], a[248:251], v[8:11]\n"
"ds_read_b128 v[76:79], v2 offset:17408\n"
"v_mul_f32 v227, v227, v128\n"
"v_mfma_f32_16x16x32_bf16 v[12:15], v[92:95], a[252:255], 0\n"
"buffer_load_dwordx4 v0, s[8:11], s59 offen offset:2048 sc1 lds\n"
"v_cvt_pk_bf16_f32 v140, v224, v225\n"
"v_mfma_f32_16x16x32_bf16 v[12:15], v[88:91], a[244:247], v[12:15]\n"
"v_cvt_pk_bf16_f32 v141, v226, v227\n"
"v_accvgpr_read_b32 v228, a208\n"
"v_mfma_f32_16x16x32_bf16 v[12:15], v[84:87], a[236:239], v[12:15]\n"
"v_accvgpr_read_b32 v229, a209\n"
"v_accvgpr_read_b32 v230, a210\n"
"v_mfma_f32_16x16x32_bf16 v[12:15], v[80:83], a[228:231], v[12:15]\n"
"s_waitcnt lgkmcnt(4)\n"
"v_accvgpr_read_b32 v231, a211\n"
"v_mul_f32 v228, v228, v128\n"
"v_mfma_f32_16x16x32_bf16 v[16:19], v[48:51], a[224:227], 0\n"
"ds_read_b128 v[80:83], v2 offset:18432\n"
"buffer_load_dwordx4 v0, s[8:11], s59 offen offset:3072 sc1 lds\n"
"v_mfma_f32_16x16x32_bf16 v[16:19], v[52:55], a[232:235], v[16:19]\n"
"ds_read_b128 v[84:87], v2 offset:19456\n"
"v_mul_f32 v229, v229, v128\n"
"v_mfma_f32_16x16x32_bf16 v[16:19], v[56:59], a[240:243], v[16:19]\n"
"s_add_u32 m0, s55, 50176\n"
"ds_read_b128 v[88:91], v2 offset:20480\n"
"v_mul_f32 v230, v230, v128\n"
"v_mfma_f32_16x16x32_bf16 v[16:19], v[60:63], a[248:251], v[16:19]\n"
"s_add_u32 s59, s59, 16384\n"
"ds_read_b128 v[92:95], v2 offset:21504\n"
"v_mul_f32 v231, v231, v128\n"
"v_mfma_f32_16x16x32_bf16 v[20:23], v[60:63], a[252:255], 0\n"
"buffer_load_dwordx4 v0, s[8:11], s59 offen sc1 lds\n"
"v_cvt_pk_bf16_f32 v142, v228, v229\n"
"v_mfma_f32_16x16x32_bf16 v[20:23], v[56:59], a[244:247], v[20:23]\n"
"v_cvt_pk_bf16_f32 v143, v230, v231\n"
"v_mfma_f32_16x16x32_bf16 v[20:23], v[52:55], a[236:239], v[20:23]\n"
"buffer_store_dwordx4 v[140:143], v0, s[8:11], s93 offen offset:3072 sc1\n"
"v_accvgpr_read_b32 v232, a196\n"
"v_mfma_f32_16x16x32_bf16 v[136:139], v[132:135], v[140:143], v[136:139]\n"
"v_mfma_f32_16x16x32_bf16 v[20:23], v[48:51], a[228:231], v[20:23]\n"
"s_waitcnt vmcnt(42) lgkmcnt(4)\n"
"v_accvgpr_read_b32 v233, a197\n"
"v_accvgpr_read_b32 v234, a198\n"
"v_mfma_f32_16x16x32_bf16 a[64:67], v[64:67], a[0:3], a[64:67]\n"
"ds_read_b128 v[48:51], v2 offset:22528\n"
"v_mfma_f32_16x16x32_bf16 a[64:67], v[68:71], a[8:11], a[64:67]\n"
"ds_read_b128 v[52:55], v2 offset:23552\n"
"v_accvgpr_read_b32 v235, a199\n"
"v_mfma_f32_16x16x32_bf16 a[64:67], v[72:75], a[16:19], a[64:67]\n"
"ds_read_b128 v[56:59], v2 offset:24576\n"
"v_mul_f32 v232, v232, v129\n"
"v_mfma_f32_16x16x32_bf16 a[64:67], v[76:79], a[24:27], a[64:67]\n"
"ds_read_b128 v[60:63], v2 offset:25600\n"
"v_mul_f32 v233, v233, v129\n"
"v_mfma_f32_16x16x32_bf16 a[68:71], v[76:79], a[28:31], a[68:71]\n"
"v_mul_f32 v234, v234, v129\n"
"v_mfma_f32_16x16x32_bf16 a[68:71], v[72:75], a[20:23], a[68:71]\n"
"v_mul_f32 v235, v235, v129\n"
"v_cvt_pk_bf16_f32 v144, v232, v233\n"
"v_mfma_f32_16x16x32_bf16 a[68:71], v[68:71], a[12:15], a[68:71]\n"
"v_cvt_pk_bf16_f32 v145, v234, v235\n"
"v_accvgpr_read_b32 v236, a212\n"
"v_mfma_f32_16x16x32_bf16 a[68:71], v[64:67], a[4:7], a[68:71]\n"
"s_waitcnt lgkmcnt(4)\n"
"v_accvgpr_read_b32 v237, a213\n"
"v_accvgpr_read_b32 v238, a214\n"
"v_mfma_f32_16x16x32_bf16 a[72:75], v[80:83], a[0:3], a[72:75]\n"
"ds_read_b128 v[64:67], v1 offset:1024\n"
"buffer_load_dwordx4 v0, s[8:11], s59 offen offset:1024 sc1 lds\n"
"v_mfma_f32_16x16x32_bf16 a[72:75], v[84:87], a[8:11], a[72:75]\n"
"ds_read_b128 v[68:71], v1 offset:2048\n"
"v_accvgpr_read_b32 v239, a215\n"
"v_mfma_f32_16x16x32_bf16 a[72:75], v[88:91], a[16:19], a[72:75]\n"
"ds_read_b128 v[72:75], v1 offset:3072\n"
"v_mul_f32 v236, v236, v129\n"
"v_mfma_f32_16x16x32_bf16 a[72:75], v[92:95], a[24:27], a[72:75]\n"
"ds_read_b128 v[76:79], v1 offset:4096\n"
"v_mul_f32 v237, v237, v129\n"
"v_mfma_f32_16x16x32_bf16 a[76:79], v[92:95], a[28:31], a[76:79]\n"
"buffer_load_dwordx4 v0, s[8:11], s59 offen offset:2048 sc1 lds\n"
"v_mul_f32 v238, v238, v129\n"
"v_mfma_f32_16x16x32_bf16 a[76:79], v[88:91], a[20:23], a[76:79]\n"
"v_mul_f32 v239, v239, v129\n"
"v_cvt_pk_bf16_f32 v146, v236, v237\n"
"v_mfma_f32_16x16x32_bf16 a[76:79], v[84:87], a[12:15], a[76:79]\n"
"buffer_load_dwordx4 v0, s[8:11], s59 offen offset:3072 sc1 lds\n"
"v_cvt_pk_bf16_f32 v147, v238, v239\n"
"v_mfma_f32_16x16x32_bf16 a[76:79], v[80:83], a[4:7], a[76:79]\n"
"s_waitcnt lgkmcnt(4)\n"
"buffer_store_dwordx4 v[144:147], v0, s[8:11], s94 offen offset:3072 sc1\n"
"v_accvgpr_read_b32 v224, a200\n"
"v_mfma_f32_16x16x32_bf16 v[136:139], v[132:135], v[144:147], v[136:139]\n"
"v_mfma_f32_16x16x32_bf16 a[80:83], v[48:51], a[0:3], a[80:83]\n"
"ds_read_b128 v[80:83], v1 offset:5120\n"
"v_mfma_f32_16x16x32_bf16 a[80:83], v[52:55], a[8:11], a[80:83]\n"
"ds_read_b128 v[84:87], v1 offset:6144\n"
"v_accvgpr_read_b32 v225, a201\n"
"v_mfma_f32_16x16x32_bf16 a[80:83], v[56:59], a[16:19], a[80:83]\n"
"ds_read_b128 v[88:91], v1 offset:7168\n"
"v_accvgpr_read_b32 v226, a202\n"
"v_mfma_f32_16x16x32_bf16 a[80:83], v[60:63], a[24:27], a[80:83]\n"
"ds_read_b128 v[92:95], v1 offset:8192\n"
"v_accvgpr_read_b32 v227, a203\n"
"v_mfma_f32_16x16x32_bf16 a[84:87], v[60:63], a[28:31], a[84:87]\n"
"v_mul_f32 v224, v224, v130\n"
"v_mfma_f32_16x16x32_bf16 a[84:87], v[56:59], a[20:23], a[84:87]\n"
"v_mul_f32 v225, v225, v130\n"
"v_mul_f32 v226, v226, v130\n"
"v_mfma_f32_16x16x32_bf16 a[84:87], v[52:55], a[12:15], a[84:87]\n"
"v_mul_f32 v227, v227, v130\n"
"v_cvt_pk_bf16_f32 v148, v224, v225\n"
"v_mfma_f32_16x16x32_bf16 a[84:87], v[48:51], a[4:7], a[84:87]\n"
"s_waitcnt lgkmcnt(4)\n"
"v_cvt_pk_bf16_f32 v149, v226, v227\n"
"v_accvgpr_read_b32 v228, a216\n"
"v_mfma_f32_16x16x32_bf16 a[88:91], v[64:67], a[0:3], a[88:91]\n"
"ds_read_b128 v[48:51], v1 offset:9216\n"
"v_mfma_f32_16x16x32_bf16 a[88:91], v[68:71], a[8:11], a[88:91]\n"
"ds_read_b128 v[52:55], v1 offset:10240\n"
"v_accvgpr_read_b32 v229, a217\n"
"v_mfma_f32_16x16x32_bf16 a[88:91], v[72:75], a[16:19], a[88:91]\n"
"ds_read_b128 v[56:59], v1 offset:11264\n"
"v_accvgpr_read_b32 v230, a218\n"
"v_mfma_f32_16x16x32_bf16 a[88:91], v[76:79], a[24:27], a[88:91]\n"
"s_add_u32 s56, s56, 4096\n"
"ds_read_b128 v[60:63], v1 offset:12288\n"
"v_accvgpr_read_b32 v231, a219\n"
"v_mfma_f32_16x16x32_bf16 a[92:95], v[76:79], a[28:31], a[92:95]\n"
"buffer_load_dwordx4 a[160:163], v0, s[8:11], s56 offen offset:0 sc1\n"
"v_mul_f32 v228, v228, v130\n"
"v_mfma_f32_16x16x32_bf16 a[92:95], v[72:75], a[20:23], a[92:95]\n"
"v_mul_f32 v229, v229, v130\n"
"v_mul_f32 v230, v230, v130\n"
"v_mfma_f32_16x16x32_bf16 a[92:95], v[68:71], a[12:15], a[92:95]\n"
"v_mul_f32 v231, v231, v130\n"
"v_cvt_pk_bf16_f32 v150, v228, v229\n"
"v_mfma_f32_16x16x32_bf16 a[92:95], v[64:67], a[4:7], a[92:95]\n"
"s_waitcnt lgkmcnt(4)\n"
"buffer_load_dwordx4 a[168:171], v0, s[8:11], s56 offen offset:1024 sc1\n"
"v_cvt_pk_bf16_f32 v151, v230, v231\n"
"v_mfma_f32_16x16x32_bf16 a[96:99], v[80:83], a[0:3], a[96:99]\n"
"ds_read_b128 v[64:67], v1 offset:13312\n"
"ds_read_b128 v[68:71], v1 offset:14336\n"
"v_mfma_f32_16x16x32_bf16 a[96:99], v[84:87], a[8:11], a[96:99]\n"
"ds_read_b128 v[72:75], v1 offset:15360\n"
"ds_read_b128 v[76:79], v1 offset:16384\n"
"v_mfma_f32_16x16x32_bf16 a[96:99], v[88:91], a[16:19], a[96:99]\n"
"buffer_store_dwordx4 v[148:151], v0, s[8:11], s95 offen offset:3072 sc1\n"
"v_mfma_f32_16x16x32_bf16 a[96:99], v[92:95], a[24:27], a[96:99]\n"
"v_mfma_f32_16x16x32_bf16 v[136:139], v[132:135], v[148:151], v[136:139]\n"
"v_accvgpr_read_b32 v232, a204\n"
"v_accvgpr_read_b32 v233, a205\n"
"v_mfma_f32_16x16x32_bf16 a[100:103], v[92:95], a[28:31], a[100:103]\n"
"v_accvgpr_read_b32 v234, a206\n"
"v_mfma_f32_16x16x32_bf16 a[100:103], v[88:91], a[20:23], a[100:103]\n"
"v_accvgpr_read_b32 v235, a207\n"
"v_mul_f32 v232, v232, v131\n"
"v_mfma_f32_16x16x32_bf16 a[100:103], v[84:87], a[12:15], a[100:103]\n"
"v_mul_f32 v233, v233, v131\n"
"v_mul_f32 v234, v234, v131\n"
"v_mfma_f32_16x16x32_bf16 a[100:103], v[80:83], a[4:7], a[100:103]\n"
"s_waitcnt vmcnt(61) lgkmcnt(0)\n"
"v_mul_f32 v235, v235, v131\n"
"v_cvt_pk_bf16_f32 v152, v232, v233\n"
"v_mfma_f32_16x16x32_bf16 a[104:107], v[48:51], a[0:3], a[104:107]\n"
"s_barrier\n"
"ds_read_b128 v[80:83], v1 offset:41984\n"
"buffer_load_dwordx4 a[176:179], v0, s[8:11], s56 offen offset:2048 sc1\n"
"v_mfma_f32_16x16x32_bf16 a[104:107], v[52:55], a[8:11], a[104:107]\n"
"ds_read_b128 v[84:87], v1 offset:43008\n"
"v_cvt_pk_bf16_f32 v153, v234, v235\n"
"v_mfma_f32_16x16x32_bf16 a[104:107], v[56:59], a[16:19], a[104:107]\n"
"s_add_u32 m0, s55, 141312\n"
"ds_read_b128 v[88:91], v1 offset:44032\n"
"v_accvgpr_read_b32 v236, a220\n"
"v_mfma_f32_16x16x32_bf16 a[104:107], v[60:63], a[24:27], a[104:107]\n"
"s_add_u32 s59, s59, 16384\n"
"ds_read_b128 v[92:95], v1 offset:45056\n"
"v_accvgpr_read_b32 v237, a221\n"
"v_mfma_f32_16x16x32_bf16 a[108:111], v[60:63], a[28:31], a[108:111]\n"
"buffer_load_dwordx4 v0, s[8:11], s59 offen sc1 lds\n"
"v_accvgpr_read_b32 v238, a222\n"
"v_mfma_f32_16x16x32_bf16 a[108:111], v[56:59], a[20:23], a[108:111]\n"
"v_accvgpr_read_b32 v239, a223\n"
"v_mul_f32 v236, v236, v131\n"
"v_mfma_f32_16x16x32_bf16 a[108:111], v[52:55], a[12:15], a[108:111]\n"
"v_mul_f32 v237, v237, v131\n"
"v_mul_f32 v238, v238, v131\n"
"v_mfma_f32_16x16x32_bf16 a[108:111], v[48:51], a[4:7], a[108:111]\n"
"s_waitcnt lgkmcnt(4)\n"
"v_mul_f32 v239, v239, v131\n"
"v_cvt_pk_bf16_f32 v154, v236, v237\n"
"v_mfma_f32_16x16x32_bf16 a[112:115], v[64:67], a[0:3], a[112:115]\n"
"ds_read_b128 v[48:51], v1 offset:46080\n"
"buffer_load_dwordx4 v0, s[8:11], s59 offen offset:1024 sc1 lds\n"
"v_mfma_f32_16x16x32_bf16 a[112:115], v[68:71], a[8:11], a[112:115]\n"
"ds_read_b128 v[52:55], v1 offset:47104\n"
"v_cvt_pk_bf16_f32 v155, v238, v239\n"
"v_mfma_f32_16x16x32_bf16 a[112:115], v[72:75], a[16:19], a[112:115]\n"
"ds_read_b128 v[56:59], v1 offset:48128\n"
"buffer_store_dwordx4 v[152:155], v0, s[8:11], s96 offen offset:3072 sc1\n"
"v_mfma_f32_16x16x32_bf16 a[112:115], v[76:79], a[24:27], a[112:115]\n"
"v_mfma_f32_16x16x32_bf16 v[136:139], v[132:135], v[152:155], v[136:139]\n"
"ds_read_b128 v[60:63], v1 offset:49152\n"
"v_mul_f32 v32, v32, v128\n"
"v_mfma_f32_16x16x32_bf16 a[116:119], v[76:79], a[28:31], a[116:119]\n"
"v_mul_f32 v33, v33, v128\n"
"v_mfma_f32_16x16x32_bf16 a[116:119], v[72:75], a[20:23], a[116:119]\n"
"v_mul_f32 v34, v34, v128\n"
"v_mul_f32 v35, v35, v128\n"
"v_mfma_f32_16x16x32_bf16 a[116:119], v[68:71], a[12:15], a[116:119]\n"
"v_cvt_pk_bf16_f32 v140, v32, v33\n"
"v_cvt_pk_bf16_f32 v141, v34, v35\n"
"v_mfma_f32_16x16x32_bf16 a[116:119], v[64:67], a[4:7], a[116:119]\n"
"s_waitcnt lgkmcnt(4)\n"
"v_accvgpr_read_b32 v228, a144\n"
"v_accvgpr_read_b32 v229, a145\n"
"v_mfma_f32_16x16x32_bf16 v[8:11], v[80:83], a[0:3], v[8:11]\n"
"ds_read_b128 v[64:67], v1 offset:50176\n"
"v_mfma_f32_16x16x32_bf16 v[8:11], v[84:87], a[8:11], v[8:11]\n"
"ds_read_b128 v[68:71], v1 offset:51200\n"
"v_accvgpr_read_b32 v230, a146\n"
"v_mfma_f32_16x16x32_bf16 v[8:11], v[88:91], a[16:19], v[8:11]\n"
"ds_read_b128 v[72:75], v1 offset:52224\n"
"v_accvgpr_read_b32 v231, a147\n"
"v_mfma_f32_16x16x32_bf16 v[8:11], v[92:95], a[24:27], v[8:11]\n"
"ds_read_b128 v[76:79], v1 offset:53248\n"
"v_mul_f32 v228, v228, v128\n"
"v_mfma_f32_16x16x32_bf16 v[12:15], v[92:95], a[28:31], v[12:15]\n"
"buffer_load_dwordx4 v0, s[8:11], s59 offen offset:2048 sc1 lds\n"
"v_mul_f32 v229, v229, v128\n"
"v_mfma_f32_16x16x32_bf16 v[12:15], v[88:91], a[20:23], v[12:15]\n"
"v_mul_f32 v230, v230, v128\n"
"v_mul_f32 v231, v231, v128\n"
"v_mfma_f32_16x16x32_bf16 v[12:15], v[84:87], a[12:15], v[12:15]\n"
"v_cvt_pk_bf16_f32 v142, v228, v229\n"
"v_cvt_pk_bf16_f32 v143, v230, v231\n"
"v_mfma_f32_16x16x32_bf16 v[12:15], v[80:83], a[4:7], v[12:15]\n"
"s_waitcnt lgkmcnt(4)\n"
"buffer_store_dwordx4 v[140:143], v0, s[8:11], s93 offen offset:1024 sc1\n"
"v_mul_f32 v36, v36, v129\n"
"v_mfma_f32_16x16x32_bf16 v[136:139], v[132:135], v[140:143], v[136:139]\n"
"v_mfma_f32_16x16x32_bf16 v[16:19], v[48:51], a[0:3], v[16:19]\n"
"ds_read_b128 v[80:83], v1 offset:54272\n"
"v_mfma_f32_16x16x32_bf16 v[16:19], v[52:55], a[8:11], v[16:19]\n"
"ds_read_b128 v[84:87], v1 offset:55296\n"
"v_mul_f32 v37, v37, v129\n"
"v_mfma_f32_16x16x32_bf16 v[16:19], v[56:59], a[16:19], v[16:19]\n"
"ds_read_b128 v[88:91], v1 offset:56320\n"
"v_mul_f32 v38, v38, v129\n"
"v_mfma_f32_16x16x32_bf16 v[16:19], v[60:63], a[24:27], v[16:19]\n"
"ds_read_b128 v[92:95], v1 offset:57344\n"
"v_mul_f32 v39, v39, v129\n"
"v_mfma_f32_16x16x32_bf16 v[20:23], v[60:63], a[28:31], v[20:23]\n"
"v_cvt_pk_bf16_f32 v144, v36, v37\n"
"v_mfma_f32_16x16x32_bf16 v[20:23], v[56:59], a[20:23], v[20:23]\n"
"v_cvt_pk_bf16_f32 v145, v38, v39\n"
"v_accvgpr_read_b32 v236, a148\n"
"v_mfma_f32_16x16x32_bf16 v[20:23], v[52:55], a[12:15], v[20:23]\n"
"v_accvgpr_read_b32 v237, a149\n"
"v_accvgpr_read_b32 v238, a150\n"
"v_mfma_f32_16x16x32_bf16 v[20:23], v[48:51], a[4:7], v[20:23]\n"
"s_waitcnt vmcnt(47) lgkmcnt(4)\n"
"v_accvgpr_read_b32 v239, a151\n"
"v_mul_f32 v236, v236, v129\n"
"v_mfma_f32_16x16x32_bf16 a[64:67], v[64:67], a[32:35], a[64:67]\n"
"ds_read_b128 v[48:51], v1 offset:25088\n"
"buffer_load_dwordx4 v0, s[8:11], s59 offen offset:3072 sc1 lds\n"
"v_mfma_f32_16x16x32_bf16 a[64:67], v[68:71], a[40:43], a[64:67]\n"
"ds_read_b128 v[52:55], v1 offset:26112\n"
"v_mul_f32 v237, v237, v129\n"
"v_mfma_f32_16x16x32_bf16 a[64:67], v[72:75], a[48:51], a[64:67]\n"
"ds_read_b128 v[56:59], v1 offset:27136\n"
"v_mul_f32 v238, v238, v129\n"
"v_mfma_f32_16x16x32_bf16 a[64:67], v[76:79], a[56:59], a[64:67]\n"
"ds_read_b128 v[60:63], v1 offset:28160\n"
"v_mul_f32 v239, v239, v129\n"
"v_mfma_f32_16x16x32_bf16 a[68:71], v[76:79], a[60:63], a[68:71]\n"
"buffer_load_dwordx4 a[184:187], v0, s[8:11], s56 offen offset:3072 sc1\n"
"v_cvt_pk_bf16_f32 v146, v236, v237\n"
"v_mfma_f32_16x16x32_bf16 a[68:71], v[72:75], a[52:55], a[68:71]\n"
"v_cvt_pk_bf16_f32 v147, v238, v239\n"
"v_mfma_f32_16x16x32_bf16 a[68:71], v[68:71], a[44:47], a[68:71]\n"
"buffer_store_dwordx4 v[144:147], v0, s[8:11], s94 offen offset:1024 sc1\n"
"v_mul_f32 v40, v40, v130\n"
"v_mfma_f32_16x16x32_bf16 v[136:139], v[132:135], v[144:147], v[136:139]\n"
"v_mfma_f32_16x16x32_bf16 a[68:71], v[64:67], a[36:39], a[68:71]\n"
"s_waitcnt lgkmcnt(4)\n"
"v_mul_f32 v41, v41, v130\n"
"v_mul_f32 v42, v42, v130\n"
"v_mfma_f32_16x16x32_bf16 a[72:75], v[80:83], a[32:35], a[72:75]\n"
"ds_read_b128 v[64:67], v1 offset:29184\n"
"v_mfma_f32_16x16x32_bf16 a[72:75], v[84:87], a[40:43], a[72:75]\n"
"ds_read_b128 v[68:71], v1 offset:30208\n"
"v_mul_f32 v43, v43, v130\n"
"v_mfma_f32_16x16x32_bf16 a[72:75], v[88:91], a[48:51], a[72:75]\n"
"ds_read_b128 v[72:75], v1 offset:31232\n"
"v_cvt_pk_bf16_f32 v148, v40, v41\n"
"v_mfma_f32_16x16x32_bf16 a[72:75], v[92:95], a[56:59], a[72:75]\n"
"ds_read_b128 v[76:79], v1 offset:32256\n"
"v_cvt_pk_bf16_f32 v149, v42, v43\n"
"v_mfma_f32_16x16x32_bf16 a[76:79], v[92:95], a[60:63], a[76:79]\n"
"v_accvgpr_read_b32 v228, a152\n"
"v_mfma_f32_16x16x32_bf16 a[76:79], v[88:91], a[52:55], a[76:79]\n"
"v_accvgpr_read_b32 v229, a153\n"
"v_accvgpr_read_b32 v230, a154\n"
"v_mfma_f32_16x16x32_bf16 a[76:79], v[84:87], a[44:47], a[76:79]\n"
"s_add_u32 s57, s57, 4096\n"
"v_accvgpr_read_b32 v231, a155\n"
"v_mul_f32 v228, v228, v130\n"
"v_mfma_f32_16x16x32_bf16 a[76:79], v[80:83], a[36:39], a[76:79]\n"
"s_waitcnt lgkmcnt(4)\n"
"v_mul_f32 v229, v229, v130\n"
"v_mul_f32 v230, v230, v130\n"
"v_mfma_f32_16x16x32_bf16 a[80:83], v[48:51], a[32:35], a[80:83]\n"
"ds_read_b128 v[80:83], v1 offset:33280\n"
"buffer_load_dwordx4 a[164:167], v0, s[8:11], s57 offen offset:0 sc1\n"
"v_mfma_f32_16x16x32_bf16 a[80:83], v[52:55], a[40:43], a[80:83]\n"
"ds_read_b128 v[84:87], v1 offset:34304\n"
"v_mul_f32 v231, v231, v130\n"
"v_mfma_f32_16x16x32_bf16 a[80:83], v[56:59], a[48:51], a[80:83]\n"
"ds_read_b128 v[88:91], v1 offset:35328\n"
"v_cvt_pk_bf16_f32 v150, v228, v229\n"
"v_mfma_f32_16x16x32_bf16 a[80:83], v[60:63], a[56:59], a[80:83]\n"
"ds_read_b128 v[92:95], v1 offset:36352\n"
"v_cvt_pk_bf16_f32 v151, v230, v231\n"
"v_mfma_f32_16x16x32_bf16 a[84:87], v[60:63], a[60:63], a[84:87]\n"
"buffer_store_dwordx4 v[148:151], v0, s[8:11], s95 offen offset:1024 sc1\n"
"v_mfma_f32_16x16x32_bf16 a[84:87], v[56:59], a[52:55], a[84:87]\n"
"v_mfma_f32_16x16x32_bf16 v[136:139], v[132:135], v[148:151], v[136:139]\n"
"v_mul_f32 v44, v44, v131\n"
"v_mul_f32 v45, v45, v131\n"
"v_mfma_f32_16x16x32_bf16 a[84:87], v[52:55], a[44:47], a[84:87]\n"
"v_mul_f32 v46, v46, v131\n"
"v_mul_f32 v47, v47, v131\n"
"v_mfma_f32_16x16x32_bf16 a[84:87], v[48:51], a[36:39], a[84:87]\n"
"s_waitcnt lgkmcnt(4)\n"
"v_cvt_pk_bf16_f32 v152, v44, v45\n"
"v_cvt_pk_bf16_f32 v153, v46, v47\n"
"v_mfma_f32_16x16x32_bf16 a[88:91], v[64:67], a[32:35], a[88:91]\n"
"ds_read_b128 v[48:51], v1 offset:37376\n"
"ds_read_b128 v[52:55], v1 offset:38400\n"
"v_mfma_f32_16x16x32_bf16 a[88:91], v[68:71], a[40:43], a[88:91]\n"
"ds_read_b128 v[56:59], v1 offset:39424\n"
"ds_read_b128 v[60:63], v1 offset:40448\n"
"v_mfma_f32_16x16x32_bf16 a[88:91], v[72:75], a[48:51], a[88:91]\n"
"buffer_load_dwordx4 a[172:175], v0, s[8:11], s57 offen offset:1024 sc1\n"
"v_accvgpr_read_b32 v236, a156\n"
"v_mfma_f32_16x16x32_bf16 a[88:91], v[76:79], a[56:59], a[88:91]\n"
"v_accvgpr_read_b32 v237, a157\n"
"v_accvgpr_read_b32 v238, a158\n"
"v_mfma_f32_16x16x32_bf16 a[92:95], v[76:79], a[60:63], a[92:95]\n"
"buffer_load_dwordx4 a[180:183], v0, s[8:11], s57 offen offset:2048 sc1\n"
"v_accvgpr_read_b32 v239, a159\n"
"v_mfma_f32_16x16x32_bf16 a[92:95], v[72:75], a[52:55], a[92:95]\n"
"v_mul_f32 v236, v236, v131\n"
"v_mul_f32 v237, v237, v131\n"
"v_mfma_f32_16x16x32_bf16 a[92:95], v[68:71], a[44:47], a[92:95]\n"
"v_mul_f32 v238, v238, v131\n"
"v_mul_f32 v239, v239, v131\n"
"v_mfma_f32_16x16x32_bf16 a[92:95], v[64:67], a[36:39], a[92:95]\n"
"s_waitcnt vmcnt(36) lgkmcnt(0)\n"
"v_cvt_pk_bf16_f32 v154, v236, v237\n"
"v_cvt_pk_bf16_f32 v155, v238, v239\n"
"v_mfma_f32_16x16x32_bf16 a[96:99], v[80:83], a[32:35], a[96:99]\n"
"s_barrier\n"
"ds_read_b128 v[64:67], v1 offset:58880\n"
"buffer_load_dwordx4 a[188:191], v0, s[8:11], s57 offen offset:3072 sc1\n"
"v_mfma_f32_16x16x32_bf16 a[96:99], v[84:87], a[40:43], a[96:99]\n"
"ds_read_b128 v[68:71], v1 offset:59904\n"
"buffer_store_dwordx4 v[152:155], v0, s[8:11], s96 offen offset:1024 sc1\n"
"v_mfma_f32_16x16x32_bf16 a[96:99], v[88:91], a[48:51], a[96:99]\n"
"v_mfma_f32_16x16x32_bf16 v[136:139], v[132:135], v[152:155], v[136:139]\n"
"ds_read_b128 v[72:75], v1 offset:60928\n"
"v_mul_f32 v96, v96, v128\n"
"v_mfma_f32_16x16x32_bf16 a[96:99], v[92:95], a[56:59], a[96:99]\n"
"ds_read_b128 v[76:79], v1 offset:61952\n"
"v_mul_f32 v97, v97, v128\n"
"v_mfma_f32_16x16x32_bf16 a[100:103], v[92:95], a[60:63], a[100:103]\n"
"v_mul_f32 v98, v98, v128\n"
"v_mfma_f32_16x16x32_bf16 a[100:103], v[88:91], a[52:55], a[100:103]\n"
"v_mul_f32 v99, v99, v128\n"
"v_cvt_pk_bf16_f32 v140, v96, v97\n"
"v_mfma_f32_16x16x32_bf16 a[100:103], v[84:87], a[44:47], a[100:103]\n"
"v_cvt_pk_bf16_f32 v141, v98, v99\n"
"v_mul_f32 v112, v112, v128\n"
"v_mfma_f32_16x16x32_bf16 a[100:103], v[80:83], a[36:39], a[100:103]\n"
"s_waitcnt lgkmcnt(4)\n"
"v_mul_f32 v113, v113, v128\n"
"v_mul_f32 v114, v114, v128\n"
"v_mfma_f32_16x16x32_bf16 a[104:107], v[48:51], a[32:35], a[104:107]\n"
"ds_read_b128 v[80:83], v1 offset:62976\n"
"v_mfma_f32_16x16x32_bf16 a[104:107], v[52:55], a[40:43], a[104:107]\n"
"ds_read_b128 v[84:87], v1 offset:64000\n"
"v_mul_f32 v115, v115, v128\n"
"v_mfma_f32_16x16x32_bf16 a[104:107], v[56:59], a[48:51], a[104:107]\n"
"ds_read_b128 v[88:91], v1 offset:65024\n"
"v_cvt_pk_bf16_f32 v142, v112, v113\n"
"v_mfma_f32_16x16x32_bf16 a[104:107], v[60:63], a[56:59], a[104:107]\n"
"ds_read_b128 v[92:95], v2 offset:512\n"
"v_cvt_pk_bf16_f32 v143, v114, v115\n"
"v_mfma_f32_16x16x32_bf16 a[108:111], v[60:63], a[60:63], a[108:111]\n"
"buffer_store_dwordx4 v[140:143], v0, s[8:11], s93 offen offset:0 sc1\n"
"v_mfma_f32_16x16x32_bf16 a[108:111], v[56:59], a[52:55], a[108:111]\n"
"v_mfma_f32_16x16x32_bf16 v[136:139], v[132:135], v[140:143], v[136:139]\n"
"v_mul_f32 v100, v100, v129\n"
"v_mul_f32 v101, v101, v129\n"
"v_mfma_f32_16x16x32_bf16 a[108:111], v[52:55], a[44:47], a[108:111]\n"
"v_mul_f32 v102, v102, v129\n"
"v_mul_f32 v103, v103, v129\n"
"v_mfma_f32_16x16x32_bf16 a[108:111], v[48:51], a[36:39], a[108:111]\n"
"s_waitcnt lgkmcnt(4)\n"
"v_cvt_pk_bf16_f32 v144, v100, v101\n"
"v_cvt_pk_bf16_f32 v145, v102, v103\n"
"v_mfma_f32_16x16x32_bf16 a[112:115], v[64:67], a[32:35], a[112:115]\n"
"ds_read_b128 v[48:51], v2 offset:1536\n"
"v_mfma_f32_16x16x32_bf16 a[112:115], v[68:71], a[40:43], a[112:115]\n"
"ds_read_b128 v[52:55], v2 offset:2560\n"
"v_mul_f32 v116, v116, v129\n"
"v_mfma_f32_16x16x32_bf16 a[112:115], v[72:75], a[48:51], a[112:115]\n"
"s_add_u32 m0, s55, 66560\n"
"ds_read_b128 v[56:59], v2 offset:3584\n"
"v_mul_f32 v117, v117, v129\n"
"v_mfma_f32_16x16x32_bf16 a[112:115], v[76:79], a[56:59], a[112:115]\n"
"s_add_u32 s59, s59, 16384\n"
"ds_read_b128 v[60:63], v2 offset:4608\n"
"v_mul_f32 v118, v118, v129\n"
"v_mfma_f32_16x16x32_bf16 a[116:119], v[76:79], a[60:63], a[116:119]\n"
"buffer_load_dwordx4 v0, s[8:11], s59 offen sc1 lds\n"
"v_mul_f32 v119, v119, v129\n"
"v_mfma_f32_16x16x32_bf16 a[116:119], v[72:75], a[52:55], a[116:119]\n"
"v_cvt_pk_bf16_f32 v146, v116, v117\n"
"v_cvt_pk_bf16_f32 v147, v118, v119\n"
"v_mfma_f32_16x16x32_bf16 a[116:119], v[68:71], a[44:47], a[116:119]\n"
"buffer_store_dwordx4 v[144:147], v0, s[8:11], s94 offen offset:0 sc1\n"
"v_mul_f32 v104, v104, v130\n"
"v_mfma_f32_16x16x32_bf16 v[136:139], v[132:135], v[144:147], v[136:139]\n"
"v_mfma_f32_16x16x32_bf16 a[116:119], v[64:67], a[36:39], a[116:119]\n"
"s_waitcnt lgkmcnt(4)\n"
"v_mul_f32 v105, v105, v130\n"
"v_mul_f32 v106, v106, v130\n"
"v_mfma_f32_16x16x32_bf16 v[8:11], v[80:83], a[32:35], v[8:11]\n"
"ds_read_b128 v[64:67], v2 offset:5632\n"
"v_mfma_f32_16x16x32_bf16 v[8:11], v[84:87], a[40:43], v[8:11]\n"
"ds_read_b128 v[68:71], v2 offset:6656\n"
"v_mul_f32 v107, v107, v130\n"
"v_mfma_f32_16x16x32_bf16 v[8:11], v[88:91], a[48:51], v[8:11]\n"
"ds_read_b128 v[72:75], v2 offset:7680\n"
"v_cvt_pk_bf16_f32 v148, v104, v105\n"
"v_mfma_f32_16x16x32_bf16 v[8:11], v[92:95], a[56:59], v[8:11]\n"
"ds_read_b128 v[76:79], v2 offset:8704\n"
"v_cvt_pk_bf16_f32 v149, v106, v107\n"
"v_mfma_f32_16x16x32_bf16 v[12:15], v[92:95], a[60:63], v[12:15]\n"
"v_mul_f32 v120, v120, v130\n"
"v_mfma_f32_16x16x32_bf16 v[12:15], v[88:91], a[52:55], v[12:15]\n"
"v_mul_f32 v121, v121, v130\n"
"v_mul_f32 v122, v122, v130\n"
"v_mfma_f32_16x16x32_bf16 v[12:15], v[84:87], a[44:47], v[12:15]\n"
"v_mul_f32 v123, v123, v130\n"
"v_cvt_pk_bf16_f32 v150, v120, v121\n"
"v_mfma_f32_16x16x32_bf16 v[12:15], v[80:83], a[36:39], v[12:15]\n"
"s_waitcnt lgkmcnt(4)\n"
"buffer_load_dwordx4 v0, s[8:11], s59 offen offset:1024 sc1 lds\n"
"v_cvt_pk_bf16_f32 v151, v122, v123\n"
"v_mfma_f32_16x16x32_bf16 v[16:19], v[48:51], a[32:35], v[16:19]\n"
"ds_read_b128 v[80:83], v0 offset:0\n"
"v_mfma_f32_16x16x32_bf16 v[16:19], v[52:55], a[40:43], v[16:19]\n"
"ds_read_b128 v[84:87], v0 offset:1024\n"
"buffer_store_dwordx4 v[148:151], v0, s[8:11], s95 offen offset:0 sc1\n"
"v_mfma_f32_16x16x32_bf16 v[16:19], v[56:59], a[48:51], v[16:19]\n"
"v_mfma_f32_16x16x32_bf16 v[136:139], v[132:135], v[148:151], v[136:139]\n"
"ds_read_b128 v[88:91], v0 offset:2048\n"
"v_mul_f32 v108, v108, v131\n"
"v_mfma_f32_16x16x32_bf16 v[16:19], v[60:63], a[56:59], v[16:19]\n"
"ds_read_b128 v[92:95], v0 offset:3072\n"
"v_mul_f32 v109, v109, v131\n"
"v_mfma_f32_16x16x32_bf16 v[20:23], v[60:63], a[60:63], v[20:23]\n"
"v_mul_f32 v110, v110, v131\n"
"v_mfma_f32_16x16x32_bf16 v[20:23], v[56:59], a[52:55], v[20:23]\n"
"v_mul_f32 v111, v111, v131\n"
"v_cvt_pk_bf16_f32 v152, v108, v109\n"
"v_mfma_f32_16x16x32_bf16 v[20:23], v[52:55], a[44:47], v[20:23]\n"
"v_cvt_pk_bf16_f32 v153, v110, v111\n"
"v_mul_f32 v124, v124, v131\n"
"v_mfma_f32_16x16x32_bf16 v[20:23], v[48:51], a[36:39], v[20:23]\n"
"s_waitcnt vmcnt(53) lgkmcnt(4)\n"
"v_mul_f32 v125, v125, v131\n"
"v_mul_f32 v126, v126, v131\n"
"v_mfma_f32_16x16x32_bf16 a[64:67], v[64:67], v[160:163], a[64:67]\n"
"ds_read_b128 v[48:51], v0 offset:4096\n"
"buffer_load_dwordx4 v0, s[8:11], s59 offen offset:2048 sc1 lds\n"
"v_mfma_f32_16x16x32_bf16 a[64:67], v[68:71], v[168:171], a[64:67]\n"
"ds_read_b128 v[52:55], v0 offset:5120\n"
"v_mul_f32 v127, v127, v131\n"
"v_mfma_f32_16x16x32_bf16 a[64:67], v[72:75], v[176:179], a[64:67]\n"
"ds_read_b128 v[56:59], v0 offset:6144\n"
"v_cvt_pk_bf16_f32 v154, v124, v125\n"
"v_mfma_f32_16x16x32_bf16 a[64:67], v[76:79], v[184:187], a[64:67]\n"
"ds_read_b128 v[60:63], v0 offset:7168\n"
"v_cvt_pk_bf16_f32 v155, v126, v127\n"
"v_mfma_f32_16x16x32_bf16 a[68:71], v[76:79], v[188:191], a[68:71]\n"
"buffer_store_dwordx4 v[152:155], v0, s[8:11], s96 offen offset:0 sc1\n"
"v_mfma_f32_16x16x32_bf16 a[68:71], v[72:75], v[180:183], a[68:71]\n"
"v_mfma_f32_16x16x32_bf16 v[136:139], v[132:135], v[152:155], v[136:139]\n"
"v_mfma_f32_16x16x32_bf16 a[68:71], v[68:71], v[172:175], a[68:71]\n"
"v_mfma_f32_16x16x32_bf16 a[68:71], v[64:67], v[164:167], a[68:71]\n"
"s_waitcnt lgkmcnt(4)\n"
"v_mfma_f32_16x16x32_bf16 a[72:75], v[80:83], v[160:163], a[72:75]\n"
"ds_read_b128 v[64:67], v0 offset:8192\n"
"v_mfma_f32_16x16x32_bf16 a[72:75], v[84:87], v[168:171], a[72:75]\n"
"ds_read_b128 v[68:71], v0 offset:9216\n"
"v_cmp_u_f32 vcc, v136, v136\n"
"v_mfma_f32_16x16x32_bf16 a[72:75], v[88:91], v[176:179], a[72:75]\n"
"ds_read_b128 v[72:75], v0 offset:10240\n"
"v_mov_b32 v137, 162304\n"
"v_mfma_f32_16x16x32_bf16 a[72:75], v[92:95], v[184:187], a[72:75]\n"
"ds_read_b128 v[76:79], v0 offset:11264\n"
"v_cndmask_b32 v31, v31, v30, vcc\n"
"v_mfma_f32_16x16x32_bf16 a[76:79], v[92:95], v[188:191], a[76:79]\n"
"buffer_load_dwordx4 v0, s[8:11], s59 offen offset:3072 sc1 lds\n"
"v_cmp_ne_u32 vcc, 0, v31\n"
"v_mfma_f32_16x16x32_bf16 a[76:79], v[88:91], v[180:183], a[76:79]\n"
"v_mfma_f32_16x16x32_bf16 a[76:79], v[84:87], v[172:175], a[76:79]\n"
"s_or_b32 s34, vcc_lo, vcc_hi\n"
"v_mfma_f32_16x16x32_bf16 a[76:79], v[80:83], v[164:167], a[76:79]\n"
"s_waitcnt lgkmcnt(4)\n"
"v_mov_b32 v31, s34\n"
"v_lshl_add_u32 v30, s52, 2, v137\n"
"v_mfma_f32_16x16x32_bf16 a[80:83], v[48:51], v[160:163], a[80:83]\n"
"s_add_u32 m0, s55, 107520\n"
"ds_read_b128 v[80:83], v0 offset:12288\n"
"ds_read_b128 v[84:87], v0 offset:13312\n"
"v_mfma_f32_16x16x32_bf16 a[80:83], v[52:55], v[168:171], a[80:83]\n"
"s_add_u32 s59, s59, 16384\n"
"ds_read_b128 v[88:91], v0 offset:14336\n"
"ds_read_b128 v[92:95], v0 offset:15360\n"
"v_mfma_f32_16x16x32_bf16 a[80:83], v[56:59], v[176:179], a[80:83]\n"
"ds_write_b32 v30, v31\n"
"buffer_load_dwordx4 v0, s[8:11], s59 offen sc1 lds\n"
"v_mfma_f32_16x16x32_bf16 a[80:83], v[60:63], v[184:187], a[80:83]\n"
"buffer_load_dwordx4 v0, s[8:11], s59 offen offset:1024 sc1 lds\n"
"v_mfma_f32_16x16x32_bf16 a[84:87], v[60:63], v[188:191], a[84:87]\n"
"v_mfma_f32_16x16x32_bf16 a[84:87], v[56:59], v[180:183], a[84:87]\n"
"v_mfma_f32_16x16x32_bf16 a[84:87], v[52:55], v[172:175], a[84:87]\n"
"v_mfma_f32_16x16x32_bf16 a[84:87], v[48:51], v[164:167], a[84:87]\n"
"s_waitcnt vmcnt(34) lgkmcnt(0)\n"
"buffer_load_dwordx4 v0, s[8:11], s59 offen offset:2048 sc1 lds\n"
"v_mfma_f32_16x16x32_bf16 a[88:91], v[64:67], v[160:163], a[88:91]\n"
"s_barrier\n"
"ds_read_b128 v[48:51], v0 offset:16384\n"
"v_mfma_f32_16x16x32_bf16 a[88:91], v[68:71], v[168:171], a[88:91]\n"
"ds_read_b128 v[52:55], v0 offset:17408\n"
"v_mfma_f32_16x16x32_bf16 a[88:91], v[72:75], v[176:179], a[88:91]\n"
"ds_read_b128 v[56:59], v0 offset:18432\n"
"v_mfma_f32_16x16x32_bf16 a[88:91], v[76:79], v[184:187], a[88:91]\n"
"ds_read_b128 v[60:63], v0 offset:19456\n"
"buffer_load_dwordx4 v0, s[8:11], s59 offen offset:3072 sc1 lds\n"
"v_mfma_f32_16x16x32_bf16 a[92:95], v[76:79], v[188:191], a[92:95]\n"
"ds_read_b128 v[136:139], v137 offset:0\n"
"v_mfma_f32_16x16x32_bf16 a[92:95], v[72:75], v[180:183], a[92:95]\n"
"s_add_u32 m0, s55, 90624\n"
"v_mfma_f32_16x16x32_bf16 a[92:95], v[68:71], v[172:175], a[92:95]\n"
"s_add_u32 s59, s59, 16384\n"
"v_mfma_f32_16x16x32_bf16 a[92:95], v[64:67], v[164:167], a[92:95]\n"
"s_waitcnt lgkmcnt(6)\n"
"buffer_load_dwordx4 v0, s[8:11], s59 offen sc1 lds\n"
"v_mfma_f32_16x16x32_bf16 a[96:99], v[80:83], v[160:163], a[96:99]\n"
"ds_read_b128 v[64:67], v0 offset:20480\n"
"v_mfma_f32_16x16x32_bf16 a[96:99], v[84:87], v[168:171], a[96:99]\n"
"ds_read_b128 v[68:71], v0 offset:21504\n"
"v_mfma_f32_16x16x32_bf16 a[96:99], v[88:91], v[176:179], a[96:99]\n"
"s_waitcnt lgkmcnt(2)\n"
"ds_read_b128 v[72:75], v0 offset:22528\n"
"v_mfma_f32_16x16x32_bf16 a[96:99], v[92:95], v[184:187], a[96:99]\n"
"ds_read_b128 v[76:79], v0 offset:23552\n"
"v_or_b32 v136, v136, v137\n"
"v_mfma_f32_16x16x32_bf16 a[100:103], v[92:95], v[188:191], a[100:103]\n"
"buffer_load_dwordx4 v0, s[8:11], s59 offen offset:1024 sc1 lds\n"
"v_or3_b32 v136, v136, v138, v139\n"
"v_mfma_f32_16x16x32_bf16 a[100:103], v[88:91], v[180:183], a[100:103]\n"
"v_readfirstlane_b32 s34, v136\n"
"v_mfma_f32_16x16x32_bf16 a[100:103], v[84:87], v[172:175], a[100:103]\n"
"v_mfma_f32_16x16x32_bf16 a[100:103], v[80:83], v[164:167], a[100:103]\n"
"s_waitcnt lgkmcnt(5)\n"
"buffer_load_dwordx4 v0, s[8:11], s59 offen offset:2048 sc1 lds\n"
"v_mfma_f32_16x16x32_bf16 a[104:107], v[48:51], v[160:163], a[104:107]\n"
"ds_read_b128 v[80:83], v0 offset:24576\n"
"v_mfma_f32_16x16x32_bf16 a[104:107], v[52:55], v[168:171], a[104:107]\n"
"ds_read_b128 v[84:87], v0 offset:25600\n"
"v_mfma_f32_16x16x32_bf16 a[104:107], v[56:59], v[176:179], a[104:107]\n"
"ds_read_b128 v[88:91], v0 offset:26624\n"
"v_mfma_f32_16x16x32_bf16 a[104:107], v[60:63], v[184:187], a[104:107]\n"
"ds_read_b128 v[92:95], v0 offset:27648\n"
"buffer_load_dwordx4 v0, s[8:11], s59 offen offset:3072 sc1 lds\n"
"v_mfma_f32_16x16x32_bf16 a[108:111], v[60:63], v[188:191], a[108:111]\n"
"s_min_u32 s34, s34, 1\n"
"s_cmp_gt_u32 s34, s85\n"
"s_cbranch_scc1 L_fb\n"
"v_mfma_f32_16x16x32_bf16 a[108:111], v[56:59], v[180:183], a[108:111]\n"
"s_add_u32 m0, s55, 124416\n"
"v_bfe_u32 v3, v0, 4, 4\n"
"v_mfma_f32_16x16x32_bf16 a[108:111], v[52:55], v[172:175], a[108:111]\n"
"s_add_u32 s59, s59, 16384\n"
"v_mul_u32_u24 v3, 1152, v3\n"
"v_mfma_f32_16x16x32_bf16 a[108:111], v[48:51], v[164:167], a[108:111]\n"
"s_waitcnt lgkmcnt(4)\n"
"buffer_load_dwordx4 v0, s[8:11], s59 offen sc1 lds\n"
"v_lshrrev_b32 v24, 8, v0\n"
"v_mfma_f32_16x16x32_bf16 a[112:115], v[64:67], v[160:163], a[112:115]\n"
"ds_read_b128 v[48:51], v0 offset:28672\n"
"v_lshl_add_u32 v3, v24, 3, v3\n"
"v_mfma_f32_16x16x32_bf16 a[112:115], v[68:71], v[168:171], a[112:115]\n"
"ds_read_b128 v[52:55], v0 offset:29696\n"
"v_and_b32 v24, 1, v24\n"
"v_mfma_f32_16x16x32_bf16 a[112:115], v[72:75], v[176:179], a[112:115]\n"
"ds_read_b128 v[56:59], v0 offset:30720\n"
"v_mad_u32_u24 v254, v24, 24, v3\n"
"v_mfma_f32_16x16x32_bf16 a[112:115], v[76:79], v[184:187], a[112:115]\n"
"ds_read_b128 v[60:63], v0 offset:31744\n"
"buffer_load_dwordx4 v0, s[8:11], s59 offen offset:1024 sc1 lds\n"
"v_mfma_f32_16x16x32_bf16 a[116:119], v[76:79], v[188:191], a[116:119]\n"
"v_mfma_f32_16x16x32_bf16 a[116:119], v[72:75], v[180:183], a[116:119]\n"
"v_mfma_f32_16x16x32_bf16 a[116:119], v[68:71], v[172:175], a[116:119]\n"
"v_mfma_f32_16x16x32_bf16 a[116:119], v[64:67], v[164:167], a[116:119]\n"
"s_waitcnt lgkmcnt(4)\n"
"buffer_load_dwordx4 v0, s[8:11], s59 offen offset:2048 sc1 lds\n"
"v_mfma_f32_16x16x32_bf16 v[8:11], v[80:83], v[160:163], v[8:11]\n"
"ds_read_b128 v[64:67], v0 offset:33792\n"
"v_mfma_f32_16x16x32_bf16 v[8:11], v[84:87], v[168:171], v[8:11]\n"
"ds_read_b128 v[68:71], v0 offset:34816\n"
"v_mfma_f32_16x16x32_bf16 v[8:11], v[88:91], v[176:179], v[8:11]\n"
"ds_read_b128 v[72:75], v0 offset:35840\n"
"v_mfma_f32_16x16x32_bf16 v[8:11], v[92:95], v[184:187], v[8:11]\n"
"ds_read_b128 v[76:79], v0 offset:36864\n"
"buffer_load_dwordx4 v0, s[8:11], s59 offen offset:3072 sc1 lds\n"
"v_mfma_f32_16x16x32_bf16 v[12:15], v[92:95], v[188:191], v[12:15]\n"
"v_mfma_f32_16x16x32_bf16 v[12:15], v[88:91], v[180:183], v[12:15]\n"
"v_mfma_f32_16x16x32_bf16 v[12:15], v[84:87], v[172:175], v[12:15]\n"
"s_add_u32 s56, s56, 4096\n"
"v_mfma_f32_16x16x32_bf16 v[12:15], v[80:83], v[164:167], v[12:15]\n"
"s_waitcnt lgkmcnt(4)\n"
"buffer_load_dwordx4 a[224:227], v0, s[8:11], s56 offen offset:0 sc1\n"
"v_mfma_f32_16x16x32_bf16 v[16:19], v[48:51], v[160:163], v[16:19]\n"
"ds_read_b128 v[80:83], v0 offset:37888\n"
"v_mfma_f32_16x16x32_bf16 v[16:19], v[52:55], v[168:171], v[16:19]\n"
"ds_read_b128 v[84:87], v0 offset:38912\n"
"v_mfma_f32_16x16x32_bf16 v[16:19], v[56:59], v[176:179], v[16:19]\n"
"ds_read_b128 v[88:91], v0 offset:39936\n"
"v_mfma_f32_16x16x32_bf16 v[16:19], v[60:63], v[184:187], v[16:19]\n"
"ds_read_b128 v[92:95], v0 offset:40960\n"
"buffer_load_dwordx4 a[232:235], v0, s[8:11], s56 offen offset:1024 sc1\n"
"v_mfma_f32_16x16x32_bf16 v[20:23], v[60:63], v[188:191], v[20:23]\n"
"v_mfma_f32_16x16x32_bf16 v[20:23], v[56:59], v[180:183], v[20:23]\n"
"v_mfma_f32_16x16x32_bf16 v[20:23], v[52:55], v[172:175], v[20:23]\n"
"v_mfma_f32_16x16x32_bf16 v[20:23], v[48:51], v[164:167], v[20:23]\n"
"s_waitcnt vmcnt(52) lgkmcnt(4)\n"
"buffer_load_dwordx4 a[240:243], v0, s[8:11], s56 offen offset:2048 sc1\n"
"v_mfma_f32_16x16x32_bf16 a[64:67], v[64:67], v[192:195], a[64:67]\n"
"ds_read_b128 v[48:51], v0 offset:41984\n"
"v_mfma_f32_16x16x32_bf16 a[64:67], v[68:71], v[200:203], a[64:67]\n"
"ds_read_b128 v[52:55], v0 offset:43008\n"
"v_mfma_f32_16x16x32_bf16 a[64:67], v[72:75], v[208:211], a[64:67]\n"
"ds_read_b128 v[56:59], v0 offset:44032\n"
"v_mfma_f32_16x16x32_bf16 a[64:67], v[76:79], v[216:219], a[64:67]\n"
"ds_read_b128 v[60:63], v0 offset:45056\n"
"buffer_load_dwordx4 a[248:251], v0, s[8:11], s56 offen offset:3072 sc1\n"
"v_mfma_f32_16x16x32_bf16 a[68:71], v[76:79], v[220:223], a[68:71]\n"
"v_mfma_f32_16x16x32_bf16 a[68:71], v[72:75], v[212:215], a[68:71]\n"
"v_mfma_f32_16x16x32_bf16 a[68:71], v[68:71], v[204:207], a[68:71]\n"
"s_add_u32 s57, s57, 4096\n"
"v_mfma_f32_16x16x32_bf16 a[68:71], v[64:67], v[196:199], a[68:71]\n"
"s_waitcnt lgkmcnt(4)\n"
"buffer_load_dwordx4 a[228:231], v0, s[8:11], s57 offen offset:0 sc1\n"
"v_mfma_f32_16x16x32_bf16 a[72:75], v[80:83], v[192:195], a[72:75]\n"
"ds_read_b128 v[64:67], v0 offset:46080\n"
"ds_read_b128 v[68:71], v0 offset:47104\n"
"v_mfma_f32_16x16x32_bf16 a[72:75], v[84:87], v[200:203], a[72:75]\n"
"ds_read_b128 v[72:75], v0 offset:48128\n"
"ds_read_b128 v[76:79], v0 offset:49152\n"
"v_mfma_f32_16x16x32_bf16 a[72:75], v[88:91], v[208:211], a[72:75]\n"
"v_mfma_f32_16x16x32_bf16 a[72:75], v[92:95], v[216:219], a[72:75]\n"
"buffer_load_dwordx4 a[236:239], v0, s[8:11], s57 offen offset:1024 sc1\n"
"v_mfma_f32_16x16x32_bf16 a[76:79], v[92:95], v[220:223], a[76:79]\n"
"v_mfma_f32_16x16x32_bf16 a[76:79], v[88:91], v[212:215], a[76:79]\n"
"v_mfma_f32_16x16x32_bf16 a[76:79], v[84:87], v[204:207], a[76:79]\n"
"s_waitcnt vmcnt(19)\n"
"v_mfma_f32_16x16x32_bf16 a[76:79], v[80:83], v[196:199], a[76:79]\n"
"s_waitcnt vmcnt(34) lgkmcnt(0)\n"
"buffer_load_dwordx4 a[244:247], v0, s[8:11], s57 offen offset:2048 sc1\n"
"v_mfma_f32_16x16x32_bf16 a[80:83], v[48:51], v[192:195], a[80:83]\n"
"s_barrier\n"
"ds_read_b128 v[80:83], v0 offset:50176\n"
"v_mfma_f32_16x16x32_bf16 a[80:83], v[52:55], v[200:203], a[80:83]\n"
"ds_read_b128 v[84:87], v0 offset:51200\n"
"v_mfma_f32_16x16x32_bf16 a[80:83], v[56:59], v[208:211], a[80:83]\n"
"ds_read_b128 v[88:91], v0 offset:52224\n"
"v_mfma_f32_16x16x32_bf16 a[80:83], v[60:63], v[216:219], a[80:83]\n"
"ds_read_b128 v[92:95], v0 offset:53248\n"
"buffer_load_dwordx4 a[252:255], v0, s[8:11], s57 offen offset:3072 sc1\n"
"v_mfma_f32_16x16x32_bf16 a[84:87], v[60:63], v[220:223], a[84:87]\n"
"s_cmp_eq_u32 s52, 0\n"
"s_cbranch_scc1 L_po\n"
"L_por:\n"
"v_mfma_f32_16x16x32_bf16 a[84:87], v[56:59], v[212:215], a[84:87]\n"
"s_add_u32 m0, s55, 0\n"
"v_mfma_f32_16x16x32_bf16 a[84:87], v[52:55], v[204:207], a[84:87]\n"
"s_add_u32 s59, s59, 16384\n"
"v_mfma_f32_16x16x32_bf16 a[84:87], v[48:51], v[196:199], a[84:87]\n"
"s_waitcnt lgkmcnt(4)\n"
"buffer_load_dwordx4 v0, s[8:11], s59 offen sc1 lds\n"
"v_mfma_f32_16x16x32_bf16 a[88:91], v[64:67], v[192:195], a[88:91]\n"
"ds_read_b128 v[48:51], v0 offset:54272\n"
"v_mfma_f32_16x16x32_bf16 a[88:91], v[68:71], v[200:203], a[88:91]\n"
"ds_read_b128 v[52:55], v0 offset:55296\n"
"v_mfma_f32_16x16x32_bf16 a[88:91], v[72:75], v[208:211], a[88:91]\n"
"ds_read_b128 v[56:59], v0 offset:56320\n"
"v_mfma_f32_16x16x32_bf16 a[88:91], v[76:79], v[216:219], a[88:91]\n"
"ds_read_b128 v[60:63], v0 offset:57344\n"
"buffer_load_dwordx4 v0, s[8:11], s59 offen offset:1024 sc1 lds\n"
"v_mfma_f32_16x16x32_bf16 a[92:95], v[76:79], v[220:223], a[92:95]\n"
"v_mfma_f32_16x16x32_bf16 a[92:95], v[72:75], v[212:215], a[92:95]\n"
"v_mfma_f32_16x16x32_bf16 a[92:95], v[68:71], v[204:207], a[92:95]\n"
"v_mfma_f32_16x16x32_bf16 a[92:95], v[64:67], v[196:199], a[92:95]\n"
"s_waitcnt lgkmcnt(4)\n"
"buffer_load_dwordx4 v0, s[8:11], s59 offen offset:2048 sc1 lds\n"
"v_mfma_f32_16x16x32_bf16 a[96:99], v[80:83], v[192:195], a[96:99]\n"
"ds_read_b128 v[64:67], v0 offset:58368\n"
"v_mfma_f32_16x16x32_bf16 a[96:99], v[84:87], v[200:203], a[96:99]\n"
"ds_read_b128 v[68:71], v0 offset:59392\n"
"v_mfma_f32_16x16x32_bf16 a[96:99], v[88:91], v[208:211], a[96:99]\n"
"ds_read_b128 v[72:75], v0 offset:60416\n"
"v_mfma_f32_16x16x32_bf16 a[96:99], v[92:95], v[216:219], a[96:99]\n"
"ds_read_b128 v[76:79], v0 offset:61440\n"
"buffer_load_dwordx4 v0, s[8:11], s59 offen offset:3072 sc1 lds\n"
"v_mfma_f32_16x16x32_bf16 a[100:103], v[92:95], v[220:223], a[100:103]\n"
"v_mfma_f32_16x16x32_bf16 a[100:103], v[88:91], v[212:215], a[100:103]\n"
"s_add_u32 m0, s55, 16384\n"
"v_mfma_f32_16x16x32_bf16 a[100:103], v[84:87], v[204:207], a[100:103]\n"
"s_add_u32 s59, s59, 16384\n"
"v_mfma_f32_16x16x32_bf16 a[100:103], v[80:83], v[196:199], a[100:103]\n"
"s_waitcnt lgkmcnt(4)\n"
"buffer_load_dwordx4 v0, s[8:11], s59 offen sc1 lds\n"
"v_mfma_f32_16x16x32_bf16 a[104:107], v[48:51], v[192:195], a[104:107]\n"
"ds_read_b128 v[80:83], v0 offset:62464\n"
"v_mfma_f32_16x16x32_bf16 a[104:107], v[52:55], v[200:203], a[104:107]\n"
"ds_read_b128 v[84:87], v0 offset:63488\n"
"v_mfma_f32_16x16x32_bf16 a[104:107], v[56:59], v[208:211], a[104:107]\n"
"ds_read_b128 v[88:91], v0 offset:64512\n"
"v_mfma_f32_16x16x32_bf16 a[104:107], v[60:63], v[216:219], a[104:107]\n"
"ds_read_b128 v[92:95], v1 offset:0\n"
"buffer_load_dwordx4 v0, s[8:11], s59 offen offset:1024 sc1 lds\n"
"v_mfma_f32_16x16x32_bf16 a[108:111], v[60:63], v[220:223], a[108:111]\n"
"v_mfma_f32_16x16x32_bf16 a[108:111], v[56:59], v[212:215], a[108:111]\n"
"v_mfma_f32_16x16x32_bf16 a[108:111], v[52:55], v[204:207], a[108:111]\n"
"v_mfma_f32_16x16x32_bf16 a[108:111], v[48:51], v[196:199], a[108:111]\n"
"s_waitcnt lgkmcnt(4)\n"
"buffer_load_dwordx4 v0, s[8:11], s59 offen offset:2048 sc1 lds\n"
"v_mfma_f32_16x16x32_bf16 a[112:115], v[64:67], v[192:195], a[112:115]\n"
"ds_read_b128 v[48:51], v2 offset:10240\n"
"v_mfma_f32_16x16x32_bf16 a[112:115], v[68:71], v[200:203], a[112:115]\n"
"ds_read_b128 v[52:55], v2 offset:11264\n"
"v_mfma_f32_16x16x32_bf16 a[112:115], v[72:75], v[208:211], a[112:115]\n"
"ds_read_b128 v[56:59], v2 offset:12288\n"
"v_mfma_f32_16x16x32_bf16 a[112:115], v[76:79], v[216:219], a[112:115]\n"
"ds_read_b128 v[60:63], v2 offset:13312\n"
"buffer_load_dwordx4 v0, s[8:11], s59 offen offset:3072 sc1 lds\n"
"v_mfma_f32_16x16x32_bf16 a[116:119], v[76:79], v[220:223], a[116:119]\n"
"v_mfma_f32_16x16x32_bf16 a[116:119], v[72:75], v[212:215], a[116:119]\n"
"v_mfma_f32_16x16x32_bf16 a[116:119], v[68:71], v[204:207], a[116:119]\n"
"s_add_u32 s56, s56, 4096\n"
"v_mfma_f32_16x16x32_bf16 a[116:119], v[64:67], v[196:199], a[116:119]\n"
"s_waitcnt lgkmcnt(4)\n"
"buffer_load_dwordx4 a[192:195], v0, s[8:11], s56 offen offset:0 sc1\n"
"v_mfma_f32_16x16x32_bf16 v[8:11], v[80:83], v[192:195], v[8:11]\n"
"ds_read_b128 v[64:67], v2 offset:14336\n"
"v_mfma_f32_16x16x32_bf16 v[8:11], v[84:87], v[200:203], v[8:11]\n"
"ds_read_b128 v[68:71], v2 offset:15360\n"
"v_mfma_f32_16x16x32_bf16 v[8:11], v[88:91], v[208:211], v[8:11]\n"
"ds_read_b128 v[72:75], v2 offset:16384\n"
"v_mfma_f32_16x16x32_bf16 v[8:11], v[92:95], v[216:219], v[8:11]\n"
"ds_read_b128 v[76:79], v2 offset:17408\n"
"buffer_load_dwordx4 a[200:203], v0, s[8:11], s56 offen offset:1024 sc1\n"
"v_mfma_f32_16x16x32_bf16 v[12:15], v[92:95], v[220:223], v[12:15]\n"
"v_mfma_f32_16x16x32_bf16 v[12:15], v[88:91], v[212:215], v[12:15]\n"
"v_mfma_f32_16x16x32_bf16 v[12:15], v[84:87], v[204:207], v[12:15]\n"
"v_mfma_f32_16x16x32_bf16 v[12:15], v[80:83], v[196:199], v[12:15]\n"
"s_waitcnt lgkmcnt(4)\n"
"buffer_load_dwordx4 a[208:211], v0, s[8:11], s56 offen offset:2048 sc1\n"
"v_mfma_f32_16x16x32_bf16 v[16:19], v[48:51], v[192:195], v[16:19]\n"
"ds_read_b128 v[80:83], v2 offset:18432\n"
"v_mfma_f32_16x16x32_bf16 v[16:19], v[52:55], v[200:203], v[16:19]\n"
"ds_read_b128 v[84:87], v2 offset:19456\n"
"v_mfma_f32_16x16x32_bf16 v[16:19], v[56:59], v[208:211], v[16:19]\n"
"ds_read_b128 v[88:91], v2 offset:20480\n"
"v_mfma_f32_16x16x32_bf16 v[16:19], v[60:63], v[216:219], v[16:19]\n"
"ds_read_b128 v[92:95], v2 offset:21504\n"
"buffer_load_dwordx4 a[216:219], v0, s[8:11], s56 offen offset:3072 sc1\n"
"v_mfma_f32_16x16x32_bf16 v[20:23], v[60:63], v[220:223], v[20:23]\n"
"v_mfma_f32_16x16x32_bf16 v[20:23], v[56:59], v[212:215], v[20:23]\n"
"v_mfma_f32_16x16x32_bf16 v[20:23], v[52:55], v[204:207], v[20:23]\n"
"s_add_u32 s57, s57, 4096\n"
"v_mfma_f32_16x16x32_bf16 v[20:23], v[48:51], v[196:199], v[20:23]\n"
"s_waitcnt vmcnt(41) lgkmcnt(4)\n"
"buffer_load_dwordx4 a[196:199], v0, s[8:11], s57 offen offset:0 sc1\n"
"v_mfma_f32_16x16x32_bf16 a[64:67], v[64:67], a[160:163], a[64:67]\n"
"ds_read_b128 v[48:51], v2 offset:22528\n"
"ds_read_b128 v[52:55], v2 offset:23552\n"
"v_mfma_f32_16x16x32_bf16 a[64:67], v[68:71], a[168:171], a[64:67]\n"
"ds_read_b128 v[56:59], v2 offset:24576\n"
"ds_read_b128 v[60:63], v2 offset:25600\n"
"v_mfma_f32_16x16x32_bf16 a[64:67], v[72:75], a[176:179], a[64:67]\n"
"v_mfma_f32_16x16x32_bf16 a[64:67], v[76:79], a[184:187], a[64:67]\n"
"buffer_load_dwordx4 a[204:207], v0, s[8:11], s57 offen offset:1024 sc1\n"
"v_mfma_f32_16x16x32_bf16 a[68:71], v[76:79], a[188:191], a[68:71]\n"
"v_mfma_f32_16x16x32_bf16 a[68:71], v[72:75], a[180:183], a[68:71]\n"
"v_mfma_f32_16x16x32_bf16 a[68:71], v[68:71], a[172:175], a[68:71]\n"
"v_mfma_f32_16x16x32_bf16 a[68:71], v[64:67], a[164:167], a[68:71]\n"
"s_waitcnt vmcnt(30) lgkmcnt(0)\n"
"buffer_load_dwordx4 a[212:215], v0, s[8:11], s57 offen offset:2048 sc1\n"
"v_mfma_f32_16x16x32_bf16 a[72:75], v[80:83], a[160:163], a[72:75]\n"
"s_barrier\n"
"ds_read_b128 v[64:67], v1 offset:1024\n"
"v_mfma_f32_16x16x32_bf16 a[72:75], v[84:87], a[168:171], a[72:75]\n"
"ds_read_b128 v[68:71], v1 offset:2048\n"
"v_mfma_f32_16x16x32_bf16 a[72:75], v[88:91], a[176:179], a[72:75]\n"
"ds_read_b128 v[72:75], v1 offset:3072\n"
"v_mfma_f32_16x16x32_bf16 a[72:75], v[92:95], a[184:187], a[72:75]\n"
"ds_read_b128 v[76:79], v1 offset:4096\n"
"buffer_load_dwordx4 a[220:223], v0, s[8:11], s57 offen offset:3072 sc1\n"
"v_mfma_f32_16x16x32_bf16 a[76:79], v[92:95], a[188:191], a[76:79]\n"
"v_mfma_f32_16x16x32_bf16 a[76:79], v[88:91], a[180:183], a[76:79]\n"
"s_add_u32 m0, s55, 33792\n"
"v_mfma_f32_16x16x32_bf16 a[76:79], v[84:87], a[172:175], a[76:79]\n"
"s_add_u32 s59, s59, 16384\n"
"v_mfma_f32_16x16x32_bf16 a[76:79], v[80:83], a[164:167], a[76:79]\n"
"s_waitcnt lgkmcnt(4)\n"
"buffer_load_dwordx4 v0, s[8:11], s59 offen sc1 lds\n"
"v_mfma_f32_16x16x32_bf16 a[80:83], v[48:51], a[160:163], a[80:83]\n"
"ds_read_b128 v[80:83], v1 offset:5120\n"
"v_mfma_f32_16x16x32_bf16 a[80:83], v[52:55], a[168:171], a[80:83]\n"
"ds_read_b128 v[84:87], v1 offset:6144\n"
"v_mfma_f32_16x16x32_bf16 a[80:83], v[56:59], a[176:179], a[80:83]\n"
"ds_read_b128 v[88:91], v1 offset:7168\n"
"v_mfma_f32_16x16x32_bf16 a[80:83], v[60:63], a[184:187], a[80:83]\n"
"ds_read_b128 v[92:95], v1 offset:8192\n"
"buffer_load_dwordx4 v0, s[8:11], s59 offen offset:1024 sc1 lds\n"
"v_mfma_f32_16x16x32_bf16 a[84:87], v[60:63], a[188:191], a[84:87]\n"
"v_mfma_f32_16x16x32_bf16 a[84:87], v[56:59], a[180:183], a[84:87]\n"
"v_mfma_f32_16x16x32_bf16 a[84:87], v[52:55], a[172:175], a[84:87]\n"
"v_mfma_f32_16x16x32_bf16 a[84:87], v[48:51], a[164:167], a[84:87]\n"
"s_waitcnt lgkmcnt(4)\n"
"buffer_load_dwordx4 v0, s[8:11], s59 offen offset:2048 sc1 lds\n"
"v_mfma_f32_16x16x32_bf16 a[88:91], v[64:67], a[160:163], a[88:91]\n"
"ds_read_b128 v[48:51], v1 offset:9216\n"
"v_mfma_f32_16x16x32_bf16 a[88:91], v[68:71], a[168:171], a[88:91]\n"
"ds_read_b128 v[52:55], v1 offset:10240\n"
"v_mfma_f32_16x16x32_bf16 a[88:91], v[72:75], a[176:179], a[88:91]\n"
"ds_read_b128 v[56:59], v1 offset:11264\n"
"v_mfma_f32_16x16x32_bf16 a[88:91], v[76:79], a[184:187], a[88:91]\n"
"ds_read_b128 v[60:63], v1 offset:12288\n"
"buffer_load_dwordx4 v0, s[8:11], s59 offen offset:3072 sc1 lds\n"
"v_mfma_f32_16x16x32_bf16 a[92:95], v[76:79], a[188:191], a[92:95]\n"
"v_mfma_f32_16x16x32_bf16 a[92:95], v[72:75], a[180:183], a[92:95]\n"
"s_add_u32 m0, s55, 50176\n"
"v_mfma_f32_16x16x32_bf16 a[92:95], v[68:71], a[172:175], a[92:95]\n"
"s_add_u32 s59, s59, 16384\n"
"v_mfma_f32_16x16x32_bf16 a[92:95], v[64:67], a[164:167], a[92:95]\n"
"s_waitcnt lgkmcnt(4)\n"
"buffer_load_dwordx4 v0, s[8:11], s59 offen sc1 lds\n"
"v_mfma_f32_16x16x32_bf16 a[96:99], v[80:83], a[160:163], a[96:99]\n"
"ds_read_b128 v[64:67], v1 offset:13312\n"
"v_mfma_f32_16x16x32_bf16 a[96:99], v[84:87], a[168:171], a[96:99]\n"
"ds_read_b128 v[68:71], v1 offset:14336\n"
"v_mfma_f32_16x16x32_bf16 a[96:99], v[88:91], a[176:179], a[96:99]\n"
"ds_read_b128 v[72:75], v1 offset:15360\n"
"v_mfma_f32_16x16x32_bf16 a[96:99], v[92:95], a[184:187], a[96:99]\n"
"ds_read_b128 v[76:79], v1 offset:16384\n"
"buffer_load_dwordx4 v0, s[8:11], s59 offen offset:1024 sc1 lds\n"
"v_mfma_f32_16x16x32_bf16 a[100:103], v[92:95], a[188:191], a[100:103]\n"
"v_mfma_f32_16x16x32_bf16 a[100:103], v[88:91], a[180:183], a[100:103]\n"
"v_mfma_f32_16x16x32_bf16 a[100:103], v[84:87], a[172:175], a[100:103]\n"
"v_mfma_f32_16x16x32_bf16 a[100:103], v[80:83], a[164:167], a[100:103]\n"
"s_waitcnt lgkmcnt(4)\n"
"buffer_load_dwordx4 v0, s[8:11], s59 offen offset:2048 sc1 lds\n"
"v_mfma_f32_16x16x32_bf16 a[104:107], v[48:51], a[160:163], a[104:107]\n"
"ds_read_b128 v[80:83], v1 offset:41984\n"
"v_mfma_f32_16x16x32_bf16 a[104:107], v[52:55], a[168:171], a[104:107]\n"
"ds_read_b128 v[84:87], v1 offset:43008\n"
"v_mfma_f32_16x16x32_bf16 a[104:107], v[56:59], a[176:179], a[104:107]\n"
"ds_read_b128 v[88:91], v1 offset:44032\n"
"v_mfma_f32_16x16x32_bf16 a[104:107], v[60:63], a[184:187], a[104:107]\n"
"ds_read_b128 v[92:95], v1 offset:45056\n"
"buffer_load_dwordx4 v0, s[8:11], s59 offen offset:3072 sc1 lds\n"
"v_mfma_f32_16x16x32_bf16 a[108:111], v[60:63], a[188:191], a[108:111]\n"
"v_mfma_f32_16x16x32_bf16 a[108:111], v[56:59], a[180:183], a[108:111]\n"
"v_mfma_f32_16x16x32_bf16 a[108:111], v[52:55], a[172:175], a[108:111]\n"
"s_add_u32 s56, s56, 4096\n"
"v_mfma_f32_16x16x32_bf16 a[108:111], v[48:51], a[164:167], a[108:111]\n"
"s_waitcnt lgkmcnt(4)\n"
"buffer_load_dwordx4 a[0:3], v0, s[8:11], s56 offen offset:0 sc1\n"
"v_mfma_f32_16x16x32_bf16 a[112:115], v[64:67], a[160:163], a[112:115]\n"
"ds_read_b128 v[48:51], v1 offset:46080\n"
"v_mfma_f32_16x16x32_bf16 a[112:115], v[68:71], a[168:171], a[112:115]\n"
"ds_read_b128 v[52:55], v1 offset:47104\n"
"v_mfma_f32_16x16x32_bf16 a[112:115], v[72:75], a[176:179], a[112:115]\n"
"ds_read_b128 v[56:59], v1 offset:48128\n"
"v_mfma_f32_16x16x32_bf16 a[112:115], v[76:79], a[184:187], a[112:115]\n"
"ds_read_b128 v[60:63], v1 offset:49152\n"
"buffer_load_dwordx4 a[8:11], v0, s[8:11], s56 offen offset:1024 sc1\n"
"v_mfma_f32_16x16x32_bf16 a[116:119], v[76:79], a[188:191], a[116:119]\n"
"v_mfma_f32_16x16x32_bf16 a[116:119], v[72:75], a[180:183], a[116:119]\n"
"v_mfma_f32_16x16x32_bf16 a[116:119], v[68:71], a[172:175], a[116:119]\n"
"v_mfma_f32_16x16x32_bf16 a[116:119], v[64:67], a[164:167], a[116:119]\n"
"s_waitcnt lgkmcnt(4)\n"
"buffer_load_dwordx4 a[16:19], v0, s[8:11], s56 offen offset:2048 sc1\n"
"v_mfma_f32_16x16x32_bf16 v[8:11], v[80:83], a[160:163], v[8:11]\n"
"ds_read_b128 v[64:67], v1 offset:50176\n"
"v_mfma_f32_16x16x32_bf16 v[8:11], v[84:87], a[168:171], v[8:11]\n"
"ds_read_b128 v[68:71], v1 offset:51200\n"
"v_mfma_f32_16x16x32_bf16 v[8:11], v[88:91], a[176:179], v[8:11]\n"
"ds_read_b128 v[72:75], v1 offset:52224\n"
"v_mfma_f32_16x16x32_bf16 v[8:11], v[92:95], a[184:187], v[8:11]\n"
"ds_read_b128 v[76:79], v1 offset:53248\n"
"buffer_load_dwordx4 a[24:27], v0, s[8:11], s56 offen offset:3072 sc1\n"
"v_mfma_f32_16x16x32_bf16 v[12:15], v[92:95], a[188:191], v[12:15]\n"
"v_mfma_f32_16x16x32_bf16 v[12:15], v[88:91], a[180:183], v[12:15]\n"
"v_mfma_f32_16x16x32_bf16 v[12:15], v[84:87], a[172:175], v[12:15]\n"
"s_add_u32 s57, s57, 4096\n"
"v_mfma_f32_16x16x32_bf16 v[12:15], v[80:83], a[164:167], v[12:15]\n"
"s_waitcnt lgkmcnt(4)\n"
"buffer_load_dwordx4 a[4:7], v0, s[8:11], s57 offen offset:0 sc1\n"
"v_mfma_f32_16x16x32_bf16 v[16:19], v[48:51], a[160:163], v[16:19]\n"
"ds_read_b128 v[80:83], v1 offset:54272\n"
"ds_read_b128 v[84:87], v1 offset:55296\n"
"v_mfma_f32_16x16x32_bf16 v[16:19], v[52:55], a[168:171], v[16:19]\n"
"ds_read_b128 v[88:91], v1 offset:56320\n"
"ds_read_b128 v[92:95], v1 offset:57344\n"
"v_mfma_f32_16x16x32_bf16 v[16:19], v[56:59], a[176:179], v[16:19]\n"
"v_mfma_f32_16x16x32_bf16 v[16:19], v[60:63], a[184:187], v[16:19]\n"
"buffer_load_dwordx4 a[12:15], v0, s[8:11], s57 offen offset:1024 sc1\n"
"v_mfma_f32_16x16x32_bf16 v[20:23], v[60:63], a[188:191], v[20:23]\n"
"v_mfma_f32_16x16x32_bf16 v[20:23], v[56:59], a[180:183], v[20:23]\n"
"v_mfma_f32_16x16x32_bf16 v[20:23], v[52:55], a[172:175], v[20:23]\n"
"s_waitcnt vmcnt(30)\n"
"v_mfma_f32_16x16x32_bf16 v[20:23], v[48:51], a[164:167], v[20:23]\n"
"s_waitcnt vmcnt(38) lgkmcnt(0)\n"
"buffer_load_dwordx4 a[20:23], v0, s[8:11], s57 offen offset:2048 sc1\n"
"v_mfma_f32_16x16x32_bf16 a[64:67], v[64:67], a[224:227], a[64:67]\n"
"s_barrier\n"
"ds_read_b128 v[48:51], v1 offset:25088\n"
"v_mfma_f32_16x16x32_bf16 a[64:67], v[68:71], a[232:235], a[64:67]\n"
"ds_read_b128 v[52:55], v1 offset:26112\n"
"v_mfma_f32_16x16x32_bf16 a[64:67], v[72:75], a[240:243], a[64:67]\n"
"ds_read_b128 v[56:59], v1 offset:27136\n"
"v_mfma_f32_16x16x32_bf16 a[64:67], v[76:79], a[248:251], a[64:67]\n"
"ds_read_b128 v[60:63], v1 offset:28160\n"
"buffer_load_dwordx4 a[28:31], v0, s[8:11], s57 offen offset:3072 sc1\n"
"v_mfma_f32_16x16x32_bf16 a[68:71], v[76:79], a[252:255], a[68:71]\n"
"v_mfma_f32_16x16x32_bf16 a[68:71], v[72:75], a[244:247], a[68:71]\n"
"s_add_u32 m0, s55, 141312\n"
"v_mfma_f32_16x16x32_bf16 a[68:71], v[68:71], a[236:239], a[68:71]\n"
"s_add_u32 s59, s59, 16384\n"
"v_mfma_f32_16x16x32_bf16 a[68:71], v[64:67], a[228:231], a[68:71]\n"
"s_waitcnt lgkmcnt(4)\n"
"buffer_load_dwordx4 v0, s[8:11], s59 offen sc1 lds\n"
"v_mfma_f32_16x16x32_bf16 a[72:75], v[80:83], a[224:227], a[72:75]\n"
"ds_read_b128 v[64:67], v1 offset:29184\n"
"v_bfe_u32 v30, v0, 4, 2\n"
"v_mfma_f32_16x16x32_bf16 a[72:75], v[84:87], a[232:235], a[72:75]\n"
"ds_read_b128 v[68:71], v1 offset:30208\n"
"v_lshlrev_b32 v30, 3, v30\n"
"v_mfma_f32_16x16x32_bf16 a[72:75], v[88:91], a[240:243], a[72:75]\n"
"ds_read_b128 v[72:75], v1 offset:31232\n"
"buffer_load_dwordx2 v[30:31], v30, s[8:11], s64 offen offset:0 sc1\n"
"v_mfma_f32_16x16x32_bf16 a[72:75], v[92:95], a[248:251], a[72:75]\n"
"ds_read_b128 v[76:79], v1 offset:32256\n"
"v_mfma_f32_16x16x32_bf16 a[76:79], v[92:95], a[252:255], a[76:79]\n"
"v_mfma_f32_16x16x32_bf16 a[76:79], v[88:91], a[244:247], a[76:79]\n"
"v_mfma_f32_16x16x32_bf16 a[76:79], v[84:87], a[236:239], a[76:79]\n"
"v_mfma_f32_16x16x32_bf16 a[76:79], v[80:83], a[228:231], a[76:79]\n"
"s_waitcnt lgkmcnt(4)\n"
"buffer_load_dwordx4 v0, s[8:11], s59 offen offset:1024 sc1 lds\n"
"v_mfma_f32_16x16x32_bf16 a[80:83], v[48:51], a[224:227], a[80:83]\n"
"ds_read_b128 v[80:83], v1 offset:33280\n"
"v_mfma_f32_16x16x32_bf16 a[80:83], v[52:55], a[232:235], a[80:83]\n"
"ds_read_b128 v[84:87], v1 offset:34304\n"
"v_mfma_f32_16x16x32_bf16 a[80:83], v[56:59], a[240:243], a[80:83]\n"
"ds_read_b128 v[88:91], v1 offset:35328\n"
"v_mfma_f32_16x16x32_bf16 a[80:83], v[60:63], a[248:251], a[80:83]\n"
"ds_read_b128 v[92:95], v1 offset:36352\n"
"buffer_load_dwordx4 v0, s[8:11], s59 offen offset:2048 sc1 lds\n"
"v_mfma_f32_16x16x32_bf16 a[84:87], v[60:63], a[252:255], a[84:87]\n"
"v_mfma_f32_16x16x32_bf16 a[84:87], v[56:59], a[244:247], a[84:87]\n"
"v_mfma_f32_16x16x32_bf16 a[84:87], v[52:55], a[236:239], a[84:87]\n"
"v_mfma_f32_16x16x32_bf16 a[84:87], v[48:51], a[228:231], a[84:87]\n"
"s_waitcnt lgkmcnt(4)\n"
"buffer_load_dwordx4 v0, s[8:11], s59 offen offset:3072 sc1 lds\n"
"v_mfma_f32_16x16x32_bf16 a[88:91], v[64:67], a[224:227], a[88:91]\n"
"ds_read_b128 v[48:51], v1 offset:37376\n"
"v_mfma_f32_16x16x32_bf16 a[88:91], v[68:71], a[232:235], a[88:91]\n"
"s_add_u32 m0, s55, 66560\n"
"ds_read_b128 v[52:55], v1 offset:38400\n"
"v_mfma_f32_16x16x32_bf16 a[88:91], v[72:75], a[240:243], a[88:91]\n"
"s_add_u32 s59, s59, 16384\n"
"ds_read_b128 v[56:59], v1 offset:39424\n"
"v_mfma_f32_16x16x32_bf16 a[88:91], v[76:79], a[248:251], a[88:91]\n"
"ds_read_b128 v[60:63], v1 offset:40448\n"
"buffer_load_dwordx4 v0, s[8:11], s59 offen sc1 lds\n"
"v_mfma_f32_16x16x32_bf16 a[92:95], v[76:79], a[252:255], a[92:95]\n"
"v_mfma_f32_16x16x32_bf16 a[92:95], v[72:75], a[244:247], a[92:95]\n"
"v_mfma_f32_16x16x32_bf16 a[92:95], v[68:71], a[236:239], a[92:95]\n"
"v_mfma_f32_16x16x32_bf16 a[92:95], v[64:67], a[228:231], a[92:95]\n"
"s_waitcnt lgkmcnt(4)\n"
"buffer_load_dwordx4 v0, s[8:11], s59 offen offset:1024 sc1 lds\n"
"v_mfma_f32_16x16x32_bf16 a[96:99], v[80:83], a[224:227], a[96:99]\n"
"ds_read_b128 v[64:67], v1 offset:58880\n"
"v_mfma_f32_16x16x32_bf16 a[96:99], v[84:87], a[232:235], a[96:99]\n"
"ds_read_b128 v[68:71], v1 offset:59904\n"
"v_mfma_f32_16x16x32_bf16 a[96:99], v[88:91], a[240:243], a[96:99]\n"
"ds_read_b128 v[72:75], v1 offset:60928\n"
"v_mfma_f32_16x16x32_bf16 a[96:99], v[92:95], a[248:251], a[96:99]\n"
"ds_read_b128 v[76:79], v1 offset:61952\n"
"buffer_load_dwordx4 v0, s[8:11], s59 offen offset:2048 sc1 lds\n"
"v_mfma_f32_16x16x32_bf16 a[100:103], v[92:95], a[252:255], a[100:103]\n"
"v_mfma_f32_16x16x32_bf16 a[100:103], v[88:91], a[244:247], a[100:103]\n"
"v_mfma_f32_16x16x32_bf16 a[100:103], v[84:87], a[236:239], a[100:103]\n"
"v_mfma_f32_16x16x32_bf16 a[100:103], v[80:83], a[228:231], a[100:103]\n"
"s_waitcnt lgkmcnt(4)\n"
"buffer_load_dwordx4 v0, s[8:11], s59 offen offset:3072 sc1 lds\n"
"v_mfma_f32_16x16x32_bf16 a[104:107], v[48:51], a[224:227], a[104:107]\n"
"ds_read_b128 v[80:83], v1 offset:62976\n"
"v_mfma_f32_16x16x32_bf16 a[104:107], v[52:55], a[232:235], a[104:107]\n"
"s_add_u32 m0, s55, 107520\n"
"ds_read_b128 v[84:87], v1 offset:64000\n"
"v_mfma_f32_16x16x32_bf16 a[104:107], v[56:59], a[240:243], a[104:107]\n"
"s_add_u32 s59, s59, 16384\n"
"ds_read_b128 v[88:91], v1 offset:65024\n"
"v_mfma_f32_16x16x32_bf16 a[104:107], v[60:63], a[248:251], a[104:107]\n"
"ds_read_b128 v[92:95], v2 offset:512\n"
"buffer_load_dwordx4 v0, s[8:11], s59 offen sc1 lds\n"
"v_mfma_f32_16x16x32_bf16 a[108:111], v[60:63], a[252:255], a[108:111]\n"
"v_mfma_f32_16x16x32_bf16 a[108:111], v[56:59], a[244:247], a[108:111]\n"
"v_mfma_f32_16x16x32_bf16 a[108:111], v[52:55], a[236:239], a[108:111]\n"
"v_mfma_f32_16x16x32_bf16 a[108:111], v[48:51], a[228:231], a[108:111]\n"
"s_waitcnt lgkmcnt(4)\n"
"buffer_load_dwordx4 v0, s[8:11], s59 offen offset:1024 sc1 lds\n"
"v_mfma_f32_16x16x32_bf16 a[112:115], v[64:67], a[224:227], a[112:115]\n"
"ds_read_b128 v[48:51], v2 offset:1536\n"
"v_mfma_f32_16x16x32_bf16 a[112:115], v[68:71], a[232:235], a[112:115]\n"
"ds_read_b128 v[52:55], v2 offset:2560\n"
"v_mfma_f32_16x16x32_bf16 a[112:115], v[72:75], a[240:243], a[112:115]\n"
"ds_read_b128 v[56:59], v2 offset:3584\n"
"v_mfma_f32_16x16x32_bf16 a[112:115], v[76:79], a[248:251], a[112:115]\n"
"ds_read_b128 v[60:63], v2 offset:4608\n"
"buffer_load_dwordx4 v0, s[8:11], s59 offen offset:2048 sc1 lds\n"
"v_mfma_f32_16x16x32_bf16 a[116:119], v[76:79], a[252:255], a[116:119]\n"
"v_mfma_f32_16x16x32_bf16 a[116:119], v[72:75], a[244:247], a[116:119]\n"
"v_mfma_f32_16x16x32_bf16 a[116:119], v[68:71], a[236:239], a[116:119]\n"
"v_mfma_f32_16x16x32_bf16 a[116:119], v[64:67], a[228:231], a[116:119]\n"
"s_waitcnt lgkmcnt(4)\n"
"buffer_load_dwordx4 v0, s[8:11], s59 offen offset:3072 sc1 lds\n"
"v_mfma_f32_16x16x32_bf16 v[8:11], v[80:83], a[224:227], v[8:11]\n"
"ds_read_b128 v[64:67], v2 offset:5632\n"
"ds_read_b128 v[68:71], v2 offset:6656\n"
"v_mfma_f32_16x16x32_bf16 v[8:11], v[84:87], a[232:235], v[8:11]\n"
"ds_read_b128 v[72:75], v2 offset:7680\n"
"ds_read_b128 v[76:79], v2 offset:8704\n"
"v_mfma_f32_16x16x32_bf16 v[8:11], v[88:91], a[240:243], v[8:11]\n"
"v_mfma_f32_16x16x32_bf16 v[8:11], v[92:95], a[248:251], v[8:11]\n"
"buffer_load_dwordx4 v[24:27], v254, s[40:43], s98 offen\n"
"v_mfma_f32_16x16x32_bf16 v[12:15], v[92:95], a[252:255], v[12:15]\n"
"v_mfma_f32_16x16x32_bf16 v[12:15], v[88:91], a[244:247], v[12:15]\n"
"v_mfma_f32_16x16x32_bf16 v[12:15], v[84:87], a[236:239], v[12:15]\n"
"v_mfma_f32_16x16x32_bf16 v[12:15], v[80:83], a[228:231], v[12:15]\n"
"s_waitcnt vmcnt(38) lgkmcnt(0)\n"
"buffer_load_dwordx4 v[248:251], v254, s[40:43], s99 offen\n"
"v_mfma_f32_16x16x32_bf16 v[16:19], v[48:51], a[224:227], v[16:19]\n"
"s_barrier\n"
"ds_read_b128 v[80:83], v0 offset:0\n"
"v_mfma_f32_16x16x32_bf16 v[16:19], v[52:55], a[232:235], v[16:19]\n"
"s_add_u32 m0, s55, 90624\n"
"ds_read_b128 v[84:87], v0 offset:1024\n"
"v_mfma_f32_16x16x32_bf16 v[16:19], v[56:59], a[240:243], v[16:19]\n"
"s_add_u32 s59, s59, 16384\n"
"ds_read_b128 v[88:91], v0 offset:2048\n"
"v_mfma_f32_16x16x32_bf16 v[16:19], v[60:63], a[248:251], v[16:19]\n"
"ds_read_b128 v[92:95], v0 offset:3072\n"
"buffer_load_dwordx4 v0, s[8:11], s59 offen sc1 lds\n"
"v_mfma_f32_16x16x32_bf16 v[20:23], v[60:63], a[252:255], v[20:23]\n"
"v_mfma_f32_16x16x32_bf16 v[20:23], v[56:59], a[244:247], v[20:23]\n"
"s_waitcnt vmcnt(14)\n"
"v_cmp_ne_u64 vcc, s[100:101], v[30:31]\n"
"v_mfma_f32_16x16x32_bf16 v[20:23], v[52:55], a[236:239], v[20:23]\n"
"v_mfma_f32_16x16x32_bf16 v[20:23], v[48:51], a[228:231], v[20:23]\n"
"s_waitcnt vmcnt(32) lgkmcnt(4)\n"
"buffer_load_dwordx4 v0, s[8:11], s59 offen offset:1024 sc1 lds\n"
"v_mfma_f32_16x16x32_bf16 a[64:67], v[64:67], a[192:195], a[64:67]\n"
"s_cbranch_vccnz L_os\n"
"L_osr:\n"
"ds_read_b128 v[48:51], v0 offset:4096\n"
"v_mfma_f32_16x16x32_bf16 a[64:67], v[68:71], a[200:203], a[64:67]\n"
"ds_read_b128 v[52:55], v0 offset:5120\n"
"v_mfma_f32_16x16x32_bf16 a[64:67], v[72:75], a[208:211], a[64:67]\n"
"ds_read_b128 v[56:59], v0 offset:6144\n"
"v_mfma_f32_16x16x32_bf16 a[64:67], v[76:79], a[216:219], a[64:67]\n"
"ds_read_b128 v[60:63], v0 offset:7168\n"
"v_and_b32 v158, 16, v0\n"
"v_mfma_f32_16x16x32_bf16 a[68:71], v[76:79], a[220:223], a[68:71]\n"
"buffer_load_dwordx4 a[128:131], v0, s[8:11], s58 offen offset:0 sc1\n"
"v_mov_b32 v159, 0x3f80\n"
"v_mfma_f32_16x16x32_bf16 a[68:71], v[72:75], a[212:215], a[68:71]\n"
"v_lshlrev_b32 v159, v158, v159\n"
"v_mfma_f32_16x16x32_bf16 a[68:71], v[68:71], a[204:207], a[68:71]\n"
"buffer_load_dwordx4 a[136:139], v0, s[8:11], s58 offen offset:1024 sc1\n"
"v_bfe_u32 v158, v0, 5, 3\n"
"v_mfma_f32_16x16x32_bf16 a[68:71], v[64:67], a[196:199], a[68:71]\n"
"s_waitcnt lgkmcnt(4)\n"
"v_lshrrev_b32 v157, 8, v0\n"
"v_mfma_f32_16x16x32_bf16 a[72:75], v[80:83], a[192:195], a[72:75]\n"
"ds_read_b128 v[64:67], v0 offset:8192\n"
"v_lshlrev_b32 v157, 1, v157\n"
"v_mfma_f32_16x16x32_bf16 a[72:75], v[84:87], a[200:203], a[72:75]\n"
"ds_read_b128 v[68:71], v0 offset:9216\n"
"v_cmp_eq_u32 vcc, v158, v157\n"
"v_mfma_f32_16x16x32_bf16 a[72:75], v[88:91], a[208:211], a[72:75]\n"
"ds_read_b128 v[72:75], v0 offset:10240\n"
"v_add_u32 v157, 1, v157\n"
"v_mfma_f32_16x16x32_bf16 a[72:75], v[92:95], a[216:219], a[72:75]\n"
"ds_read_b128 v[76:79], v0 offset:11264\n"
"v_mov_b32 v152, 0\n"
"v_mfma_f32_16x16x32_bf16 a[76:79], v[92:95], a[220:223], a[76:79]\n"
"buffer_load_dwordx4 a[144:147], v0, s[8:11], s58 offen offset:2048 sc1\n"
"v_cndmask_b32 v154, 0, v159, vcc\n"
"v_mfma_f32_16x16x32_bf16 a[76:79], v[88:91], a[212:215], a[76:79]\n"
"buffer_load_dwordx4 a[152:155], v0, s[8:11], s58 offen offset:3072 sc1\n"
"v_cmp_eq_u32 vcc, v158, v157\n"
"v_mfma_f32_16x16x32_bf16 a[76:79], v[84:87], a[204:207], a[76:79]\n"
"v_mov_b32 v153, 0\n"
"v_mfma_f32_16x16x32_bf16 a[76:79], v[80:83], a[196:199], a[76:79]\n"
"s_waitcnt lgkmcnt(4)\n"
"v_mov_b32 v156, 0\n"
"v_mfma_f32_16x16x32_bf16 a[80:83], v[48:51], a[192:195], a[80:83]\n"
"ds_read_b128 v[80:83], v0 offset:12288\n"
"v_cndmask_b32 v155, 0, v159, vcc\n"
"v_mfma_f32_16x16x32_bf16 a[80:83], v[52:55], a[200:203], a[80:83]\n"
"ds_read_b128 v[84:87], v0 offset:13312\n"
"v_mov_b32 v157, 0\n"
"v_mfma_f32_16x16x32_bf16 a[80:83], v[56:59], a[208:211], a[80:83]\n"
"ds_read_b128 v[88:91], v0 offset:14336\n"
"buffer_load_dwordx4 a[132:135], v0, s[8:11], s63 offen offset:0 sc1\n"
"v_mfma_f32_16x16x32_bf16 a[80:83], v[60:63], a[216:219], a[80:83]\n"
"ds_read_b128 v[92:95], v0 offset:15360\n"
"v_mfma_f32_16x16x32_bf16 a[84:87], v[60:63], a[220:223], a[84:87]\n"
"buffer_load_dwordx4 a[140:143], v0, s[8:11], s63 offen offset:1024 sc1\n"
"v_mfma_f32_16x16x32_bf16 a[84:87], v[56:59], a[212:215], a[84:87]\n"
"v_mfma_f32_16x16x32_bf16 a[84:87], v[52:55], a[204:207], a[84:87]\n"
"v_mfma_f32_16x16x32_bf16 a[84:87], v[48:51], a[196:199], a[84:87]\n"
"s_waitcnt lgkmcnt(4)\n"
"v_mfma_f32_16x16x32_bf16 a[88:91], v[64:67], a[192:195], a[88:91]\n"
"ds_read_b128 v[48:51], v0 offset:16384\n"
"buffer_load_dwordx4 a[148:151], v0, s[8:11], s63 offen offset:2048 sc1\n"
"v_mfma_f32_16x16x32_bf16 a[88:91], v[68:71], a[200:203], a[88:91]\n"
"ds_read_b128 v[52:55], v0 offset:17408\n"
"v_mfma_f32_16x16x32_bf16 a[88:91], v[72:75], a[208:211], a[88:91]\n"
"ds_read_b128 v[56:59], v0 offset:18432\n"
"v_mfma_f32_16x16x32_bf16 a[88:91], v[76:79], a[216:219], a[88:91]\n"
"ds_read_b128 v[60:63], v0 offset:19456\n"
"buffer_load_dwordx4 a[156:159], v0, s[8:11], s63 offen offset:3072 sc1\n"
"v_mfma_f32_16x16x32_bf16 a[92:95], v[76:79], a[220:223], a[92:95]\n"
"v_mfma_f32_16x16x32_bf16 a[92:95], v[72:75], a[212:215], a[92:95]\n"
"v_mfma_f32_16x16x32_bf16 a[92:95], v[68:71], a[204:207], a[92:95]\n"
"v_mfma_f32_16x16x32_bf16 a[92:95], v[64:67], a[196:199], a[92:95]\n"
"s_waitcnt lgkmcnt(4)\n"
"buffer_load_dwordx4 v0, s[8:11], s59 offen offset:2048 sc1 lds\n"
"v_mfma_f32_16x16x32_bf16 a[96:99], v[80:83], a[192:195], a[96:99]\n"
"ds_read_b128 v[64:67], v0 offset:20480\n"
"v_mfma_f32_16x16x32_bf16 a[96:99], v[84:87], a[200:203], a[96:99]\n"
"ds_read_b128 v[68:71], v0 offset:21504\n"
"v_mfma_f32_16x16x32_bf16 a[96:99], v[88:91], a[208:211], a[96:99]\n"
"ds_read_b128 v[72:75], v0 offset:22528\n"
"buffer_load_dwordx4 v0, s[8:11], s59 offen offset:3072 sc1 lds\n"
"v_mfma_f32_16x16x32_bf16 a[96:99], v[92:95], a[216:219], a[96:99]\n"
"ds_read_b128 v[76:79], v0 offset:23552\n"
"v_mfma_f32_16x16x32_bf16 a[100:103], v[92:95], a[220:223], a[100:103]\n"
"v_mfma_f32_16x16x32_bf16 a[100:103], v[88:91], a[212:215], a[100:103]\n"
"s_add_u32 s58, s58, 4096\n"
"v_mfma_f32_16x16x32_bf16 a[100:103], v[84:87], a[204:207], a[100:103]\n"
"buffer_load_dwordx4 a[32:35], v0, s[8:11], s58 offen offset:0 sc1\n"
"v_mfma_f32_16x16x32_bf16 a[100:103], v[80:83], a[196:199], a[100:103]\n"
"s_waitcnt lgkmcnt(4)\n"
"v_mfma_f32_16x16x32_bf16 a[104:107], v[48:51], a[192:195], a[104:107]\n"
"ds_read_b128 v[80:83], v0 offset:24576\n"
"v_mfma_f32_16x16x32_bf16 a[104:107], v[52:55], a[200:203], a[104:107]\n"
"ds_read_b128 v[84:87], v0 offset:25600\n"
"buffer_load_dwordx4 a[40:43], v0, s[8:11], s58 offen offset:1024 sc1\n"
"v_mfma_f32_16x16x32_bf16 a[104:107], v[56:59], a[208:211], a[104:107]\n"
"ds_read_b128 v[88:91], v0 offset:26624\n"
"v_mfma_f32_16x16x32_bf16 a[104:107], v[60:63], a[216:219], a[104:107]\n"
"ds_read_b128 v[92:95], v0 offset:27648\n"
"v_mfma_f32_16x16x32_bf16 a[108:111], v[60:63], a[220:223], a[108:111]\n"
"v_mfma_f32_16x16x32_bf16 a[108:111], v[56:59], a[212:215], a[108:111]\n"
"buffer_load_dwordx4 a[48:51], v0, s[8:11], s58 offen offset:2048 sc1\n"
"v_mfma_f32_16x16x32_bf16 a[108:111], v[52:55], a[204:207], a[108:111]\n"
"v_mfma_f32_16x16x32_bf16 a[108:111], v[48:51], a[196:199], a[108:111]\n"
"s_waitcnt lgkmcnt(4)\n"
"v_mfma_f32_16x16x32_bf16 a[112:115], v[64:67], a[192:195], a[112:115]\n"
"ds_read_b128 v[48:51], v0 offset:28672\n"
"ds_read_b128 v[52:55], v0 offset:29696\n"
"v_mfma_f32_16x16x32_bf16 a[112:115], v[68:71], a[200:203], a[112:115]\n"
"ds_read_b128 v[56:59], v0 offset:30720\n"
"ds_read_b128 v[60:63], v0 offset:31744\n"
"v_mfma_f32_16x16x32_bf16 a[112:115], v[72:75], a[208:211], a[112:115]\n"
"buffer_load_dwordx4 a[56:59], v0, s[8:11], s58 offen offset:3072 sc1\n"
"v_mfma_f32_16x16x32_bf16 a[112:115], v[76:79], a[216:219], a[112:115]\n"
"s_add_u32 s63, s63, 4096\n"
"v_mfma_f32_16x16x32_bf16 a[116:119], v[76:79], a[220:223], a[116:119]\n"
"buffer_load_dwordx4 a[36:39], v0, s[8:11], s63 offen offset:0 sc1\n"
"v_mfma_f32_16x16x32_bf16 a[116:119], v[72:75], a[212:215], a[116:119]\n"
"v_mfma_f32_16x16x32_bf16 a[116:119], v[68:71], a[204:207], a[116:119]\n"
"v_mfma_f32_16x16x32_bf16 a[116:119], v[64:67], a[196:199], a[116:119]\n"
"s_waitcnt vmcnt(40) lgkmcnt(0)\n"
"buffer_load_dwordx4 a[44:47], v0, s[8:11], s63 offen offset:1024 sc1\n"
"v_mfma_f32_16x16x32_bf16 v[8:11], v[80:83], a[192:195], v[8:11]\n"
"s_barrier\n"
"ds_read_b128 v[64:67], v0 offset:33792\n"
"v_mfma_f32_16x16x32_bf16 v[8:11], v[84:87], a[200:203], v[8:11]\n"
"ds_read_b128 v[68:71], v0 offset:34816\n"
"v_mfma_f32_16x16x32_bf16 v[8:11], v[88:91], a[208:211], v[8:11]\n"
"ds_read_b128 v[72:75], v0 offset:35840\n"
"v_mfma_f32_16x16x32_bf16 v[8:11], v[92:95], a[216:219], v[8:11]\n"
"ds_read_b128 v[76:79], v0 offset:36864\n"
"buffer_load_dwordx4 a[52:55], v0, s[8:11], s63 offen offset:2048 sc1\n"
"v_mfma_f32_16x16x32_bf16 v[12:15], v[92:95], a[220:223], v[12:15]\n"
"v_mfma_f32_16x16x32_bf16 v[12:15], v[88:91], a[212:215], v[12:15]\n"
"v_mfma_f32_16x16x32_bf16 v[12:15], v[84:87], a[204:207], v[12:15]\n"
"buffer_load_dwordx4 a[60:63], v0, s[8:11], s63 offen offset:3072 sc1\n"
"v_mfma_f32_16x16x32_bf16 v[12:15], v[80:83], a[196:199], v[12:15]\n"
"s_waitcnt lgkmcnt(4)\n"
"v_mfma_f32_16x16x32_bf16 v[16:19], v[48:51], a[192:195], v[16:19]\n"
"s_add_u32 m0, s55, 124416\n"
"ds_read_b128 v[80:83], v0 offset:37888\n"
"v_mfma_f32_16x16x32_bf16 v[16:19], v[52:55], a[200:203], v[16:19]\n"
"s_add_u32 s59, s59, 16384\n"
"ds_read_b128 v[84:87], v0 offset:38912\n"
"v_mfma_f32_16x16x32_bf16 v[16:19], v[56:59], a[208:211], v[16:19]\n"
"ds_read_b128 v[88:91], v0 offset:39936\n"
"buffer_load_dwordx4 v0, s[8:11], s59 offen sc1 lds\n"
"v_mfma_f32_16x16x32_bf16 v[16:19], v[60:63], a[216:219], v[16:19]\n"
"ds_read_b128 v[92:95], v0 offset:40960\n"
"v_mfma_f32_16x16x32_bf16 v[20:23], v[60:63], a[220:223], v[20:23]\n"
"v_mfma_f32_16x16x32_bf16 v[20:23], v[56:59], a[212:215], v[20:23]\n"
"buffer_load_dwordx4 v0, s[8:11], s59 offen offset:1024 sc1 lds\n"
"v_mfma_f32_16x16x32_bf16 v[20:23], v[52:55], a[204:207], v[20:23]\n"
"v_mfma_f32_16x16x32_bf16 v[20:23], v[48:51], a[196:199], v[20:23]\n"
"s_waitcnt vmcnt(37) lgkmcnt(4)\n"
"v_mfma_f32_16x16x32_bf16 a[64:67], v[64:67], a[0:3], a[64:67]\n"
"ds_read_b128 v[48:51], v0 offset:41984\n"
"v_mfma_f32_16x16x32_bf16 a[64:67], v[68:71], a[8:11], a[64:67]\n"
"ds_read_b128 v[52:55], v0 offset:43008\n"
"buffer_load_dwordx4 v0, s[8:11], s59 offen offset:2048 sc1 lds\n"
"v_mfma_f32_16x16x32_bf16 a[64:67], v[72:75], a[16:19], a[64:67]\n"
"ds_read_b128 v[56:59], v0 offset:44032\n"
"v_mfma_f32_16x16x32_bf16 a[64:67], v[76:79], a[24:27], a[64:67]\n"
"ds_read_b128 v[60:63], v0 offset:45056\n"
"v_mfma_f32_16x16x32_bf16 a[68:71], v[76:79], a[28:31], a[68:71]\n"
"buffer_load_dwordx4 v0, s[8:11], s59 offen offset:3072 sc1 lds\n"
"v_mfma_f32_16x16x32_bf16 a[68:71], v[72:75], a[20:23], a[68:71]\n"
"s_add_u32 m0, s55, 0\n"
"v_mfma_f32_16x16x32_bf16 a[68:71], v[68:71], a[12:15], a[68:71]\n"
"s_add_u32 s59, s59, 16384\n"
"v_accvgpr_read_b32 v96, a64\n"
"v_accvgpr_read_b32 v97, a65\n"
"v_mfma_f32_16x16x32_bf16 a[68:71], v[64:67], a[4:7], a[68:71]\n"
"s_waitcnt lgkmcnt(4)\n"
"v_accvgpr_read_b32 v98, a66\n"
"v_accvgpr_read_b32 v99, a67\n"
"v_mfma_f32_16x16x32_bf16 a[72:75], v[80:83], a[0:3], a[72:75]\n"
"ds_read_b128 v[64:67], v0 offset:46080\n"
"buffer_load_dwordx4 v0, s[8:11], s59 offen sc1 lds\n"
"v_mfma_f32_16x16x32_bf16 a[72:75], v[84:87], a[8:11], a[72:75]\n"
"ds_read_b128 v[68:71], v0 offset:47104\n"
"v_mfma_f32_16x16x32_bf16 a[72:75], v[88:91], a[16:19], a[72:75]\n"
"ds_read_b128 v[72:75], v0 offset:48128\n"
"v_accvgpr_read_b32 v100, a68\n"
"v_mfma_f32_16x16x32_bf16 a[72:75], v[92:95], a[24:27], a[72:75]\n"
"ds_read_b128 v[76:79], v0 offset:49152\n"
"v_accvgpr_read_b32 v101, a69\n"
"v_mfma_f32_16x16x32_bf16 a[76:79], v[92:95], a[28:31], a[76:79]\n"
"v_accvgpr_read_b32 v102, a70\n"
"v_accvgpr_read_b32 v103, a71\n"
"v_mfma_f32_16x16x32_bf16 a[76:79], v[88:91], a[20:23], a[76:79]\n"
"v_cvt_pk_bf16_f32 v112, v96, v97\n"
"v_cvt_pk_bf16_f32 v113, v98, v99\n"
"v_mfma_f32_16x16x32_bf16 a[76:79], v[84:87], a[12:15], a[76:79]\n"
"v_cvt_pk_bf16_f32 v114, v100, v101\n"
"v_cvt_pk_bf16_f32 v115, v102, v103\n"
"v_mfma_f32_16x16x32_bf16 a[76:79], v[80:83], a[4:7], a[76:79]\n"
"s_waitcnt lgkmcnt(4)\n"
"buffer_store_dwordx4 v[112:115], v0, s[8:11], s89 offen offset:0 sc1\n"
"v_accvgpr_read_b32 v104, a72\n"
"v_mfma_f32_16x16x32_bf16 a[80:83], v[48:51], a[0:3], a[80:83]\n"
"ds_read_b128 v[80:83], v0 offset:50176\n"
"v_accvgpr_read_b32 v105, a73\n"
"v_mfma_f32_16x16x32_bf16 a[80:83], v[52:55], a[8:11], a[80:83]\n"
"ds_read_b128 v[84:87], v0 offset:51200\n"
"v_accvgpr_read_b32 v106, a74\n"
"v_mfma_f32_16x16x32_bf16 a[80:83], v[56:59], a[16:19], a[80:83]\n"
"ds_read_b128 v[88:91], v0 offset:52224\n"
"v_accvgpr_read_b32 v107, a75\n"
"v_mfma_f32_16x16x32_bf16 a[80:83], v[60:63], a[24:27], a[80:83]\n"
"ds_read_b128 v[92:95], v0 offset:53248\n"
"v_accvgpr_read_b32 v108, a76\n"
"v_mfma_f32_16x16x32_bf16 a[84:87], v[60:63], a[28:31], a[84:87]\n"
"v_accvgpr_read_b32 v109, a77\n"
"v_accvgpr_read_b32 v110, a78\n"
"v_mfma_f32_16x16x32_bf16 a[84:87], v[56:59], a[20:23], a[84:87]\n"
"v_accvgpr_read_b32 v111, a79\n"
"v_cvt_pk_bf16_f32 v116, v104, v105\n"
"v_mfma_f32_16x16x32_bf16 a[84:87], v[52:55], a[12:15], a[84:87]\n"
"v_cvt_pk_bf16_f32 v117, v106, v107\n"
"v_cvt_pk_bf16_f32 v118, v108, v109\n"
"v_mfma_f32_16x16x32_bf16 a[84:87], v[48:51], a[4:7], a[84:87]\n"
"s_waitcnt lgkmcnt(4)\n"
"v_cvt_pk_bf16_f32 v119, v110, v111\n"
"v_accvgpr_read_b32 v96, a80\n"
"v_mfma_f32_16x16x32_bf16 a[88:91], v[64:67], a[0:3], a[88:91]\n"
"ds_read_b128 v[48:51], v0 offset:54272\n"
"v_accvgpr_read_b32 v97, a81\n"
"v_mfma_f32_16x16x32_bf16 a[88:91], v[68:71], a[8:11], a[88:91]\n"
"ds_read_b128 v[52:55], v0 offset:55296\n"
"v_accvgpr_read_b32 v98, a82\n"
"v_mfma_f32_16x16x32_bf16 a[88:91], v[72:75], a[16:19], a[88:91]\n"
"ds_read_b128 v[56:59], v0 offset:56320\n"
"v_accvgpr_read_b32 v99, a83\n"
"v_mfma_f32_16x16x32_bf16 a[88:91], v[76:79], a[24:27], a[88:91]\n"
"ds_read_b128 v[60:63], v0 offset:57344\n"
"buffer_store_dwordx4 v[116:119], v0, s[8:11], s86 offen offset:0 sc1\n"
"v_mfma_f32_16x16x32_bf16 a[92:95], v[76:79], a[28:31], a[92:95]\n"
"v_accvgpr_read_b32 v100, a84\n"
"v_accvgpr_read_b32 v101, a85\n"
"v_mfma_f32_16x16x32_bf16 a[92:95], v[72:75], a[20:23], a[92:95]\n"
"v_accvgpr_read_b32 v102, a86\n"
"v_accvgpr_read_b32 v103, a87\n"
"v_mfma_f32_16x16x32_bf16 a[92:95], v[68:71], a[12:15], a[92:95]\n"
"v_cvt_pk_bf16_f32 v120, v96, v97\n"
"v_cvt_pk_bf16_f32 v121, v98, v99\n"
"v_mfma_f32_16x16x32_bf16 a[92:95], v[64:67], a[4:7], a[92:95]\n"
"s_waitcnt lgkmcnt(4)\n"
"v_cvt_pk_bf16_f32 v122, v100, v101\n"
"v_cvt_pk_bf16_f32 v123, v102, v103\n"
"v_mfma_f32_16x16x32_bf16 a[96:99], v[80:83], a[0:3], a[96:99]\n"
"ds_read_b128 v[64:67], v0 offset:58368\n"
"buffer_load_dwordx4 v0, s[8:11], s59 offen offset:1024 sc1 lds\n"
"v_mfma_f32_16x16x32_bf16 a[96:99], v[84:87], a[8:11], a[96:99]\n"
"ds_read_b128 v[68:71], v0 offset:59392\n"
"buffer_load_dwordx4 v0, s[8:11], s59 offen offset:2048 sc1 lds\n"
"v_mfma_f32_16x16x32_bf16 a[96:99], v[88:91], a[16:19], a[96:99]\n"
"ds_read_b128 v[72:75], v0 offset:60416\n"
"buffer_load_dwordx4 v0, s[8:11], s59 offen offset:3072 sc1 lds\n"
"v_mfma_f32_16x16x32_bf16 a[96:99], v[92:95], a[24:27], a[96:99]\n"
"ds_read_b128 v[76:79], v0 offset:61440\n"
"v_accvgpr_read_b32 v104, a88\n"
"v_mfma_f32_16x16x32_bf16 a[100:103], v[92:95], a[28:31], a[100:103]\n"
"v_accvgpr_read_b32 v105, a89\n"
"v_accvgpr_read_b32 v106, a90\n"
"v_mfma_f32_16x16x32_bf16 a[100:103], v[88:91], a[20:23], a[100:103]\n"
"v_accvgpr_read_b32 v107, a91\n"
"v_accvgpr_read_b32 v108, a92\n"
"v_mfma_f32_16x16x32_bf16 a[100:103], v[84:87], a[12:15], a[100:103]\n"
"v_accvgpr_read_b32 v109, a93\n"
"v_accvgpr_read_b32 v110, a94\n"
"v_mfma_f32_16x16x32_bf16 a[100:103], v[80:83], a[4:7], a[100:103]\n"
"s_waitcnt lgkmcnt(4)\n"
"buffer_store_dwordx4 v[120:123], v0, s[8:11], s86 offen offset:1024 sc1\n"
"v_accvgpr_read_b32 v111, a95\n"
"v_mfma_f32_16x16x32_bf16 a[104:107], v[48:51], a[0:3], a[104:107]\n"
"ds_read_b128 v[80:83], v0 offset:62464\n"
"ds_read_b128 v[84:87], v0 offset:63488\n"
"v_mfma_f32_16x16x32_bf16 a[104:107], v[52:55], a[8:11], a[104:107]\n"
"ds_read_b128 v[88:91], v0 offset:64512\n"
"ds_read_b128 v[92:95], v1 offset:0\n"
"v_mfma_f32_16x16x32_bf16 a[104:107], v[56:59], a[16:19], a[104:107]\n"
"v_cvt_pk_bf16_f32 v124, v104, v105\n"
"v_cvt_pk_bf16_f32 v125, v106, v107\n"
"v_mfma_f32_16x16x32_bf16 a[104:107], v[60:63], a[24:27], a[104:107]\n"
"s_add_u32 s58, s58, 4096\n"
"v_cvt_pk_bf16_f32 v126, v108, v109\n"
"v_cvt_pk_bf16_f32 v127, v110, v111\n"
"v_mfma_f32_16x16x32_bf16 a[108:111], v[60:63], a[28:31], a[108:111]\n"
"buffer_load_dwordx4 a[160:163], v0, s[8:11], s58 offen offset:0 sc1\n"
"v_mfma_f32_16x16x32_bf16 a[108:111], v[56:59], a[20:23], a[108:111]\n"
"v_mfma_f32_16x16x32_bf16 a[108:111], v[52:55], a[12:15], a[108:111]\n"
"v_mfma_f32_16x16x32_bf16 a[108:111], v[48:51], a[4:7], a[108:111]\n"
"s_waitcnt vmcnt(38) lgkmcnt(0)\n"
"v_accvgpr_read_b32 v96, a96\n"
"v_accvgpr_read_b32 v97, a97\n"
"v_mfma_f32_16x16x32_bf16 a[112:115], v[64:67], a[0:3], a[112:115]\n"
"s_barrier\n"
"ds_read_b128 v[48:51], v2 offset:10240\n"
"v_accvgpr_read_b32 v98, a98\n"
"v_mfma_f32_16x16x32_bf16 a[112:115], v[68:71], a[8:11], a[112:115]\n"
"ds_read_b128 v[52:55], v2 offset:11264\n"
"v_accvgpr_read_b32 v99, a99\n"
"v_mfma_f32_16x16x32_bf16 a[112:115], v[72:75], a[16:19], a[112:115]\n"
"ds_read_b128 v[56:59], v2 offset:12288\n"
"v_accvgpr_read_b32 v100, a100\n"
"v_mfma_f32_16x16x32_bf16 a[112:115], v[76:79], a[24:27], a[112:115]\n"
"ds_read_b128 v[60:63], v2 offset:13312\n"
"buffer_store_dwordx4 v[124:127], v0, s[8:11], s87 offen offset:0 sc1\n"
"v_mfma_f32_16x16x32_bf16 a[116:119], v[76:79], a[28:31], a[116:119]\n"
"v_accvgpr_read_b32 v101, a101\n"
"v_accvgpr_read_b32 v102, a102\n"
"v_mfma_f32_16x16x32_bf16 a[116:119], v[72:75], a[20:23], a[116:119]\n"
"v_accvgpr_read_b32 v103, a103\n"
"v_cvt_pk_bf16_f32 v112, v96, v97\n"
"v_mfma_f32_16x16x32_bf16 a[116:119], v[68:71], a[12:15], a[116:119]\n"
"v_cvt_pk_bf16_f32 v113, v98, v99\n"
"v_cvt_pk_bf16_f32 v114, v100, v101\n"
"v_mfma_f32_16x16x32_bf16 a[116:119], v[64:67], a[4:7], a[116:119]\n"
"s_waitcnt lgkmcnt(4)\n"
"buffer_load_dwordx4 a[168:171], v0, s[8:11], s58 offen offset:1024 sc1\n"
"v_cvt_pk_bf16_f32 v115, v102, v103\n"
"v_mfma_f32_16x16x32_bf16 v[8:11], v[80:83], a[0:3], v[8:11]\n"
"ds_read_b128 v[64:67], v2 offset:14336\n"
"v_mfma_f32_16x16x32_bf16 v[8:11], v[84:87], a[8:11], v[8:11]\n"
"ds_read_b128 v[68:71], v2 offset:15360\n"
"v_mfma_f32_16x16x32_bf16 v[8:11], v[88:91], a[16:19], v[8:11]\n"
"ds_read_b128 v[72:75], v2 offset:16384\n"
"buffer_load_dwordx4 a[176:179], v0, s[8:11], s58 offen offset:2048 sc1\n"
"v_mfma_f32_16x16x32_bf16 v[8:11], v[92:95], a[24:27], v[8:11]\n"
"ds_read_b128 v[76:79], v2 offset:17408\n"
"v_accvgpr_read_b32 v104, a104\n"
"v_mfma_f32_16x16x32_bf16 v[12:15], v[92:95], a[28:31], v[12:15]\n"
"v_accvgpr_read_b32 v105, a105\n"
"v_accvgpr_read_b32 v106, a106\n"
"v_mfma_f32_16x16x32_bf16 v[12:15], v[88:91], a[20:23], v[12:15]\n"
"v_accvgpr_read_b32 v107, a107\n"
"v_accvgpr_read_b32 v108, a108\n"
"v_mfma_f32_16x16x32_bf16 v[12:15], v[84:87], a[12:15], v[12:15]\n"
"v_accvgpr_read_b32 v109, a109\n"
"v_accvgpr_read_b32 v110, a110\n"
"v_mfma_f32_16x16x32_bf16 v[12:15], v[80:83], a[4:7], v[12:15]\n"
"s_waitcnt lgkmcnt(4)\n"
"buffer_store_dwordx4 v[112:115], v0, s[8:11], s87 offen offset:1024 sc1\n"
"v_accvgpr_read_b32 v111, a111\n"
"v_mfma_f32_16x16x32_bf16 v[16:19], v[48:51], a[0:3], v[16:19]\n"
"ds_read_b128 v[80:83], v2 offset:18432\n"
"v_cvt_pk_bf16_f32 v116, v104, v105\n"
"v_mfma_f32_16x16x32_bf16 v[16:19], v[52:55], a[8:11], v[16:19]\n"
"ds_read_b128 v[84:87], v2 offset:19456\n"
"v_cvt_pk_bf16_f32 v117, v106, v107\n"
"v_mfma_f32_16x16x32_bf16 v[16:19], v[56:59], a[16:19], v[16:19]\n"
"ds_read_b128 v[88:91], v2 offset:20480\n"
"v_cvt_pk_bf16_f32 v118, v108, v109\n"
"v_mfma_f32_16x16x32_bf16 v[16:19], v[60:63], a[24:27], v[16:19]\n"
"ds_read_b128 v[92:95], v2 offset:21504\n"
"v_cvt_pk_bf16_f32 v119, v110, v111\n"
"v_mfma_f32_16x16x32_bf16 v[20:23], v[60:63], a[28:31], v[20:23]\n"
"buffer_load_dwordx4 a[184:187], v0, s[8:11], s58 offen offset:3072 sc1\n"
"v_mfma_f32_16x16x32_bf16 v[20:23], v[56:59], a[20:23], v[20:23]\n"
"v_mfma_f32_16x16x32_bf16 v[20:23], v[52:55], a[12:15], v[20:23]\n"
"v_mfma_f32_16x16x32_bf16 v[20:23], v[48:51], a[4:7], v[20:23]\n"
"s_waitcnt vmcnt(27) lgkmcnt(4)\n"
"v_accvgpr_read_b32 v96, a112\n"
"v_accvgpr_read_b32 v97, a113\n"
"v_mfma_f32_16x16x32_bf16 v[160:163], v[64:67], a[128:131], 0\n"
"ds_read_b128 v[48:51], v2 offset:22528\n"
"v_accvgpr_read_b32 v98, a114\n"
"v_mfma_f32_16x16x32_bf16 v[160:163], v[68:71], a[136:139], v[160:163]\n"
"ds_read_b128 v[52:55], v2 offset:23552\n"
"v_accvgpr_read_b32 v99, a115\n"
"v_mfma_f32_16x16x32_bf16 v[160:163], v[72:75], a[144:147], v[160:163]\n"
"ds_read_b128 v[56:59], v2 offset:24576\n"
"v_accvgpr_read_b32 v100, a116\n"
"v_mfma_f32_16x16x32_bf16 v[160:163], v[76:79], a[152:155], v[160:163]\n"
"ds_read_b128 v[60:63], v2 offset:25600\n"
"buffer_store_dwordx4 v[116:119], v0, s[8:11], s88 offen offset:0 sc1\n"
"v_mfma_f32_16x16x32_bf16 v[164:167], v[76:79], a[156:159], 0\n"
"v_accvgpr_read_b32 v101, a117\n"
"v_accvgpr_read_b32 v102, a118\n"
"v_mfma_f32_16x16x32_bf16 v[164:167], v[72:75], a[148:151], v[164:167]\n"
"v_accvgpr_read_b32 v103, a119\n"
"v_cvt_pk_bf16_f32 v120, v96, v97\n"
"v_mfma_f32_16x16x32_bf16 v[164:167], v[68:71], a[140:143], v[164:167]\n"
"s_add_u32 s63, s63, 4096\n"
"v_cvt_pk_bf16_f32 v121, v98, v99\n"
"v_cvt_pk_bf16_f32 v122, v100, v101\n"
"v_mfma_f32_16x16x32_bf16 v[164:167], v[64:67], a[132:135], v[164:167]\n"
"s_waitcnt lgkmcnt(4)\n"
"buffer_load_dwordx4 a[164:167], v0, s[8:11], s63 offen offset:0 sc1\n"
"v_cvt_pk_bf16_f32 v123, v102, v103\n"
"v_mfma_f32_16x16x32_bf16 v[168:171], v[80:83], a[128:131], 0\n"
"ds_read_b128 v[64:67], v1 offset:1024\n"
"v_mfma_f32_16x16x32_bf16 v[168:171], v[84:87], a[136:139], v[168:171]\n"
"ds_read_b128 v[68:71], v1 offset:2048\n"
"v_mfma_f32_16x16x32_bf16 v[168:171], v[88:91], a[144:147], v[168:171]\n"
"ds_read_b128 v[72:75], v1 offset:3072\n"
"buffer_load_dwordx4 a[172:175], v0, s[8:11], s63 offen offset:1024 sc1\n"
"v_mfma_f32_16x16x32_bf16 v[168:171], v[92:95], a[152:155], v[168:171]\n"
"ds_read_b128 v[76:79], v1 offset:4096\n"
"v_mfma_f32_16x16x32_bf16 v[172:175], v[92:95], a[156:159], 0\n"
"v_mfma_f32_16x16x32_bf16 v[172:175], v[88:91], a[148:151], v[172:175]\n"
"v_mfma_f32_16x16x32_bf16 v[172:175], v[84:87], a[140:143], v[172:175]\n"
"buffer_load_dwordx4 a[180:183], v0, s[8:11], s63 offen offset:2048 sc1\n"
"v_mfma_f32_16x16x32_bf16 v[172:175], v[80:83], a[132:135], v[172:175]\n"
"s_waitcnt lgkmcnt(4)\n"
"buffer_store_dwordx4 v[120:123], v0, s[8:11], s88 offen offset:1024 sc1\n"
"v_mfma_f32_16x16x32_bf16 v[176:179], v[48:51], a[128:131], 0\n"
"s_add_u32 s86, s86, 6144\n"
"ds_read_b128 v[80:83], v1 offset:5120\n"
"v_mfma_f32_16x16x32_bf16 v[176:179], v[52:55], a[136:139], v[176:179]\n"
"s_add_u32 s87, s87, 6144\n"
"ds_read_b128 v[84:87], v1 offset:6144\n"
"v_mfma_f32_16x16x32_bf16 v[176:179], v[56:59], a[144:147], v[176:179]\n"
"s_add_u32 s88, s88, 6144\n"
"ds_read_b128 v[88:91], v1 offset:7168\n"
"v_mfma_f32_16x16x32_bf16 v[176:179], v[60:63], a[152:155], v[176:179]\n"
"s_add_u32 s89, s89, 4096\n"
"ds_read_b128 v[92:95], v1 offset:8192\n"
"v_mfma_f32_16x16x32_bf16 v[180:183], v[60:63], a[156:159], 0\n"
"v_bfe_u32 v30, v0, 4, 2\n"
"v_mfma_f32_16x16x32_bf16 v[180:183], v[56:59], a[148:151], v[180:183]\n"
"v_lshlrev_b32 v30, 3, v30\n"
"v_mfma_f32_16x16x32_bf16 v[180:183], v[52:55], a[140:143], v[180:183]\n"
"buffer_load_dwordx2 v[30:31], v30, s[8:11], s64 offen offset:32 sc1\n"
"v_mfma_f32_16x16x32_bf16 v[180:183], v[48:51], a[132:135], v[180:183]\n"
"s_waitcnt lgkmcnt(4)\n"
"v_mfma_f32_16x16x32_bf16 v[184:187], v[64:67], a[128:131], 0\n"
"ds_read_b128 v[48:51], v1 offset:9216\n"
"v_mfma_f32_16x16x32_bf16 v[184:187], v[68:71], a[136:139], v[184:187]\n"
"ds_read_b128 v[52:55], v1 offset:10240\n"
"v_mfma_f32_16x16x32_bf16 v[184:187], v[72:75], a[144:147], v[184:187]\n"
"ds_read_b128 v[56:59], v1 offset:11264\n"
"v_mfma_f32_16x16x32_bf16 v[184:187], v[76:79], a[152:155], v[184:187]\n"
"ds_read_b128 v[60:63], v1 offset:12288\n"
"v_mfma_f32_16x16x32_bf16 v[188:191], v[76:79], a[156:159], 0\n"
"buffer_load_dwordx4 a[188:191], v0, s[8:11], s63 offen offset:3072 sc1\n"
"v_mfma_f32_16x16x32_bf16 v[188:191], v[72:75], a[148:151], v[188:191]\n"
"s_add_u32 m0, s55, 16384\n"
"v_mfma_f32_16x16x32_bf16 v[188:191], v[68:71], a[140:143], v[188:191]\n"
"s_add_u32 s59, s59, 16384\n"
"v_mfma_f32_16x16x32_bf16 v[188:191], v[64:67], a[132:135], v[188:191]\n"
"s_waitcnt lgkmcnt(4)\n"
"buffer_load_dwordx4 v0, s[8:11], s59 offen sc1 lds\n"
"v_mfma_f32_16x16x32_bf16 v[192:195], v[80:83], a[128:131], 0\n"
"ds_read_b128 v[64:67], v1 offset:13312\n"
"ds_read_b128 v[68:71], v1 offset:14336\n"
"v_mfma_f32_16x16x32_bf16 v[192:195], v[84:87], a[136:139], v[192:195]\n"
"ds_read_b128 v[72:75], v1 offset:15360\n"
"ds_read_b128 v[76:79], v1 offset:16384\n"
"v_mfma_f32_16x16x32_bf16 v[192:195], v[88:91], a[144:147], v[192:195]\n"
"v_mfma_f32_16x16x32_bf16 v[192:195], v[92:95], a[152:155], v[192:195]\n"
"buffer_load_dwordx4 v0, s[8:11], s59 offen offset:1024 sc1 lds\n"
"v_mfma_f32_16x16x32_bf16 v[196:199], v[92:95], a[156:159], 0\n"
"v_mfma_f32_16x16x32_bf16 v[196:199], v[88:91], a[148:151], v[196:199]\n"
"v_mfma_f32_16x16x32_bf16 v[196:199], v[84:87], a[140:143], v[196:199]\n"
"buffer_load_dwordx4 v0, s[8:11], s59 offen offset:2048 sc1 lds\n"
"v_mfma_f32_16x16x32_bf16 v[196:199], v[80:83], a[132:135], v[196:199]\n"
"s_waitcnt vmcnt(35) lgkmcnt(0)\n"
"v_mfma_f32_16x16x32_bf16 v[200:203], v[48:51], a[128:131], 0\n"
"s_barrier\n"
"ds_read_b128 v[80:83], v1 offset:41984\n"
"v_mfma_f32_16x16x32_bf16 v[200:203], v[52:55], a[136:139], v[200:203]\n"
"ds_read_b128 v[84:87], v1 offset:43008\n"
"v_mfma_f32_16x16x32_bf16 v[200:203], v[56:59], a[144:147], v[200:203]\n"
"ds_read_b128 v[88:91], v1 offset:44032\n"
"buffer_load_dwordx4 v0, s[8:11], s59 offen offset:3072 sc1 lds\n"
"v_mfma_f32_16x16x32_bf16 v[200:203], v[60:63], a[152:155], v[200:203]\n"
"s_add_u32 m0, s55, 33792\n"
"ds_read_b128 v[92:95], v1 offset:45056\n"
"v_mfma_f32_16x16x32_bf16 v[204:207], v[60:63], a[156:159], 0\n"
"s_add_u32 s59, s59, 16384\n"
"v_mfma_f32_16x16x32_bf16 v[204:207], v[56:59], a[148:151], v[204:207]\n"
"buffer_load_dwordx4 v0, s[8:11], s59 offen sc1 lds\n"
"v_mfma_f32_16x16x32_bf16 v[204:207], v[52:55], a[140:143], v[204:207]\n"
"v_mfma_f32_16x16x32_bf16 v[204:207], v[48:51], a[132:135], v[204:207]\n"
"s_waitcnt lgkmcnt(4)\n"
"v_mfma_f32_16x16x32_bf16 v[208:211], v[64:67], a[128:131], 0\n"
"ds_read_b128 v[48:51], v1 offset:46080\n"
"v_mfma_f32_16x16x32_bf16 v[208:211], v[68:71], a[136:139], v[208:211]\n"
"ds_read_b128 v[52:55], v1 offset:47104\n"
"buffer_load_dwordx4 v0, s[8:11], s59 offen offset:1024 sc1 lds\n"
"v_mfma_f32_16x16x32_bf16 v[208:211], v[72:75], a[144:147], v[208:211]\n"
"ds_read_b128 v[56:59], v1 offset:48128\n"
"v_mfma_f32_16x16x32_bf16 v[208:211], v[76:79], a[152:155], v[208:211]\n"
"ds_read_b128 v[60:63], v1 offset:49152\n"
"v_mfma_f32_16x16x32_bf16 v[212:215], v[76:79], a[156:159], 0\n"
"buffer_load_dwordx4 v0, s[8:11], s59 offen offset:2048 sc1 lds\n"
"v_mfma_f32_16x16x32_bf16 v[212:215], v[72:75], a[148:151], v[212:215]\n"
"v_mfma_f32_16x16x32_bf16 v[212:215], v[68:71], a[140:143], v[212:215]\n"
"v_mfma_f32_16x16x32_bf16 v[212:215], v[64:67], a[132:135], v[212:215]\n"
"s_waitcnt lgkmcnt(4)\n"
"v_mfma_f32_16x16x32_bf16 v[216:219], v[80:83], a[128:131], 0\n"
"ds_read_b128 v[64:67], v1 offset:50176\n"
"buffer_load_dwordx4 v0, s[8:11], s59 offen offset:3072 sc1 lds\n"
"v_mfma_f32_16x16x32_bf16 v[216:219], v[84:87], a[136:139], v[216:219]\n"
"ds_read_b128 v[68:71], v1 offset:51200\n"
"v_mfma_f32_16x16x32_bf16 v[216:219], v[88:91], a[144:147], v[216:219]\n"
"s_add_u32 s58, s58, 4096\n"
"ds_read_b128 v[72:75], v1 offset:52224\n"
"v_mfma_f32_16x16x32_bf16 v[216:219], v[92:95], a[152:155], v[216:219]\n"
"ds_read_b128 v[76:79], v1 offset:53248\n"
"buffer_load_dwordx4 a[224:227], v0, s[8:11], s58 offen offset:0 sc1\n"
"v_mfma_f32_16x16x32_bf16 v[220:223], v[92:95], a[156:159], 0\n"
"v_mfma_f32_16x16x32_bf16 v[220:223], v[88:91], a[148:151], v[220:223]\n"
"v_mfma_f32_16x16x32_bf16 v[220:223], v[84:87], a[140:143], v[220:223]\n"
"v_mfma_f32_16x16x32_bf16 v[220:223], v[80:83], a[132:135], v[220:223]\n"
"s_waitcnt lgkmcnt(4)\n"
"buffer_load_dwordx4 a[232:235], v0, s[8:11], s58 offen offset:1024 sc1\n"
"v_mfma_f32_16x16x32_bf16 v[224:227], v[48:51], a[128:131], 0\n"
"ds_read_b128 v[80:83], v1 offset:54272\n"
"v_mfma_f32_16x16x32_bf16 v[224:227], v[52:55], a[136:139], v[224:227]\n"
"ds_read_b128 v[84:87], v1 offset:55296\n"
"v_mfma_f32_16x16x32_bf16 v[224:227], v[56:59], a[144:147], v[224:227]\n"
"ds_read_b128 v[88:91], v1 offset:56320\n"
"buffer_load_dwordx4 a[240:243], v0, s[8:11], s58 offen offset:2048 sc1\n"
"v_mfma_f32_16x16x32_bf16 v[224:227], v[60:63], a[152:155], v[224:227]\n"
"ds_read_b128 v[92:95], v1 offset:57344\n"
"v_mfma_f32_16x16x32_bf16 v[228:231], v[60:63], a[156:159], 0\n"
"v_mfma_f32_16x16x32_bf16 v[228:231], v[56:59], a[148:151], v[228:231]\n"
"v_mfma_f32_16x16x32_bf16 v[228:231], v[52:55], a[140:143], v[228:231]\n"
"buffer_load_dwordx4 a[248:251], v0, s[8:11], s58 offen offset:3072 sc1\n"
"v_mfma_f32_16x16x32_bf16 v[228:231], v[48:51], a[132:135], v[228:231]\n"
"s_waitcnt vmcnt(36) lgkmcnt(4)\n"
"v_mfma_f32_16x16x32_bf16 v[160:163], v[64:67], a[32:35], v[160:163]\n"
"s_add_u32 s63, s63, 4096\n"
"ds_read_b128 v[48:51], v1 offset:25088\n"
"v_mfma_f32_16x16x32_bf16 v[160:163], v[68:71], a[40:43], v[160:163]\n"
"ds_read_b128 v[52:55], v1 offset:26112\n"
"buffer_load_dwordx4 a[228:231], v0, s[8:11], s63 offen offset:0 sc1\n"
"v_mfma_f32_16x16x32_bf16 v[160:163], v[72:75], a[48:51], v[160:163]\n"
"ds_read_b128 v[56:59], v1 offset:27136\n"
"v_mfma_f32_16x16x32_bf16 v[160:163], v[76:79], a[56:59], v[160:163]\n"
"ds_read_b128 v[60:63], v1 offset:28160\n"
"v_mfma_f32_16x16x32_bf16 v[164:167], v[76:79], a[60:63], v[164:167]\n"
"v_mfma_f32_16x16x32_bf16 v[164:167], v[72:75], a[52:55], v[164:167]\n"
"buffer_load_dwordx4 a[236:239], v0, s[8:11], s63 offen offset:1024 sc1\n"
"v_mfma_f32_16x16x32_bf16 v[164:167], v[68:71], a[44:47], v[164:167]\n"
"v_mfma_f32_16x16x32_bf16 v[164:167], v[64:67], a[36:39], v[164:167]\n"
"s_waitcnt lgkmcnt(4)\n"
"v_mfma_f32_16x16x32_bf16 v[168:171], v[80:83], a[32:35], v[168:171]\n"
"ds_read_b128 v[64:67], v1 offset:29184\n"
"buffer_load_dwordx4 a[244:247], v0, s[8:11], s63 offen offset:2048 sc1\n"
"v_mfma_f32_16x16x32_bf16 v[168:171], v[84:87], a[40:43], v[168:171]\n"
"s_waitcnt vmcnt(16)\n"
"ds_read_b128 v[68:71], v1 offset:30208\n"
"v_cmp_ne_u64 vcc, s[100:101], v[30:31]\n"
"v_mfma_f32_16x16x32_bf16 v[168:171], v[88:91], a[48:51], v[168:171]\n"
"ds_read_b128 v[72:75], v1 offset:31232\n"
"v_mfma_f32_16x16x32_bf16 v[168:171], v[92:95], a[56:59], v[168:171]\n"
"s_cbranch_vccnz L_ot\n"
"L_otr:\n"
"ds_read_b128 v[76:79], v1 offset:32256\n"
"v_mfma_f32_16x16x32_bf16 v[172:175], v[92:95], a[60:63], v[172:175]\n"
"buffer_load_dwordx4 a[252:255], v0, s[8:11], s63 offen offset:3072 sc1\n"
"v_mfma_f32_16x16x32_bf16 v[172:175], v[88:91], a[52:55], v[172:175]\n"
"s_add_u32 m0, s55, 50176\n"
"v_mfma_f32_16x16x32_bf16 v[172:175], v[84:87], a[44:47], v[172:175]\n"
"s_add_u32 s59, s59, 16384\n"
"v_mfma_f32_16x16x32_bf16 v[172:175], v[80:83], a[36:39], v[172:175]\n"
"s_waitcnt lgkmcnt(4)\n"
"buffer_load_dwordx4 v0, s[8:11], s59 offen sc1 lds\n"
"v_mfma_f32_16x16x32_bf16 v[176:179], v[48:51], a[32:35], v[176:179]\n"
"ds_read_b128 v[80:83], v1 offset:33280\n"
"v_mfma_f32_16x16x32_bf16 v[176:179], v[52:55], a[40:43], v[176:179]\n"
"ds_read_b128 v[84:87], v1 offset:34304\n"
"v_mfma_f32_16x16x32_bf16 v[176:179], v[56:59], a[48:51], v[176:179]\n"
"ds_read_b128 v[88:91], v1 offset:35328\n"
"v_mfma_f32_16x16x32_bf16 v[176:179], v[60:63], a[56:59], v[176:179]\n"
"ds_read_b128 v[92:95], v1 offset:36352\n"
"buffer_load_dwordx4 v0, s[8:11], s59 offen offset:1024 sc1 lds\n"
"v_mfma_f32_16x16x32_bf16 v[180:183], v[60:63], a[60:63], v[180:183]\n"
"v_mfma_f32_16x16x32_bf16 v[180:183], v[56:59], a[52:55], v[180:183]\n"
"v_mfma_f32_16x16x32_bf16 v[180:183], v[52:55], a[44:47], v[180:183]\n"
"buffer_load_dwordx4 v0, s[8:11], s59 offen offset:2048 sc1 lds\n"
"v_mfma_f32_16x16x32_bf16 v[180:183], v[48:51], a[36:39], v[180:183]\n"
"s_waitcnt lgkmcnt(4)\n"
"v_mfma_f32_16x16x32_bf16 v[184:187], v[64:67], a[32:35], v[184:187]\n"
"ds_read_b128 v[48:51], v1 offset:37376\n"
"ds_read_b128 v[52:55], v1 offset:38400\n"
"v_mfma_f32_16x16x32_bf16 v[184:187], v[68:71], a[40:43], v[184:187]\n"
"ds_read_b128 v[56:59], v1 offset:39424\n"
"ds_read_b128 v[60:63], v1 offset:40448\n"
"v_mfma_f32_16x16x32_bf16 v[184:187], v[72:75], a[48:51], v[184:187]\n"
"buffer_load_dwordx4 v0, s[8:11], s59 offen offset:3072 sc1 lds\n"
"v_mfma_f32_16x16x32_bf16 v[184:187], v[76:79], a[56:59], v[184:187]\n"
"s_add_u32 m0, s55, 141312\n"
"v_mfma_f32_16x16x32_bf16 v[188:191], v[76:79], a[60:63], v[188:191]\n"
"s_add_u32 s59, s59, 16384\n"
"v_mfma_f32_16x16x32_bf16 v[188:191], v[72:75], a[52:55], v[188:191]\n"
"buffer_load_dwordx4 v0, s[8:11], s59 offen sc1 lds\n"
"v_mfma_f32_16x16x32_bf16 v[188:191], v[68:71], a[44:47], v[188:191]\n"
"v_mfma_f32_16x16x32_bf16 v[188:191], v[64:67], a[36:39], v[188:191]\n"
"s_waitcnt vmcnt(35) lgkmcnt(0)\n"
"v_mfma_f32_16x16x32_bf16 v[192:195], v[80:83], a[32:35], v[192:195]\n"
"s_barrier\n"
"ds_read_b128 v[64:67], v1 offset:58880\n"
"v_mfma_f32_16x16x32_bf16 v[192:195], v[84:87], a[40:43], v[192:195]\n"
"ds_read_b128 v[68:71], v1 offset:59904\n"
"buffer_load_dwordx4 v0, s[8:11], s59 offen offset:1024 sc1 lds\n"
"v_mfma_f32_16x16x32_bf16 v[192:195], v[88:91], a[48:51], v[192:195]\n"
"ds_read_b128 v[72:75], v1 offset:60928\n"
"v_mfma_f32_16x16x32_bf16 v[192:195], v[92:95], a[56:59], v[192:195]\n"
"ds_read_b128 v[76:79], v1 offset:61952\n"
"v_mfma_f32_16x16x32_bf16 v[196:199], v[92:95], a[60:63], v[196:199]\n"
"buffer_load_dwordx4 v0, s[8:11], s59 offen offset:2048 sc1 lds\n"
"v_mfma_f32_16x16x32_bf16 v[196:199], v[88:91], a[52:55], v[196:199]\n"
"v_mfma_f32_16x16x32_bf16 v[196:199], v[84:87], a[44:47], v[196:199]\n"
"v_mfma_f32_16x16x32_bf16 v[196:199], v[80:83], a[36:39], v[196:199]\n"
"s_waitcnt lgkmcnt(4)\n"
"v_mfma_f32_16x16x32_bf16 v[200:203], v[48:51], a[32:35], v[200:203]\n"
"ds_read_b128 v[80:83], v1 offset:62976\n"
"buffer_load_dwordx4 v0, s[8:11], s59 offen offset:3072 sc1 lds\n"
"v_mfma_f32_16x16x32_bf16 v[200:203], v[52:55], a[40:43], v[200:203]\n"
"ds_read_b128 v[84:87], v1 offset:64000\n"
"v_mfma_f32_16x16x32_bf16 v[200:203], v[56:59], a[48:51], v[200:203]\n"
"ds_read_b128 v[88:91], v1 offset:65024\n"
"v_mfma_f32_16x16x32_bf16 v[200:203], v[60:63], a[56:59], v[200:203]\n"
"ds_read_b128 v[92:95], v2 offset:512\n"
"buffer_load_dwordx2 v[28:29], v3, s[40:43], s62 offen offset:0\n"
"v_mfma_f32_16x16x32_bf16 v[204:207], v[60:63], a[60:63], v[204:207]\n"
"v_mfma_f32_16x16x32_bf16 v[204:207], v[56:59], a[52:55], v[204:207]\n"
"v_mfma_f32_16x16x32_bf16 v[204:207], v[52:55], a[44:47], v[204:207]\n"
"v_mfma_f32_16x16x32_bf16 v[204:207], v[48:51], a[36:39], v[204:207]\n"
"s_waitcnt lgkmcnt(4)\n"
"buffer_load_dwordx2 v[252:253], v3, s[40:43], s65 offen offset:0\n"
"v_mfma_f32_16x16x32_bf16 v[208:211], v[64:67], a[32:35], v[208:211]\n"
"ds_read_b128 v[48:51], v2 offset:1536\n"
"v_mfma_f32_16x16x32_bf16 v[208:211], v[68:71], a[40:43], v[208:211]\n"
"s_add_u32 s58, s58, 4096\n"
"ds_read_b128 v[52:55], v2 offset:2560\n"
"v_mfma_f32_16x16x32_bf16 v[208:211], v[72:75], a[48:51], v[208:211]\n"
"ds_read_b128 v[56:59], v2 offset:3584\n"
"buffer_load_dwordx4 a[192:195], v0, s[8:11], s58 offen offset:0 sc1\n"
"v_mfma_f32_16x16x32_bf16 v[208:211], v[76:79], a[56:59], v[208:211]\n"
"ds_read_b128 v[60:63], v2 offset:4608\n"
"v_mfma_f32_16x16x32_bf16 v[212:215], v[76:79], a[60:63], v[212:215]\n"
"v_mfma_f32_16x16x32_bf16 v[212:215], v[72:75], a[52:55], v[212:215]\n"
"v_mfma_f32_16x16x32_bf16 v[212:215], v[68:71], a[44:47], v[212:215]\n"
"buffer_load_dwordx4 a[200:203], v0, s[8:11], s58 offen offset:1024 sc1\n"
"v_mfma_f32_16x16x32_bf16 v[212:215], v[64:67], a[36:39], v[212:215]\n"
"s_waitcnt lgkmcnt(4)\n"
"v_mfma_f32_16x16x32_bf16 v[216:219], v[80:83], a[32:35], v[216:219]\n"
"ds_read_b128 v[64:67], v2 offset:5632\n"
"v_mfma_f32_16x16x32_bf16 v[216:219], v[84:87], a[40:43], v[216:219]\n"
"ds_read_b128 v[68:71], v2 offset:6656\n"
"buffer_load_dwordx4 a[208:211], v0, s[8:11], s58 offen offset:2048 sc1\n"
"v_mfma_f32_16x16x32_bf16 v[216:219], v[88:91], a[48:51], v[216:219]\n"
"ds_read_b128 v[72:75], v2 offset:7680\n"
"v_mfma_f32_16x16x32_bf16 v[216:219], v[92:95], a[56:59], v[216:219]\n"
"ds_read_b128 v[76:79], v2 offset:8704\n"
"v_mfma_f32_16x16x32_bf16 v[220:223], v[92:95], a[60:63], v[220:223]\n"
"v_mfma_f32_16x16x32_bf16 v[220:223], v[88:91], a[52:55], v[220:223]\n"
"buffer_load_dwordx4 a[216:219], v0, s[8:11], s58 offen offset:3072 sc1\n"
"v_mfma_f32_16x16x32_bf16 v[220:223], v[84:87], a[44:47], v[220:223]\n"
"s_add_u32 s63, s63, 4096\n"
"v_mfma_f32_16x16x32_bf16 v[220:223], v[80:83], a[36:39], v[220:223]\n"
"s_waitcnt lgkmcnt(4)\n"
"v_mfma_f32_16x16x32_bf16 v[224:227], v[48:51], a[32:35], v[224:227]\n"
"ds_read_b128 v[80:83], v0 offset:0\n"
"buffer_load_dwordx4 a[196:199], v0, s[8:11], s63 offen offset:0 sc1\n"
"v_mfma_f32_16x16x32_bf16 v[224:227], v[52:55], a[40:43], v[224:227]\n"
"ds_read_b128 v[84:87], v0 offset:1024\n"
"v_mfma_f32_16x16x32_bf16 v[224:227], v[56:59], a[48:51], v[224:227]\n"
"ds_read_b128 v[88:91], v0 offset:2048\n"
"v_mfma_f32_16x16x32_bf16 v[224:227], v[60:63], a[56:59], v[224:227]\n"
"ds_read_b128 v[92:95], v0 offset:3072\n"
"v_mfma_f32_16x16x32_bf16 v[228:231], v[60:63], a[60:63], v[228:231]\n"
"buffer_load_dwordx4 a[204:207], v0, s[8:11], s63 offen offset:1024 sc1\n"
"v_mfma_f32_16x16x32_bf16 v[228:231], v[56:59], a[52:55], v[228:231]\n"
"v_mfma_f32_16x16x32_bf16 v[228:231], v[52:55], a[44:47], v[228:231]\n"
"v_mfma_f32_16x16x32_bf16 v[228:231], v[48:51], a[36:39], v[228:231]\n"
"s_waitcnt vmcnt(32) lgkmcnt(4)\n"
"buffer_load_dwordx4 a[212:215], v0, s[8:11], s63 offen offset:2048 sc1\n"
"v_mfma_f32_16x16x32_bf16 v[160:163], v[64:67], a[160:163], v[160:163]\n"
"ds_read_b128 v[48:51], v0 offset:4096\n"
"v_mfma_f32_16x16x32_bf16 v[160:163], v[68:71], a[168:171], v[160:163]\n"
"ds_read_b128 v[52:55], v0 offset:5120\n"
"v_mfma_f32_16x16x32_bf16 v[160:163], v[72:75], a[176:179], v[160:163]\n"
"ds_read_b128 v[56:59], v0 offset:6144\n"
"v_mfma_f32_16x16x32_bf16 v[160:163], v[76:79], a[184:187], v[160:163]\n"
"ds_read_b128 v[60:63], v0 offset:7168\n"
"buffer_load_dwordx4 a[220:223], v0, s[8:11], s63 offen offset:3072 sc1\n"
"v_mfma_f32_16x16x32_bf16 v[164:167], v[76:79], a[188:191], v[164:167]\n"
"s_add_u32 m0, s55, 66560\n"
"v_mfma_f32_16x16x32_bf16 v[164:167], v[72:75], a[180:183], v[164:167]\n"
"s_add_u32 s59, s59, 16384\n"
"v_mfma_f32_16x16x32_bf16 v[164:167], v[68:71], a[172:175], v[164:167]\n"
"buffer_load_dwordx4 v0, s[8:11], s59 offen sc1 lds\n"
"v_mfma_f32_16x16x32_bf16 v[164:167], v[64:67], a[164:167], v[164:167]\n"
"s_waitcnt lgkmcnt(4)\n"
"v_mfma_f32_16x16x32_bf16 v[168:171], v[80:83], a[160:163], v[168:171]\n"
"ds_read_b128 v[64:67], v0 offset:8192\n"
"v_mfma_f32_16x16x32_bf16 v[168:171], v[84:87], a[168:171], v[168:171]\n"
"ds_read_b128 v[68:71], v0 offset:9216\n"
"v_mfma_f32_16x16x32_bf16 v[168:171], v[88:91], a[176:179], v[168:171]\n"
"ds_read_b128 v[72:75], v0 offset:10240\n"
"buffer_load_dwordx4 v[96:99], v0, s[16:19], s80 offen offset:0 sc1\n"
"v_mfma_f32_16x16x32_bf16 v[168:171], v[92:95], a[184:187], v[168:171]\n"
"ds_read_b128 v[76:79], v0 offset:11264\n"
"v_mfma_f32_16x16x32_bf16 v[172:175], v[92:95], a[188:191], v[172:175]\n"
"v_mfma_f32_16x16x32_bf16 v[172:175], v[88:91], a[180:183], v[172:175]\n"
"buffer_load_dwordx4 v[100:103], v0, s[16:19], s80 offen offset:1024 sc1\n"
"v_mfma_f32_16x16x32_bf16 v[172:175], v[84:87], a[172:175], v[172:175]\n"
"v_mfma_f32_16x16x32_bf16 v[172:175], v[80:83], a[164:167], v[172:175]\n"
"s_waitcnt lgkmcnt(4)\n"
"v_mfma_f32_16x16x32_bf16 v[176:179], v[48:51], a[160:163], v[176:179]\n"
"ds_read_b128 v[80:83], v0 offset:12288\n"
"ds_read_b128 v[84:87], v0 offset:13312\n"
"v_mfma_f32_16x16x32_bf16 v[176:179], v[52:55], a[168:171], v[176:179]\n"
"ds_read_b128 v[88:91], v0 offset:14336\n"
"ds_read_b128 v[92:95], v0 offset:15360\n"
"v_mfma_f32_16x16x32_bf16 v[176:179], v[56:59], a[176:179], v[176:179]\n"
"buffer_load_dwordx4 v[104:107], v0, s[16:19], s80 offen offset:2048 sc1\n"
"v_mfma_f32_16x16x32_bf16 v[176:179], v[60:63], a[184:187], v[176:179]\n"
"v_mfma_f32_16x16x32_bf16 v[180:183], v[60:63], a[188:191], v[180:183]\n"
"buffer_load_dwordx4 v[108:111], v0, s[16:19], s80 offen offset:3072 sc1\n"
"v_mfma_f32_16x16x32_bf16 v[180:183], v[56:59], a[180:183], v[180:183]\n"
"v_mfma_f32_16x16x32_bf16 v[180:183], v[52:55], a[172:175], v[180:183]\n"
"v_mfma_f32_16x16x32_bf16 v[180:183], v[48:51], a[164:167], v[180:183]\n"
"s_waitcnt vmcnt(31) lgkmcnt(0)\n"
"v_mfma_f32_16x16x32_bf16 v[184:187], v[64:67], a[160:163], v[184:187]\n"
"s_barrier\n"
"ds_read_b128 v[48:51], v0 offset:16384\n"
"buffer_load_dwordx4 v[112:115], v0, s[16:19], 0 offen offset:0 sc1\n"
"v_mfma_f32_16x16x32_bf16 v[184:187], v[68:71], a[168:171], v[184:187]\n"
"ds_read_b128 v[52:55], v0 offset:17408\n"
"v_mfma_f32_16x16x32_bf16 v[184:187], v[72:75], a[176:179], v[184:187]\n"
"ds_read_b128 v[56:59], v0 offset:18432\n"
"v_mfma_f32_16x16x32_bf16 v[184:187], v[76:79], a[184:187], v[184:187]\n"
"ds_read_b128 v[60:63], v0 offset:19456\n"
"buffer_load_dwordx4 v[116:119], v0, s[16:19], 0 offen offset:1024 sc1\n"
"v_mfma_f32_16x16x32_bf16 v[188:191], v[76:79], a[188:191], v[188:191]\n"
"v_mfma_f32_16x16x32_bf16 v[188:191], v[72:75], a[180:183], v[188:191]\n"
"v_mfma_f32_16x16x32_bf16 v[188:191], v[68:71], a[172:175], v[188:191]\n"
"v_mfma_f32_16x16x32_bf16 v[188:191], v[64:67], a[164:167], v[188:191]\n"
"s_waitcnt lgkmcnt(4)\n"
"buffer_load_dwordx4 v[120:123], v0, s[16:19], 0 offen offset:2048 sc1\n"
"v_mfma_f32_16x16x32_bf16 v[192:195], v[80:83], a[160:163], v[192:195]\n"
"ds_read_b128 v[64:67], v0 offset:20480\n"
"v_mfma_f32_16x16x32_bf16 v[192:195], v[84:87], a[168:171], v[192:195]\n"
"ds_read_b128 v[68:71], v0 offset:21504\n"
"v_mfma_f32_16x16x32_bf16 v[192:195], v[88:91], a[176:179], v[192:195]\n"
"ds_read_b128 v[72:75], v0 offset:22528\n"
"buffer_load_dwordx4 v[124:127], v0, s[16:19], 0 offen offset:3072 sc1\n"
"v_mfma_f32_16x16x32_bf16 v[192:195], v[92:95], a[184:187], v[192:195]\n"
"ds_read_b128 v[76:79], v0 offset:23552\n"
"v_mfma_f32_16x16x32_bf16 v[196:199], v[92:95], a[188:191], v[196:199]\n"
"v_mfma_f32_16x16x32_bf16 v[196:199], v[88:91], a[180:183], v[196:199]\n"
"v_mfma_f32_16x16x32_bf16 v[196:199], v[84:87], a[172:175], v[196:199]\n"
"buffer_load_dwordx4 v[128:131], v0, s[16:19], s60 offen offset:0 sc1\n"
"v_mfma_f32_16x16x32_bf16 v[196:199], v[80:83], a[164:167], v[196:199]\n"
"s_waitcnt lgkmcnt(4)\n"
"v_mfma_f32_16x16x32_bf16 v[200:203], v[48:51], a[160:163], v[200:203]\n"
"ds_read_b128 v[80:83], v0 offset:24576\n"
"v_mfma_f32_16x16x32_bf16 v[200:203], v[52:55], a[168:171], v[200:203]\n"
"ds_read_b128 v[84:87], v0 offset:25600\n"
"buffer_load_dwordx4 v[132:135], v0, s[16:19], s60 offen offset:1024 sc1\n"
"v_mfma_f32_16x16x32_bf16 v[200:203], v[56:59], a[176:179], v[200:203]\n"
"ds_read_b128 v[88:91], v0 offset:26624\n"
"v_mfma_f32_16x16x32_bf16 v[200:203], v[60:63], a[184:187], v[200:203]\n"
"ds_read_b128 v[92:95], v0 offset:27648\n"
"v_mfma_f32_16x16x32_bf16 v[204:207], v[60:63], a[188:191], v[204:207]\n"
"v_mfma_f32_16x16x32_bf16 v[204:207], v[56:59], a[180:183], v[204:207]\n"
"buffer_load_dwordx4 v0, s[8:11], s59 offen offset:1024 sc1 lds\n"
"v_mfma_f32_16x16x32_bf16 v[204:207], v[52:55], a[172:175], v[204:207]\n"
"v_mfma_f32_16x16x32_bf16 v[204:207], v[48:51], a[164:167], v[204:207]\n"
"s_waitcnt lgkmcnt(4)\n"
"v_mfma_f32_16x16x32_bf16 v[208:211], v[64:67], a[160:163], v[208:211]\n"
"ds_read_b128 v[48:51], v0 offset:28672\n"
"buffer_load_dwordx4 v0, s[8:11], s59 offen offset:2048 sc1 lds\n"
"v_mfma_f32_16x16x32_bf16 v[208:211], v[68:71], a[168:171], v[208:211]\n"
"ds_read_b128 v[52:55], v0 offset:29696\n"
"v_mfma_f32_16x16x32_bf16 v[208:211], v[72:75], a[176:179], v[208:211]\n"
"ds_read_b128 v[56:59], v0 offset:30720\n"
"v_mfma_f32_16x16x32_bf16 v[208:211], v[76:79], a[184:187], v[208:211]\n"
"ds_read_b128 v[60:63], v0 offset:31744\n"
"v_mfma_f32_16x16x32_bf16 v[212:215], v[76:79], a[188:191], v[212:215]\n"
"buffer_load_dwordx4 v0, s[8:11], s59 offen offset:3072 sc1 lds\n"
"v_mfma_f32_16x16x32_bf16 v[212:215], v[72:75], a[180:183], v[212:215]\n"
"s_add_u32 m0, s55, 107520\n"
"v_mfma_f32_16x16x32_bf16 v[212:215], v[68:71], a[172:175], v[212:215]\n"
"s_add_u32 s59, s59, 16384\n"
"v_mfma_f32_16x16x32_bf16 v[212:215], v[64:67], a[164:167], v[212:215]\n"
"s_waitcnt lgkmcnt(4)\n"
"buffer_load_dwordx4 v0, s[8:11], s59 offen sc1 lds\n"
"v_mfma_f32_16x16x32_bf16 v[216:219], v[80:83], a[160:163], v[216:219]\n"
"ds_read_b128 v[64:67], v0 offset:33792\n"
"v_mfma_f32_16x16x32_bf16 v[216:219], v[84:87], a[168:171], v[216:219]\n"
"ds_read_b128 v[68:71], v0 offset:34816\n"
"v_mfma_f32_16x16x32_bf16 v[216:219], v[88:91], a[176:179], v[216:219]\n"
"ds_read_b128 v[72:75], v0 offset:35840\n"
"v_mfma_f32_16x16x32_bf16 v[216:219], v[92:95], a[184:187], v[216:219]\n"
"ds_read_b128 v[76:79], v0 offset:36864\n"
"buffer_load_dwordx4 v0, s[8:11], s59 offen offset:1024 sc1 lds\n"
"v_mfma_f32_16x16x32_bf16 v[220:223], v[92:95], a[188:191], v[220:223]\n"
"v_mfma_f32_16x16x32_bf16 v[220:223], v[88:91], a[180:183], v[220:223]\n"
"v_mfma_f32_16x16x32_bf16 v[220:223], v[84:87], a[172:175], v[220:223]\n"
"buffer_load_dwordx4 v0, s[8:11], s59 offen offset:2048 sc1 lds\n"
"v_mfma_f32_16x16x32_bf16 v[220:223], v[80:83], a[164:167], v[220:223]\n"
"s_waitcnt lgkmcnt(4)\n"
"v_mfma_f32_16x16x32_bf16 v[224:227], v[48:51], a[160:163], v[224:227]\n"
"ds_read_b128 v[80:83], v0 offset:37888\n"
"v_mfma_f32_16x16x32_bf16 v[224:227], v[52:55], a[168:171], v[224:227]\n"
"ds_read_b128 v[84:87], v0 offset:38912\n"
"v_mfma_f32_16x16x32_bf16 v[224:227], v[56:59], a[176:179], v[224:227]\n"
"ds_read_b128 v[88:91], v0 offset:39936\n"
"buffer_load_dwordx4 v0, s[8:11], s59 offen offset:3072 sc1 lds\n"
"v_mfma_f32_16x16x32_bf16 v[224:227], v[60:63], a[184:187], v[224:227]\n"
"ds_read_b128 v[92:95], v0 offset:40960\n"
"v_mfma_f32_16x16x32_bf16 v[228:231], v[60:63], a[188:191], v[228:231]\n"
"s_add_u32 s58, s58, 4096\n"
"v_mfma_f32_16x16x32_bf16 v[228:231], v[56:59], a[180:183], v[228:231]\n"
"buffer_load_dwordx4 a[0:3], v0, s[8:11], s58 offen offset:0 sc1\n"
"v_mfma_f32_16x16x32_bf16 v[228:231], v[52:55], a[172:175], v[228:231]\n"
"v_mfma_f32_16x16x32_bf16 v[228:231], v[48:51], a[164:167], v[228:231]\n"
"s_waitcnt vmcnt(37) lgkmcnt(4)\n"
"v_mfma_f32_16x16x32_bf16 v[160:163], v[64:67], a[224:227], v[160:163]\n"
"ds_read_b128 v[48:51], v0 offset:41984\n"
"v_mfma_f32_16x16x32_bf16 v[160:163], v[68:71], a[232:235], v[160:163]\n"
"ds_read_b128 v[52:55], v0 offset:43008\n"
"buffer_load_dwordx4 a[8:11], v0, s[8:11], s58 offen offset:1024 sc1\n"
"v_mfma_f32_16x16x32_bf16 v[160:163], v[72:75], a[240:243], v[160:163]\n"
"ds_read_b128 v[56:59], v0 offset:44032\n"
"v_mfma_f32_16x16x32_bf16 v[160:163], v[76:79], a[248:251], v[160:163]\n"
"ds_read_b128 v[60:63], v0 offset:45056\n"
"v_mfma_f32_16x16x32_bf16 v[164:167], v[76:79], a[252:255], v[164:167]\n"
"buffer_load_dwordx4 a[16:19], v0, s[8:11], s58 offen offset:2048 sc1\n"
"v_mfma_f32_16x16x32_bf16 v[164:167], v[72:75], a[244:247], v[164:167]\n"
"v_mfma_f32_16x16x32_bf16 v[164:167], v[68:71], a[236:239], v[164:167]\n"
"v_mfma_f32_16x16x32_bf16 v[164:167], v[64:67], a[228:231], v[164:167]\n"
"s_waitcnt lgkmcnt(4)\n"
"v_mfma_f32_16x16x32_bf16 v[168:171], v[80:83], a[224:227], v[168:171]\n"
"ds_read_b128 v[64:67], v0 offset:46080\n"
"ds_read_b128 v[68:71], v0 offset:47104\n"
"v_mfma_f32_16x16x32_bf16 v[168:171], v[84:87], a[232:235], v[168:171]\n"
"s_add_u32 m0, s55, 90624\n"
"ds_read_b128 v[72:75], v0 offset:48128\n"
"ds_read_b128 v[76:79], v0 offset:49152\n"
"v_mfma_f32_16x16x32_bf16 v[168:171], v[88:91], a[240:243], v[168:171]\n"
"s_add_u32 s59, s59, 16384\n"
"buffer_load_dwordx4 a[24:27], v0, s[8:11], s58 offen offset:3072 sc1\n"
"v_mfma_f32_16x16x32_bf16 v[168:171], v[92:95], a[248:251], v[168:171]\n"
"buffer_load_dwordx4 v0, s[8:11], s59 offen sc1 lds\n"
"v_mfma_f32_16x16x32_bf16 v[172:175], v[92:95], a[252:255], v[172:175]\n"
"v_mfma_f32_16x16x32_bf16 v[172:175], v[88:91], a[244:247], v[172:175]\n"
"v_mfma_f32_16x16x32_bf16 v[172:175], v[84:87], a[236:239], v[172:175]\n"
"s_add_u32 s63, s63, 4096\n"
"v_mfma_f32_16x16x32_bf16 v[172:175], v[80:83], a[228:231], v[172:175]\n"
"s_waitcnt vmcnt(33) lgkmcnt(0)\n"
"buffer_load_dwordx4 a[4:7], v0, s[8:11], s63 offen offset:0 sc1\n"
"v_mfma_f32_16x16x32_bf16 v[176:179], v[48:51], a[224:227], v[176:179]\n"
"s_barrier\n"
"ds_read_b128 v[80:83], v0 offset:50176\n"
"v_mfma_f32_16x16x32_bf16 v[176:179], v[52:55], a[232:235], v[176:179]\n"
"ds_read_b128 v[84:87], v0 offset:51200\n"
"v_mfma_f32_16x16x32_bf16 v[176:179], v[56:59], a[240:243], v[176:179]\n"
"ds_read_b128 v[88:91], v0 offset:52224\n"
"buffer_load_dwordx4 a[12:15], v0, s[8:11], s63 offen offset:1024 sc1\n"
"v_mfma_f32_16x16x32_bf16 v[176:179], v[60:63], a[248:251], v[176:179]\n"
"ds_read_b128 v[92:95], v0 offset:53248\n"
"v_mfma_f32_16x16x32_bf16 v[180:183], v[60:63], a[252:255], v[180:183]\n"
"v_mfma_f32_16x16x32_bf16 v[180:183], v[56:59], a[244:247], v[180:183]\n"
"v_mfma_f32_16x16x32_bf16 v[180:183], v[52:55], a[236:239], v[180:183]\n"
"buffer_load_dwordx4 a[20:23], v0, s[8:11], s63 offen offset:2048 sc1\n"
"v_mfma_f32_16x16x32_bf16 v[180:183], v[48:51], a[228:231], v[180:183]\n"
"s_waitcnt lgkmcnt(4)\n"
"v_mfma_f32_16x16x32_bf16 v[184:187], v[64:67], a[224:227], v[184:187]\n"
"ds_read_b128 v[48:51], v0 offset:54272\n"
"v_mfma_f32_16x16x32_bf16 v[184:187], v[68:71], a[232:235], v[184:187]\n"
"ds_read_b128 v[52:55], v0 offset:55296\n"
"buffer_load_dwordx4 a[28:31], v0, s[8:11], s63 offen offset:3072 sc1\n"
"v_mfma_f32_16x16x32_bf16 v[184:187], v[72:75], a[240:243], v[184:187]\n"
"ds_read_b128 v[56:59], v0 offset:56320\n"
"v_mfma_f32_16x16x32_bf16 v[184:187], v[76:79], a[248:251], v[184:187]\n"
"ds_read_b128 v[60:63], v0 offset:57344\n"
"v_mfma_f32_16x16x32_bf16 v[188:191], v[76:79], a[252:255], v[188:191]\n"
"s_add_u32 s58, s58, 4096\n"
"v_mfma_f32_16x16x32_bf16 v[188:191], v[72:75], a[244:247], v[188:191]\n"
"buffer_load_dwordx4 a[128:131], v0, s[8:11], s58 offen offset:0 sc1\n"
"v_mfma_f32_16x16x32_bf16 v[188:191], v[68:71], a[236:239], v[188:191]\n"
"v_mfma_f32_16x16x32_bf16 v[188:191], v[64:67], a[228:231], v[188:191]\n"
"s_waitcnt lgkmcnt(4)\n"
"v_mfma_f32_16x16x32_bf16 v[192:195], v[80:83], a[224:227], v[192:195]\n"
"ds_read_b128 v[64:67], v0 offset:58368\n"
"buffer_load_dwordx4 a[136:139], v0, s[8:11], s58 offen offset:1024 sc1\n"
"v_mfma_f32_16x16x32_bf16 v[192:195], v[84:87], a[232:235], v[192:195]\n"
"ds_read_b128 v[68:71], v0 offset:59392\n"
"v_mfma_f32_16x16x32_bf16 v[192:195], v[88:91], a[240:243], v[192:195]\n"
"ds_read_b128 v[72:75], v0 offset:60416\n"
"v_mfma_f32_16x16x32_bf16 v[192:195], v[92:95], a[248:251], v[192:195]\n"
"ds_read_b128 v[76:79], v0 offset:61440\n"
"v_mfma_f32_16x16x32_bf16 v[196:199], v[92:95], a[252:255], v[196:199]\n"
"buffer_load_dwordx4 a[144:147], v0, s[8:11], s58 offen offset:2048 sc1\n"
"v_mfma_f32_16x16x32_bf16 v[196:199], v[88:91], a[244:247], v[196:199]\n"
"v_mfma_f32_16x16x32_bf16 v[196:199], v[84:87], a[236:239], v[196:199]\n"
"v_mfma_f32_16x16x32_bf16 v[196:199], v[80:83], a[228:231], v[196:199]\n"
"s_waitcnt lgkmcnt(4)\n"
"buffer_load_dwordx4 a[152:155], v0, s[8:11], s58 offen offset:3072 sc1\n"
"v_mfma_f32_16x16x32_bf16 v[200:203], v[48:51], a[224:227], v[200:203]\n"
"ds_read_b128 v[80:83], v0 offset:62464\n"
"v_mfma_f32_16x16x32_bf16 v[200:203], v[52:55], a[232:235], v[200:203]\n"
"ds_read_b128 v[84:87], v0 offset:63488\n"
"v_mfma_f32_16x16x32_bf16 v[200:203], v[56:59], a[240:243], v[200:203]\n"
"s_add_u32 s63, s63, 4096\n"
"ds_read_b128 v[88:91], v0 offset:64512\n"
"v_mfma_f32_16x16x32_bf16 v[200:203], v[60:63], a[248:251], v[200:203]\n"
"ds_read_b128 v[92:95], v1 offset:0\n"
"buffer_load_dwordx4 a[132:135], v0, s[8:11], s63 offen offset:0 sc1\n"
"v_mfma_f32_16x16x32_bf16 v[204:207], v[60:63], a[252:255], v[204:207]\n"
"v_mfma_f32_16x16x32_bf16 v[204:207], v[56:59], a[244:247], v[204:207]\n"
"v_mfma_f32_16x16x32_bf16 v[204:207], v[52:55], a[236:239], v[204:207]\n"
"buffer_load_dwordx4 a[140:143], v0, s[8:11], s63 offen offset:1024 sc1\n"
"v_mfma_f32_16x16x32_bf16 v[204:207], v[48:51], a[228:231], v[204:207]\n"
"s_waitcnt lgkmcnt(4)\n"
"v_mfma_f32_16x16x32_bf16 v[208:211], v[64:67], a[224:227], v[208:211]\n"
"s_waitcnt vmcnt(22)\n"
"ds_read_b128 v[48:51], v2 offset:10240\n"
"v_mfma_f32_16x16x32_bf16 v[208:211], v[68:71], a[232:235], v[208:211]\n"
"ds_read_b128 v[52:55], v2 offset:11264\n"
"v_max3_u32 v158, v96, v97, v98\n"
"v_mfma_f32_16x16x32_bf16 v[208:211], v[72:75], a[240:243], v[208:211]\n"
"ds_read_b128 v[56:59], v2 offset:12288\n"
"v_max3_u32 v158, v158, v99, v100\n"
"v_mfma_f32_16x16x32_bf16 v[208:211], v[76:79], a[248:251], v[208:211]\n"
"ds_read_b128 v[60:63], v2 offset:13312\n"
"v_max3_u32 v158, v158, v101, v102\n"
"v_mfma_f32_16x16x32_bf16 v[212:215], v[76:79], a[252:255], v[212:215]\n"
"buffer_load_dwordx4 a[148:151], v0, s[8:11], s63 offen offset:2048 sc1\n"
"v_max3_u32 v158, v158, v103, v104\n"
"v_mfma_f32_16x16x32_bf16 v[212:215], v[72:75], a[244:247], v[212:215]\n"
"v_max3_u32 v158, v158, v105, v106\n"
"v_max3_u32 v158, v158, v107, v108\n"
"v_mfma_f32_16x16x32_bf16 v[212:215], v[68:71], a[236:239], v[212:215]\n"
"v_max3_u32 v158, v158, v109, v110\n"
"v_max3_u32 v158, v158, v111, v112\n"
"v_mfma_f32_16x16x32_bf16 v[212:215], v[64:67], a[228:231], v[212:215]\n"
"s_waitcnt vmcnt(34) lgkmcnt(4)\n"
"v_max3_u32 v158, v158, v113, v114\n"
"v_max3_u32 v158, v158, v115, v116\n"
"v_mfma_f32_16x16x32_bf16 v[160:163], v[80:83], a[192:195], v[160:163]\n"
"ds_read_b128 v[64:67], v2 offset:14336\n"
"buffer_load_dwordx4 a[156:159], v0, s[8:11], s63 offen offset:3072 sc1\n"
"v_mfma_f32_16x16x32_bf16 v[160:163], v[84:87], a[200:203], v[160:163]\n"
"ds_read_b128 v[68:71], v2 offset:15360\n"
"v_max3_u32 v158, v158, v117, v118\n"
"v_mfma_f32_16x16x32_bf16 v[160:163], v[88:91], a[208:211], v[160:163]\n"
"ds_read_b128 v[72:75], v2 offset:16384\n"
"v_max3_u32 v158, v158, v119, v120\n"
"v_mfma_f32_16x16x32_bf16 v[160:163], v[92:95], a[216:219], v[160:163]\n"
"s_add_u32 s58, s58, 4096\n"
"ds_read_b128 v[76:79], v2 offset:17408\n"
"v_max3_u32 v158, v158, v121, v122\n"
"v_mfma_f32_16x16x32_bf16 v[164:167], v[92:95], a[220:223], v[164:167]\n"
"buffer_load_dwordx4 a[32:35], v0, s[8:11], s58 offen offset:0 sc1\n"
"v_max3_u32 v158, v158, v123, v124\n"
"v_mfma_f32_16x16x32_bf16 v[164:167], v[88:91], a[212:215], v[164:167]\n"
"v_max3_u32 v158, v158, v125, v126\n"
"v_max3_u32 v158, v158, v127, v128\n"
"v_mfma_f32_16x16x32_bf16 v[164:167], v[84:87], a[204:207], v[164:167]\n"
"v_max3_u32 v158, v158, v129, v130\n"
"v_max3_u32 v158, v158, v131, v132\n"
"v_mfma_f32_16x16x32_bf16 v[164:167], v[80:83], a[196:199], v[164:167]\n"
"s_waitcnt lgkmcnt(4)\n"
"v_max3_u32 v158, v158, v133, v134\n"
"v_max_u32 v158, v158, v135\n"
"v_mfma_f32_16x16x32_bf16 v[168:171], v[48:51], a[192:195], v[168:171]\n"
"ds_read_b128 v[80:83], v2 offset:18432\n"
"buffer_load_dwordx4 a[40:43], v0, s[8:11], s58 offen offset:1024 sc1\n"
"v_mfma_f32_16x16x32_bf16 v[168:171], v[52:55], a[200:203], v[168:171]\n"
"ds_read_b128 v[84:87], v2 offset:19456\n"
"v_cmp_eq_u32 vcc, -1, v158\n"
"v_mfma_f32_16x16x32_bf16 v[168:171], v[56:59], a[208:211], v[168:171]\n"
"ds_read_b128 v[88:91], v2 offset:20480\n"
"buffer_load_dwordx4 a[48:51], v0, s[8:11], s58 offen offset:2048 sc1\n"
"v_mfma_f32_16x16x32_bf16 v[168:171], v[60:63], a[216:219], v[168:171]\n"
"s_cbranch_vccnz L_gr\n"
"L_grx:\n"
"ds_read_b128 v[92:95], v2 offset:21504\n"
"v_mfma_f32_16x16x32_bf16 v[144:147], v[154:157], v[96:99], 0\n"
"v_mfma_f32_16x16x32_bf16 v[172:175], v[60:63], a[220:223], v[172:175]\n"
"buffer_load_dwordx4 a[56:59], v0, s[8:11], s58 offen offset:3072 sc1\n"
"v_mfma_f32_16x16x32_bf16 v[172:175], v[56:59], a[212:215], v[172:175]\n"
"v_mfma_f32_16x16x32_bf16 v[148:151], v[152:155], v[96:99], 0\n"
"v_mfma_f32_16x16x32_bf16 v[172:175], v[52:55], a[204:207], v[172:175]\n"
"s_add_u32 s63, s63, 4096\n"
"v_mfma_f32_16x16x32_bf16 v[172:175], v[48:51], a[196:199], v[172:175]\n"
"s_waitcnt lgkmcnt(4)\n"
"buffer_load_dwordx4 a[36:39], v0, s[8:11], s63 offen offset:0 sc1\n"
"v_mfma_f32_16x16x32_bf16 v[144:147], v[154:157], v[100:103], v[144:147]\n"
"v_mfma_f32_16x16x32_bf16 v[176:179], v[64:67], a[192:195], v[176:179]\n"
"ds_read_b128 v[48:51], v2 offset:22528\n"
"ds_read_b128 v[52:55], v2 offset:23552\n"
"v_mfma_f32_16x16x32_bf16 v[176:179], v[68:71], a[200:203], v[176:179]\n"
"ds_read_b128 v[56:59], v2 offset:24576\n"
"ds_read_b128 v[60:63], v2 offset:25600\n"
"v_mfma_f32_16x16x32_bf16 v[8:11], v[154:157], v[112:115], v[8:11]\n"
"v_mfma_f32_16x16x32_bf16 v[176:179], v[72:75], a[208:211], v[176:179]\n"
"buffer_load_dwordx4 a[44:47], v0, s[8:11], s63 offen offset:1024 sc1\n"
"v_mfma_f32_16x16x32_bf16 v[176:179], v[76:79], a[216:219], v[176:179]\n"
"v_mfma_f32_16x16x32_bf16 v[16:19], v[154:157], v[116:119], v[16:19]\n"
"v_mfma_f32_16x16x32_bf16 v[180:183], v[76:79], a[220:223], v[180:183]\n"
"v_mfma_f32_16x16x32_bf16 v[180:183], v[72:75], a[212:215], v[180:183]\n"
"v_mfma_f32_16x16x32_bf16 v[148:151], v[152:155], v[100:103], v[148:151]\n"
"v_mfma_f32_16x16x32_bf16 v[180:183], v[68:71], a[204:207], v[180:183]\n"
"buffer_load_dwordx4 a[52:55], v0, s[8:11], s63 offen offset:2048 sc1\n"
"v_mfma_f32_16x16x32_bf16 v[180:183], v[64:67], a[196:199], v[180:183]\n"
"s_waitcnt vmcnt(24) lgkmcnt(0)\n"
"v_mfma_f32_16x16x32_bf16 v[12:15], v[152:155], v[112:115], v[12:15]\n"
"v_mfma_f32_16x16x32_bf16 v[184:187], v[80:83], a[192:195], v[184:187]\n"
"s_barrier\n"
"ds_read_b128 v[64:67], v1 offset:1024\n"
"v_mfma_f32_16x16x32_bf16 v[184:187], v[84:87], a[200:203], v[184:187]\n"
"ds_read_b128 v[68:71], v1 offset:2048\n"
"v_mfma_f32_16x16x32_bf16 v[20:23], v[152:155], v[116:119], v[20:23]\n"
"v_mfma_f32_16x16x32_bf16 v[184:187], v[88:91], a[208:211], v[184:187]\n"
"ds_read_b128 v[72:75], v1 offset:3072\n"
"buffer_load_dwordx4 a[60:63], v0, s[8:11], s63 offen offset:3072 sc1\n"
"v_mfma_f32_16x16x32_bf16 v[184:187], v[92:95], a[216:219], v[184:187]\n"
"ds_read_b128 v[76:79], v1 offset:4096\n"
"v_mfma_f32_16x16x32_bf16 v[144:147], v[154:157], v[104:107], v[144:147]\n"
"v_mfma_f32_16x16x32_bf16 v[188:191], v[92:95], a[220:223], v[188:191]\n"
"v_mfma_f32_16x16x32_bf16 v[188:191], v[88:91], a[212:215], v[188:191]\n"
"buffer_load_dwordx4 v0, s[8:11], s59 offen offset:1024 sc1 lds\n"
"v_mfma_f32_16x16x32_bf16 v[8:11], v[154:157], v[120:123], v[8:11]\n"
"v_mfma_f32_16x16x32_bf16 v[188:191], v[84:87], a[204:207], v[188:191]\n"
"v_mfma_f32_16x16x32_bf16 v[188:191], v[80:83], a[196:199], v[188:191]\n"
"s_waitcnt lgkmcnt(4)\n"
"v_mfma_f32_16x16x32_bf16 v[16:19], v[154:157], v[124:127], v[16:19]\n"
"v_mfma_f32_16x16x32_bf16 v[192:195], v[48:51], a[192:195], v[192:195]\n"
"ds_read_b128 v[80:83], v1 offset:5120\n"
"buffer_load_dwordx4 v0, s[8:11], s59 offen offset:2048 sc1 lds\n"
"v_mfma_f32_16x16x32_bf16 v[192:195], v[52:55], a[200:203], v[192:195]\n"
"ds_read_b128 v[84:87], v1 offset:6144\n"
"v_mfma_f32_16x16x32_bf16 v[148:151], v[152:155], v[104:107], v[148:151]\n"
"v_mfma_f32_16x16x32_bf16 v[192:195], v[56:59], a[208:211], v[192:195]\n"
"ds_read_b128 v[88:91], v1 offset:7168\n"
"v_mfma_f32_16x16x32_bf16 v[192:195], v[60:63], a[216:219], v[192:195]\n"
"ds_read_b128 v[92:95], v1 offset:8192\n"
"v_mfma_f32_16x16x32_bf16 v[12:15], v[152:155], v[120:123], v[12:15]\n"
"v_mfma_f32_16x16x32_bf16 v[196:199], v[60:63], a[220:223], v[196:199]\n"
"buffer_load_dwordx4 v0, s[8:11], s59 offen offset:3072 sc1 lds\n"
"v_mfma_f32_16x16x32_bf16 v[196:199], v[56:59], a[212:215], v[196:199]\n"
"s_add_u32 m0, s55, 124416\n"
"v_mfma_f32_16x16x32_bf16 v[20:23], v[152:155], v[124:127], v[20:23]\n"
"v_mfma_f32_16x16x32_bf16 v[196:199], v[52:55], a[204:207], v[196:199]\n"
"s_add_u32 s59, s59, 16384\n"
"v_mfma_f32_16x16x32_bf16 v[196:199], v[48:51], a[196:199], v[196:199]\n"
"s_waitcnt lgkmcnt(4)\n"
"buffer_load_dwordx4 v0, s[8:11], s59 offen sc1 lds\n"
"v_mfma_f32_16x16x32_bf16 v[144:147], v[154:157], v[108:111], v[144:147]\n"
"v_mfma_f32_16x16x32_bf16 v[200:203], v[64:67], a[192:195], v[200:203]\n"
"ds_read_b128 v[48:51], v1 offset:9216\n"
"v_mfma_f32_16x16x32_bf16 v[200:203], v[68:71], a[200:203], v[200:203]\n"
"ds_read_b128 v[52:55], v1 offset:10240\n"
"v_mfma_f32_16x16x32_bf16 v[8:11], v[154:157], v[128:131], v[8:11]\n"
"v_mfma_f32_16x16x32_bf16 v[200:203], v[72:75], a[208:211], v[200:203]\n"
"ds_read_b128 v[56:59], v1 offset:11264\n"
"v_mfma_f32_16x16x32_bf16 v[200:203], v[76:79], a[216:219], v[200:203]\n"
"ds_read_b128 v[60:63], v1 offset:12288\n"
"v_mfma_f32_16x16x32_bf16 v[16:19], v[154:157], v[132:135], v[16:19]\n"
"v_mfma_f32_16x16x32_bf16 v[204:207], v[76:79], a[220:223], v[204:207]\n"
"buffer_load_dwordx4 v0, s[8:11], s59 offen offset:1024 sc1 lds\n"
"v_mfma_f32_16x16x32_bf16 v[204:207], v[72:75], a[212:215], v[204:207]\n"
"v_mfma_f32_16x16x32_bf16 v[148:151], v[152:155], v[108:111], v[148:151]\n"
"v_mfma_f32_16x16x32_bf16 v[204:207], v[68:71], a[204:207], v[204:207]\n"
"buffer_load_dwordx4 v0, s[8:11], s59 offen offset:2048 sc1 lds\n"
"v_mfma_f32_16x16x32_bf16 v[204:207], v[64:67], a[196:199], v[204:207]\n"
"s_waitcnt lgkmcnt(4)\n"
"v_mfma_f32_16x16x32_bf16 v[12:15], v[152:155], v[128:131], v[12:15]\n"
"v_mfma_f32_16x16x32_bf16 v[208:211], v[80:83], a[192:195], v[208:211]\n"
"ds_read_b128 v[64:67], v1 offset:13312\n"
"v_mfma_f32_16x16x32_bf16 v[208:211], v[84:87], a[200:203], v[208:211]\n"
"ds_read_b128 v[68:71], v1 offset:14336\n"
"v_mfma_f32_16x16x32_bf16 v[20:23], v[152:155], v[132:135], v[20:23]\n"
"v_mfma_f32_16x16x32_bf16 v[208:211], v[88:91], a[208:211], v[208:211]\n"
"ds_read_b128 v[72:75], v1 offset:15360\n"
"buffer_load_dwordx4 v0, s[8:11], s59 offen offset:3072 sc1 lds\n"
"v_mfma_f32_16x16x32_bf16 v[208:211], v[92:95], a[216:219], v[208:211]\n"
"ds_read_b128 v[76:79], v1 offset:16384\n"
"v_mfma_f32_16x16x32_bf16 v[212:215], v[92:95], a[220:223], v[212:215]\n"
"v_permlane16_swap_b32 v24, v26\n"
"v_mfma_f32_16x16x32_bf16 v[212:215], v[88:91], a[212:215], v[212:215]\n"
"s_add_u32 m0, s55, 0\n"
"v_permlane16_swap_b32 v25, v27\n"
"v_permlane16_swap_b32 v248, v250\n"
"v_mfma_f32_16x16x32_bf16 v[212:215], v[84:87], a[204:207], v[212:215]\n"
"s_add_u32 s59, s59, 16384\n"
"v_permlane16_swap_b32 v249, v251\n"
"v_mul_f32 v96, 0xbfb8aa3b, v8\n"
"v_mfma_f32_16x16x32_bf16 v[212:215], v[80:83], a[196:199], v[212:215]\n"
"s_waitcnt vmcnt(23) lgkmcnt(4)\n"
"v_mul_f32 v97, 0xbfb8aa3b, v9\n"
"v_mul_f32 v98, 0xbfb8aa3b, v10\n"
"v_mfma_f32_16x16x32_bf16 v[160:163], v[48:51], a[0:3], v[160:163]\n"
"ds_read_b128 v[80:83], v1 offset:41984\n"
"buffer_load_dwordx4 v0, s[8:11], s59 offen sc1 lds\n"
"v_mfma_f32_16x16x32_bf16 v[160:163], v[52:55], a[8:11], v[160:163]\n"
"ds_read_b128 v[84:87], v1 offset:43008\n"
"v_mul_f32 v99, 0xbfb8aa3b, v11\n"
"v_mfma_f32_16x16x32_bf16 v[160:163], v[56:59], a[16:19], v[160:163]\n"
"ds_read_b128 v[88:91], v1 offset:44032\n"
"v_mul_f32 v100, 0xbfb8aa3b, v12\n"
"v_mfma_f32_16x16x32_bf16 v[160:163], v[60:63], a[24:27], v[160:163]\n"
"ds_read_b128 v[92:95], v1 offset:45056\n"
"v_mul_f32 v101, 0xbfb8aa3b, v13\n"
"v_mfma_f32_16x16x32_bf16 v[164:167], v[60:63], a[28:31], v[164:167]\n"
"buffer_load_dwordx4 v0, s[8:11], s59 offen offset:1024 sc1 lds\n"
"v_mul_f32 v102, 0xbfb8aa3b, v14\n"
"v_mfma_f32_16x16x32_bf16 v[164:167], v[56:59], a[20:23], v[164:167]\n"
"buffer_load_dwordx4 v0, s[8:11], s59 offen offset:2048 sc1 lds\n"
"v_mul_f32 v103, 0xbfb8aa3b, v15\n"
"v_mfma_f32_16x16x32_bf16 v[164:167], v[52:55], a[12:15], v[164:167]\n"
"v_exp_f32 v96, v96\n"
"v_mfma_f32_16x16x32_bf16 v[164:167], v[48:51], a[4:7], v[164:167]\n"
"s_waitcnt vmcnt(18) lgkmcnt(4)\n"
"v_exp_f32 v97, v97\n"
"v_mfma_f32_16x16x32_bf16 v[160:163], v[64:67], a[128:131], v[160:163]\n"
"ds_read_b128 v[48:51], v1 offset:46080\n"
"buffer_load_dwordx4 v0, s[8:11], s59 offen offset:3072 sc1 lds\n"
"v_mfma_f32_16x16x32_bf16 v[160:163], v[68:71], a[136:139], v[160:163]\n"
"s_add_u32 m0, s55, 16384\n"
"ds_read_b128 v[52:55], v1 offset:47104\n"
"v_mfma_f32_16x16x32_bf16 v[160:163], v[72:75], a[144:147], v[160:163]\n"
"s_add_u32 s59, s59, 16384\n"
"ds_read_b128 v[56:59], v1 offset:48128\n"
"v_mfma_f32_16x16x32_bf16 v[160:163], v[76:79], a[152:155], v[160:163]\n"
"ds_read_b128 v[60:63], v1 offset:49152\n"
"buffer_load_dwordx4 v0, s[8:11], s59 offen sc1 lds\n"
"v_mfma_f32_16x16x32_bf16 v[164:167], v[76:79], a[156:159], v[164:167]\n"
"v_mfma_f32_16x16x32_bf16 v[164:167], v[72:75], a[148:151], v[164:167]\n"
"v_exp_f32 v98, v98\n"
"v_mfma_f32_16x16x32_bf16 v[164:167], v[68:71], a[140:143], v[164:167]\n"
"v_exp_f32 v99, v99\n"
"v_mfma_f32_16x16x32_bf16 v[164:167], v[64:67], a[132:135], v[164:167]\n"
"s_waitcnt vmcnt(12) lgkmcnt(4)\n"
"v_exp_f32 v100, v100\n"
"v_mfma_f32_16x16x32_bf16 v[160:163], v[80:83], a[32:35], v[160:163]\n"
"ds_read_b128 v[64:67], v1 offset:50176\n"
"buffer_load_dwordx4 v0, s[8:11], s59 offen offset:1024 sc1 lds\n"
"v_mfma_f32_16x16x32_bf16 v[160:163], v[84:87], a[40:43], v[160:163]\n"
"ds_read_b128 v[68:71], v1 offset:51200\n"
"v_mfma_f32_16x16x32_bf16 v[160:163], v[88:91], a[48:51], v[160:163]\n"
"ds_read_b128 v[72:75], v1 offset:52224\n"
"buffer_load_dwordx4 v0, s[8:11], s59 offen offset:2048 sc1 lds\n"
"v_mfma_f32_16x16x32_bf16 v[160:163], v[92:95], a[56:59], v[160:163]\n"
"ds_read_b128 v[76:79], v1 offset:53248\n"
"v_mfma_f32_16x16x32_bf16 v[164:167], v[92:95], a[60:63], v[164:167]\n"
"v_mfma_f32_16x16x32_bf16 v[164:167], v[88:91], a[52:55], v[164:167]\n"
"v_exp_f32 v101, v101\n"
"v_mfma_f32_16x16x32_bf16 v[164:167], v[84:87], a[44:47], v[164:167]\n"
"v_cvt_pk_bf16_f32 v160, v160, v161\n"
"v_cvt_pk_bf16_f32 v161, v162, v163\n"
"v_mfma_f32_16x16x32_bf16 v[164:167], v[80:83], a[36:39], v[164:167]\n"
"s_waitcnt lgkmcnt(4)\n"
"v_exp_f32 v102, v102\n"
"v_mfma_f32_16x16x32_bf16 v[168:171], v[48:51], a[0:3], v[168:171]\n"
"ds_read_b128 v[80:83], v1 offset:54272\n"
"ds_read_b128 v[84:87], v1 offset:55296\n"
"v_mfma_f32_16x16x32_bf16 v[168:171], v[52:55], a[8:11], v[168:171]\n"
"ds_read_b128 v[88:91], v1 offset:56320\n"
"ds_read_b128 v[92:95], v1 offset:57344\n"
"v_mfma_f32_16x16x32_bf16 v[168:171], v[56:59], a[16:19], v[168:171]\n"
"v_cvt_pk_bf16_f32 v162, v164, v165\n"
"v_cvt_pk_bf16_f32 v163, v166, v167\n"
"v_mfma_f32_16x16x32_bf16 v[168:171], v[60:63], a[24:27], v[168:171]\n"
"buffer_store_dwordx4 v[160:163], v0, s[8:11], s89 offen offset:0 sc1\n"
"v_mfma_f32_16x16x32_bf16 v[172:175], v[60:63], a[28:31], v[172:175]\n"
"v_mfma_f32_16x16x32_bf16 v[172:175], v[56:59], a[20:23], v[172:175]\n"
"v_exp_f32 v103, v103\n"
"v_mfma_f32_16x16x32_bf16 v[172:175], v[52:55], a[12:15], v[172:175]\n"
"v_add_f32 v96, 1.0, v96\n"
"v_add_f32 v97, 1.0, v97\n"
"v_mfma_f32_16x16x32_bf16 v[172:175], v[48:51], a[4:7], v[172:175]\n"
"s_waitcnt vmcnt(8) lgkmcnt(0)\n"
"v_add_f32 v98, 1.0, v98\n"
"v_add_f32 v99, 1.0, v99\n"
"v_mfma_f32_16x16x32_bf16 v[168:171], v[64:67], a[128:131], v[168:171]\n"
"s_barrier\n"
"ds_read_b128 v[48:51], v1 offset:25088\n"
"buffer_load_dwordx4 v0, s[8:11], s59 offen offset:3072 sc1 lds\n"
"v_mfma_f32_16x16x32_bf16 v[168:171], v[68:71], a[136:139], v[168:171]\n"
"ds_read_b128 v[52:55], v1 offset:26112\n"
"v_add_f32 v100, 1.0, v100\n"
"v_mfma_f32_16x16x32_bf16 v[168:171], v[72:75], a[144:147], v[168:171]\n"
"s_add_u32 m0, s55, 33792\n"
"ds_read_b128 v[56:59], v1 offset:27136\n"
"v_add_f32 v101, 1.0, v101\n"
"v_mfma_f32_16x16x32_bf16 v[168:171], v[76:79], a[152:155], v[168:171]\n"
"s_add_u32 s59, s59, 16384\n"
"ds_read_b128 v[60:63], v1 offset:28160\n"
"v_add_f32 v102, 1.0, v102\n"
"v_mfma_f32_16x16x32_bf16 v[172:175], v[76:79], a[156:159], v[172:175]\n"
"buffer_load_dwordx4 v0, s[8:11], s59 offen sc1 lds\n"
"v_add_f32 v103, 1.0, v103\n"
"v_mfma_f32_16x16x32_bf16 v[172:175], v[72:75], a[148:151], v[172:175]\n"
"v_rcp_f32 v96, v96\n"
"v_mfma_f32_16x16x32_bf16 v[172:175], v[68:71], a[140:143], v[172:175]\n"
"v_rcp_f32 v97, v97\n"
"v_mfma_f32_16x16x32_bf16 v[172:175], v[64:67], a[132:135], v[172:175]\n"
"s_waitcnt lgkmcnt(4)\n"
"v_rcp_f32 v98, v98\n"
"v_mfma_f32_16x16x32_bf16 v[168:171], v[80:83], a[32:35], v[168:171]\n"
"ds_read_b128 v[64:67], v1 offset:29184\n"
"buffer_load_dwordx4 v0, s[8:11], s59 offen offset:1024 sc1 lds\n"
"v_mfma_f32_16x16x32_bf16 v[168:171], v[84:87], a[40:43], v[168:171]\n"
"ds_read_b128 v[68:71], v1 offset:30208\n"
"buffer_load_dwordx4 v0, s[8:11], s59 offen offset:2048 sc1 lds\n"
"v_mfma_f32_16x16x32_bf16 v[168:171], v[88:91], a[48:51], v[168:171]\n"
"ds_read_b128 v[72:75], v1 offset:31232\n"
"v_mfma_f32_16x16x32_bf16 v[168:171], v[92:95], a[56:59], v[168:171]\n"
"ds_read_b128 v[76:79], v1 offset:32256\n"
"buffer_load_dwordx4 v0, s[8:11], s59 offen offset:3072 sc1 lds\n"
"v_mfma_f32_16x16x32_bf16 v[172:175], v[92:95], a[60:63], v[172:175]\n"
"v_mfma_f32_16x16x32_bf16 v[172:175], v[88:91], a[52:55], v[172:175]\n"
"s_add_u32 m0, s55, 50176\n"
"v_rcp_f32 v99, v99\n"
"v_mfma_f32_16x16x32_bf16 v[172:175], v[84:87], a[44:47], v[172:175]\n"
"s_add_u32 s59, s59, 16384\n"
"v_cvt_pk_bf16_f32 v168, v168, v169\n"
"v_cvt_pk_bf16_f32 v169, v170, v171\n"
"v_mfma_f32_16x16x32_bf16 v[172:175], v[80:83], a[36:39], v[172:175]\n"
"s_waitcnt lgkmcnt(4)\n"
"v_rcp_f32 v100, v100\n"
"v_mfma_f32_16x16x32_bf16 v[176:179], v[48:51], a[0:3], v[176:179]\n"
"ds_read_b128 v[80:83], v1 offset:33280\n"
"buffer_load_dwordx4 v0, s[8:11], s59 offen sc1 lds\n"
"v_mfma_f32_16x16x32_bf16 v[176:179], v[52:55], a[8:11], v[176:179]\n"
"ds_read_b128 v[84:87], v1 offset:34304\n"
"v_mfma_f32_16x16x32_bf16 v[176:179], v[56:59], a[16:19], v[176:179]\n"
"ds_read_b128 v[88:91], v1 offset:35328\n"
"v_cvt_pk_bf16_f32 v170, v172, v173\n"
"v_mfma_f32_16x16x32_bf16 v[176:179], v[60:63], a[24:27], v[176:179]\n"
"ds_read_b128 v[92:95], v1 offset:36352\n"
"v_cvt_pk_bf16_f32 v171, v174, v175\n"
"v_mfma_f32_16x16x32_bf16 v[180:183], v[60:63], a[28:31], v[180:183]\n"
"buffer_store_dwordx4 v[168:171], v0, s[8:11], s86 offen offset:0 sc1\n"
"v_mfma_f32_16x16x32_bf16 v[180:183], v[56:59], a[20:23], v[180:183]\n"
"v_rcp_f32 v101, v101\n"
"v_mfma_f32_16x16x32_bf16 v[180:183], v[52:55], a[12:15], v[180:183]\n"
"v_rcp_f32 v102, v102\n"
"v_mfma_f32_16x16x32_bf16 v[180:183], v[48:51], a[4:7], v[180:183]\n"
"s_waitcnt lgkmcnt(4)\n"
"v_rcp_f32 v103, v103\n"
"v_mfma_f32_16x16x32_bf16 v[176:179], v[64:67], a[128:131], v[176:179]\n"
"ds_read_b128 v[48:51], v1 offset:37376\n"
"v_mfma_f32_16x16x32_bf16 v[176:179], v[68:71], a[136:139], v[176:179]\n"
"ds_read_b128 v[52:55], v1 offset:38400\n"
"v_mul_f32 v8, v8, v96\n"
"v_mfma_f32_16x16x32_bf16 v[176:179], v[72:75], a[144:147], v[176:179]\n"
"ds_read_b128 v[56:59], v1 offset:39424\n"
"v_mul_f32 v9, v9, v97\n"
"v_mfma_f32_16x16x32_bf16 v[176:179], v[76:79], a[152:155], v[176:179]\n"
"ds_read_b128 v[60:63], v1 offset:40448\n"
"v_mul_f32 v10, v10, v98\n"
"v_mfma_f32_16x16x32_bf16 v[180:183], v[76:79], a[156:159], v[180:183]\n"
"buffer_load_dwordx4 v0, s[8:11], s59 offen offset:1024 sc1 lds\n"
"v_mul_f32 v11, v11, v99\n"
"v_mfma_f32_16x16x32_bf16 v[180:183], v[72:75], a[148:151], v[180:183]\n"
"v_mul_f32 v12, v12, v100\n"
"v_mul_f32 v13, v13, v101\n"
"v_mfma_f32_16x16x32_bf16 v[180:183], v[68:71], a[140:143], v[180:183]\n"
"v_mul_f32 v14, v14, v102\n"
"v_mul_f32 v15, v15, v103\n"
"v_mfma_f32_16x16x32_bf16 v[180:183], v[64:67], a[132:135], v[180:183]\n"
"s_waitcnt lgkmcnt(4)\n"
"v_mul_f32 v104, 0xbfb8aa3b, v16\n"
"v_mul_f32 v105, 0xbfb8aa3b, v17\n"
"v_mfma_f32_16x16x32_bf16 v[176:179], v[80:83], a[32:35], v[176:179]\n"
"ds_read_b128 v[64:67], v1 offset:58880\n"
"buffer_load_dwordx4 v0, s[8:11], s59 offen offset:2048 sc1 lds\n"
"v_mfma_f32_16x16x32_bf16 v[176:179], v[84:87], a[40:43], v[176:179]\n"
"ds_read_b128 v[68:71], v1 offset:59904\n"
"v_mul_f32 v106, 0xbfb8aa3b, v18\n"
"v_mfma_f32_16x16x32_bf16 v[176:179], v[88:91], a[48:51], v[176:179]\n"
"ds_read_b128 v[72:75], v1 offset:60928\n"
"v_mul_f32 v107, 0xbfb8aa3b, v19\n"
"v_mfma_f32_16x16x32_bf16 v[176:179], v[92:95], a[56:59], v[176:179]\n"
"ds_read_b128 v[76:79], v1 offset:61952\n"
"v_mul_f32 v108, 0xbfb8aa3b, v20\n"
"v_mfma_f32_16x16x32_bf16 v[180:183], v[92:95], a[60:63], v[180:183]\n"
"buffer_load_dwordx4 v0, s[8:11], s59 offen offset:3072 sc1 lds\n"
"v_mul_f32 v109, 0xbfb8aa3b, v21\n"
"v_mfma_f32_16x16x32_bf16 v[180:183], v[88:91], a[52:55], v[180:183]\n"
"v_mul_f32 v110, 0xbfb8aa3b, v22\n"
"v_mul_f32 v111, 0xbfb8aa3b, v23\n"
"v_mfma_f32_16x16x32_bf16 v[180:183], v[84:87], a[44:47], v[180:183]\n"
"s_sub_u32 s59, s59, s55\n"
"v_cvt_pk_bf16_f32 v176, v176, v177\n"
"v_cvt_pk_bf16_f32 v177, v178, v179\n"
"v_mfma_f32_16x16x32_bf16 v[180:183], v[80:83], a[36:39], v[180:183]\n"
"s_waitcnt lgkmcnt(4)\n"
"v_exp_f32 v104, v104\n"
"v_mfma_f32_16x16x32_bf16 v[184:187], v[48:51], a[0:3], v[184:187]\n"
"s_lshr_b32 s55, s55, 2\n"
"ds_read_b128 v[80:83], v1 offset:62976\n"
"v_mfma_f32_16x16x32_bf16 v[184:187], v[52:55], a[8:11], v[184:187]\n"
"s_add_u32 s59, s59, s55\n"
"ds_read_b128 v[84:87], v1 offset:64000\n"
"v_mfma_f32_16x16x32_bf16 v[184:187], v[56:59], a[16:19], v[184:187]\n"
"s_add_u32 m0, s55, 141312\n"
"ds_read_b128 v[88:91], v1 offset:65024\n"
"v_cvt_pk_bf16_f32 v178, v180, v181\n"
"v_mfma_f32_16x16x32_bf16 v[184:187], v[60:63], a[24:27], v[184:187]\n"
"s_add_u32 s59, s59, 16384\n"
"ds_read_b128 v[92:95], v2 offset:512\n"
"v_cvt_pk_bf16_f32 v179, v182, v183\n"
"v_mfma_f32_16x16x32_bf16 v[188:191], v[60:63], a[28:31], v[188:191]\n"
"buffer_store_dwordx4 v[176:179], v0, s[8:11], s86 offen offset:1024 sc1\n"
"buffer_load_dwordx4 v0, s[8:11], s59 offen sc1 lds\n"
"v_mfma_f32_16x16x32_bf16 v[188:191], v[56:59], a[20:23], v[188:191]\n"
"v_exp_f32 v105, v105\n"
"v_mfma_f32_16x16x32_bf16 v[188:191], v[52:55], a[12:15], v[188:191]\n"
"v_exp_f32 v106, v106\n"
"v_mfma_f32_16x16x32_bf16 v[188:191], v[48:51], a[4:7], v[188:191]\n"
"s_waitcnt lgkmcnt(4)\n"
"v_exp_f32 v107, v107\n"
"v_mfma_f32_16x16x32_bf16 v[184:187], v[64:67], a[128:131], v[184:187]\n"
"ds_read_b128 v[48:51], v2 offset:1536\n"
"v_mfma_f32_16x16x32_bf16 v[184:187], v[68:71], a[136:139], v[184:187]\n"
"ds_read_b128 v[52:55], v2 offset:2560\n"
"v_mfma_f32_16x16x32_bf16 v[184:187], v[72:75], a[144:147], v[184:187]\n"
"ds_read_b128 v[56:59], v2 offset:3584\n"
"v_mfma_f32_16x16x32_bf16 v[184:187], v[76:79], a[152:155], v[184:187]\n"
"ds_read_b128 v[60:63], v2 offset:4608\n"
"v_mfma_f32_16x16x32_bf16 v[188:191], v[76:79], a[156:159], v[188:191]\n"
"v_mfma_f32_16x16x32_bf16 v[188:191], v[72:75], a[148:151], v[188:191]\n"
"v_exp_f32 v108, v108\n"
"v_mfma_f32_16x16x32_bf16 v[188:191], v[68:71], a[140:143], v[188:191]\n"
"v_exp_f32 v109, v109\n"
"v_mfma_f32_16x16x32_bf16 v[188:191], v[64:67], a[132:135], v[188:191]\n"
"s_waitcnt lgkmcnt(4)\n"
"v_exp_f32 v110, v110\n"
"v_mfma_f32_16x16x32_bf16 v[184:187], v[80:83], a[32:35], v[184:187]\n"
"ds_read_b128 v[64:67], v2 offset:5632\n"
"ds_read_b128 v[68:71], v2 offset:6656\n"
"v_mfma_f32_16x16x32_bf16 v[184:187], v[84:87], a[40:43], v[184:187]\n"
"ds_read_b128 v[72:75], v2 offset:7680\n"
"ds_read_b128 v[76:79], v2 offset:8704\n"
"v_mfma_f32_16x16x32_bf16 v[184:187], v[88:91], a[48:51], v[184:187]\n"
"v_mfma_f32_16x16x32_bf16 v[184:187], v[92:95], a[56:59], v[184:187]\n"
"v_exp_f32 v111, v111\n"
"v_mfma_f32_16x16x32_bf16 v[188:191], v[92:95], a[60:63], v[188:191]\n"
"buffer_load_dwordx4 v[232:235], v0, s[16:19], s81 offen offset:0 sc1\n"
"v_add_f32 v104, 1.0, v104\n"
"v_mfma_f32_16x16x32_bf16 v[188:191], v[88:91], a[52:55], v[188:191]\n"
"v_add_f32 v105, 1.0, v105\n"
"v_add_f32 v106, 1.0, v106\n"
"v_mfma_f32_16x16x32_bf16 v[188:191], v[84:87], a[44:47], v[188:191]\n"
"v_cvt_pk_bf16_f32 v184, v184, v185\n"
"v_cvt_pk_bf16_f32 v185, v186, v187\n"
"v_mfma_f32_16x16x32_bf16 v[188:191], v[80:83], a[36:39], v[188:191]\n"
"s_waitcnt vmcnt(12) lgkmcnt(0)\n"
"v_add_f32 v107, 1.0, v107\n"
"v_add_f32 v108, 1.0, v108\n"
"v_mfma_f32_16x16x32_bf16 v[192:195], v[48:51], a[0:3], v[192:195]\n"
"s_barrier\n"
"ds_read_b128 v[80:83], v0 offset:0\n"
"buffer_load_dwordx4 v[236:239], v0, s[16:19], s81 offen offset:1024 sc1\n"
"v_mfma_f32_16x16x32_bf16 v[192:195], v[52:55], a[8:11], v[192:195]\n"
"ds_read_b128 v[84:87], v0 offset:1024\n"
"v_add_f32 v109, 1.0, v109\n"
"v_mfma_f32_16x16x32_bf16 v[192:195], v[56:59], a[16:19], v[192:195]\n"
"ds_read_b128 v[88:91], v0 offset:2048\n"
"v_cvt_pk_bf16_f32 v186, v188, v189\n"
"v_mfma_f32_16x16x32_bf16 v[192:195], v[60:63], a[24:27], v[192:195]\n"
"ds_read_b128 v[92:95], v0 offset:3072\n"
"v_cvt_pk_bf16_f32 v187, v190, v191\n"
"v_mfma_f32_16x16x32_bf16 v[196:199], v[60:63], a[28:31], v[196:199]\n"
"buffer_store_dwordx4 v[184:187], v0, s[8:11], s87 offen offset:0 sc1\n"
"v_add_f32 v110, 1.0, v110\n"
"v_mfma_f32_16x16x32_bf16 v[196:199], v[56:59], a[20:23], v[196:199]\n"
"buffer_load_dwordx4 v[240:243], v0, s[16:19], s81 offen offset:2048 sc1\n"
"v_add_f32 v111, 1.0, v111\n"
"v_mfma_f32_16x16x32_bf16 v[196:199], v[52:55], a[12:15], v[196:199]\n"
"v_rcp_f32 v104, v104\n"
"v_mfma_f32_16x16x32_bf16 v[196:199], v[48:51], a[4:7], v[196:199]\n"
"s_waitcnt lgkmcnt(4)\n"
"v_rcp_f32 v105, v105\n"
"v_mfma_f32_16x16x32_bf16 v[192:195], v[64:67], a[128:131], v[192:195]\n"
"ds_read_b128 v[48:51], v0 offset:4096\n"
"buffer_load_dwordx4 v[244:247], v0, s[16:19], s81 offen offset:3072 sc1\n"
"v_mfma_f32_16x16x32_bf16 v[192:195], v[68:71], a[136:139], v[192:195]\n"
"ds_read_b128 v[52:55], v0 offset:5120\n"
"v_mfma_f32_16x16x32_bf16 v[192:195], v[72:75], a[144:147], v[192:195]\n"
"ds_read_b128 v[56:59], v0 offset:6144\n"
"v_mfma_f32_16x16x32_bf16 v[192:195], v[76:79], a[152:155], v[192:195]\n"
"ds_read_b128 v[60:63], v0 offset:7168\n"
"v_mfma_f32_16x16x32_bf16 v[196:199], v[76:79], a[156:159], v[196:199]\n"
"v_rcp_f32 v106, v106\n"
"v_mfma_f32_16x16x32_bf16 v[196:199], v[72:75], a[148:151], v[196:199]\n"
"v_rcp_f32 v107, v107\n"
"v_mfma_f32_16x16x32_bf16 v[196:199], v[68:71], a[140:143], v[196:199]\n"
"v_rcp_f32 v108, v108\n"
"v_mfma_f32_16x16x32_bf16 v[196:199], v[64:67], a[132:135], v[196:199]\n"
"s_waitcnt lgkmcnt(4)\n"
"v_rcp_f32 v109, v109\n"
"v_mfma_f32_16x16x32_bf16 v[192:195], v[80:83], a[32:35], v[192:195]\n"
"ds_read_b128 v[64:67], v0 offset:8192\n"
"v_mfma_f32_16x16x32_bf16 v[192:195], v[84:87], a[40:43], v[192:195]\n"
"ds_read_b128 v[68:71], v0 offset:9216\n"
"v_mfma_f32_16x16x32_bf16 v[192:195], v[88:91], a[48:51], v[192:195]\n"
"ds_read_b128 v[72:75], v0 offset:10240\n"
"v_mfma_f32_16x16x32_bf16 v[192:195], v[92:95], a[56:59], v[192:195]\n"
"ds_read_b128 v[76:79], v0 offset:11264\n"
"v_mfma_f32_16x16x32_bf16 v[196:199], v[92:95], a[60:63], v[196:199]\n"
"v_rcp_f32 v110, v110\n"
"v_mfma_f32_16x16x32_bf16 v[196:199], v[88:91], a[52:55], v[196:199]\n"
"v_rcp_f32 v111, v111\n"
"v_mfma_f32_16x16x32_bf16 v[196:199], v[84:87], a[44:47], v[196:199]\n"
"v_cvt_pk_bf16_f32 v192, v192, v193\n"
"v_cvt_pk_bf16_f32 v193, v194, v195\n"
"v_mfma_f32_16x16x32_bf16 v[196:199], v[80:83], a[36:39], v[196:199]\n"
"s_waitcnt lgkmcnt(4)\n"
"v_mul_f32 v16, v16, v104\n"
"v_mul_f32 v17, v17, v105\n"
"v_mfma_f32_16x16x32_bf16 v[200:203], v[48:51], a[0:3], v[200:203]\n"
"ds_read_b128 v[80:83], v0 offset:12288\n"
"v_mul_f32 v18, v18, v106\n"
"v_mfma_f32_16x16x32_bf16 v[200:203], v[52:55], a[8:11], v[200:203]\n"
"ds_read_b128 v[84:87], v0 offset:13312\n"
"v_mul_f32 v19, v19, v107\n"
"v_mfma_f32_16x16x32_bf16 v[200:203], v[56:59], a[16:19], v[200:203]\n"
"ds_read_b128 v[88:91], v0 offset:14336\n"
"v_cvt_pk_bf16_f32 v194, v196, v197\n"
"v_mfma_f32_16x16x32_bf16 v[200:203], v[60:63], a[24:27], v[200:203]\n"
"ds_read_b128 v[92:95], v0 offset:15360\n"
"v_cvt_pk_bf16_f32 v195, v198, v199\n"
"v_mfma_f32_16x16x32_bf16 v[204:207], v[60:63], a[28:31], v[204:207]\n"
"buffer_store_dwordx4 v[192:195], v0, s[8:11], s87 offen offset:1024 sc1\n"
"v_mul_f32 v20, v20, v108\n"
"v_mfma_f32_16x16x32_bf16 v[204:207], v[56:59], a[20:23], v[204:207]\n"
"v_mul_f32 v21, v21, v109\n"
"v_mul_f32 v22, v22, v110\n"
"v_mfma_f32_16x16x32_bf16 v[204:207], v[52:55], a[12:15], v[204:207]\n"
"v_mul_f32 v23, v23, v111\n"
"v_mul_f32 v96, 0xbfb8aa3b, v144\n"
"v_mfma_f32_16x16x32_bf16 v[204:207], v[48:51], a[4:7], v[204:207]\n"
"s_waitcnt lgkmcnt(4)\n"
"v_mul_f32 v97, 0xbfb8aa3b, v145\n"
"v_mul_f32 v98, 0xbfb8aa3b, v146\n"
"v_mfma_f32_16x16x32_bf16 v[200:203], v[64:67], a[128:131], v[200:203]\n"
"ds_read_b128 v[48:51], v0 offset:16384\n"
"v_mul_f32 v99, 0xbfb8aa3b, v147\n"
"v_mfma_f32_16x16x32_bf16 v[200:203], v[68:71], a[136:139], v[200:203]\n"
"ds_read_b128 v[52:55], v0 offset:17408\n"
"v_mul_f32 v100, 0xbfb8aa3b, v148\n"
"v_mfma_f32_16x16x32_bf16 v[200:203], v[72:75], a[144:147], v[200:203]\n"
"ds_read_b128 v[56:59], v0 offset:18432\n"
"v_mul_f32 v101, 0xbfb8aa3b, v149\n"
"v_mfma_f32_16x16x32_bf16 v[200:203], v[76:79], a[152:155], v[200:203]\n"
"ds_read_b128 v[60:63], v0 offset:19456\n"
"v_mul_f32 v102, 0xbfb8aa3b, v150\n"
"v_mfma_f32_16x16x32_bf16 v[204:207], v[76:79], a[156:159], v[204:207]\n"
"v_mul_f32 v103, 0xbfb8aa3b, v151\n"
"v_mfma_f32_16x16x32_bf16 v[204:207], v[72:75], a[148:151], v[204:207]\n"
"v_exp_f32 v96, v96\n"
"v_mfma_f32_16x16x32_bf16 v[204:207], v[68:71], a[140:143], v[204:207]\n"
"v_exp_f32 v97, v97\n"
"v_mfma_f32_16x16x32_bf16 v[204:207], v[64:67], a[132:135], v[204:207]\n"
"s_waitcnt lgkmcnt(4)\n"
"v_exp_f32 v98, v98\n"
"v_mfma_f32_16x16x32_bf16 v[200:203], v[80:83], a[32:35], v[200:203]\n"
"ds_read_b128 v[64:67], v0 offset:20480\n"
"v_mfma_f32_16x16x32_bf16 v[200:203], v[84:87], a[40:43], v[200:203]\n"
"ds_read_b128 v[68:71], v0 offset:21504\n"
"v_mfma_f32_16x16x32_bf16 v[200:203], v[88:91], a[48:51], v[200:203]\n"
"ds_read_b128 v[72:75], v0 offset:22528\n"
"v_mfma_f32_16x16x32_bf16 v[200:203], v[92:95], a[56:59], v[200:203]\n"
"ds_read_b128 v[76:79], v0 offset:23552\n"
"v_mfma_f32_16x16x32_bf16 v[204:207], v[92:95], a[60:63], v[204:207]\n"
"v_exp_f32 v99, v99\n"
"v_mfma_f32_16x16x32_bf16 v[204:207], v[88:91], a[52:55], v[204:207]\n"
"v_exp_f32 v100, v100\n"
"v_mfma_f32_16x16x32_bf16 v[204:207], v[84:87], a[44:47], v[204:207]\n"
"v_cvt_pk_bf16_f32 v200, v200, v201\n"
"v_cvt_pk_bf16_f32 v201, v202, v203\n"
"v_mfma_f32_16x16x32_bf16 v[204:207], v[80:83], a[36:39], v[204:207]\n"
"s_waitcnt lgkmcnt(4)\n"
"v_exp_f32 v101, v101\n"
"v_mfma_f32_16x16x32_bf16 v[208:211], v[48:51], a[0:3], v[208:211]\n"
"ds_read_b128 v[80:83], v0 offset:24576\n"
"buffer_load_dwordx4 v[32:35], v0, s[16:19], s60 offen offset:2048 sc1\n"
"v_mfma_f32_16x16x32_bf16 v[208:211], v[52:55], a[8:11], v[208:211]\n"
"ds_read_b128 v[84:87], v0 offset:25600\n"
"v_mfma_f32_16x16x32_bf16 v[208:211], v[56:59], a[16:19], v[208:211]\n"
"ds_read_b128 v[88:91], v0 offset:26624\n"
"v_cvt_pk_bf16_f32 v202, v204, v205\n"
"v_mfma_f32_16x16x32_bf16 v[208:211], v[60:63], a[24:27], v[208:211]\n"
"ds_read_b128 v[92:95], v0 offset:27648\n"
"v_cvt_pk_bf16_f32 v203, v206, v207\n"
"v_mfma_f32_16x16x32_bf16 v[212:215], v[60:63], a[28:31], v[212:215]\n"
"buffer_store_dwordx4 v[200:203], v0, s[8:11], s88 offen offset:0 sc1\n"
"buffer_load_dwordx4 v[36:39], v0, s[16:19], s60 offen offset:3072 sc1\n"
"v_mfma_f32_16x16x32_bf16 v[212:215], v[56:59], a[20:23], v[212:215]\n"
"v_exp_f32 v102, v102\n"
"v_mfma_f32_16x16x32_bf16 v[212:215], v[52:55], a[12:15], v[212:215]\n"
"v_exp_f32 v103, v103\n"
"v_mfma_f32_16x16x32_bf16 v[212:215], v[48:51], a[4:7], v[212:215]\n"
"s_waitcnt lgkmcnt(4)\n"
"v_add_f32 v96, 1.0, v96\n"
"v_add_f32 v97, 1.0, v97\n"
"v_mfma_f32_16x16x32_bf16 v[208:211], v[64:67], a[128:131], v[208:211]\n"
"ds_read_b128 v[48:51], v0 offset:28672\n"
"ds_read_b128 v[52:55], v0 offset:29696\n"
"v_mfma_f32_16x16x32_bf16 v[208:211], v[68:71], a[136:139], v[208:211]\n"
"ds_read_b128 v[56:59], v0 offset:30720\n"
"ds_read_b128 v[60:63], v0 offset:31744\n"
"v_mfma_f32_16x16x32_bf16 v[208:211], v[72:75], a[144:147], v[208:211]\n"
"v_add_f32 v98, 1.0, v98\n"
"v_add_f32 v99, 1.0, v99\n"
"v_mfma_f32_16x16x32_bf16 v[208:211], v[76:79], a[152:155], v[208:211]\n"
"v_add_f32 v100, 1.0, v100\n"
"v_add_f32 v101, 1.0, v101\n"
"v_mfma_f32_16x16x32_bf16 v[212:215], v[76:79], a[156:159], v[212:215]\n"
"v_add_f32 v102, 1.0, v102\n"
"v_add_f32 v103, 1.0, v103\n"
"v_mfma_f32_16x16x32_bf16 v[212:215], v[72:75], a[148:151], v[212:215]\n"
"v_rcp_f32 v96, v96\n"
"v_mfma_f32_16x16x32_bf16 v[212:215], v[68:71], a[140:143], v[212:215]\n"
"v_rcp_f32 v97, v97\n"
"v_mfma_f32_16x16x32_bf16 v[212:215], v[64:67], a[132:135], v[212:215]\n"
"s_waitcnt vmcnt(11) lgkmcnt(0)\n"
"v_rcp_f32 v98, v98\n"
"v_mfma_f32_16x16x32_bf16 v[208:211], v[80:83], a[32:35], v[208:211]\n"
"s_barrier\n"
"ds_read_b128 v[64:67], v0 offset:33792\n"
"v_mfma_f32_16x16x32_bf16 v[208:211], v[84:87], a[40:43], v[208:211]\n"
"ds_read_b128 v[68:71], v0 offset:34816\n"
"v_mfma_f32_16x16x32_bf16 v[208:211], v[88:91], a[48:51], v[208:211]\n"
"ds_read_b128 v[72:75], v0 offset:35840\n"
"v_mfma_f32_16x16x32_bf16 v[208:211], v[92:95], a[56:59], v[208:211]\n"
"ds_read_b128 v[76:79], v0 offset:36864\n"
"v_mfma_f32_16x16x32_bf16 v[212:215], v[92:95], a[60:63], v[212:215]\n"
"v_rcp_f32 v99, v99\n"
"v_mfma_f32_16x16x32_bf16 v[212:215], v[88:91], a[52:55], v[212:215]\n"
"v_rcp_f32 v100, v100\n"
"v_mfma_f32_16x16x32_bf16 v[212:215], v[84:87], a[44:47], v[212:215]\n"
"v_cvt_pk_bf16_f32 v208, v208, v209\n"
"v_cvt_pk_bf16_f32 v209, v210, v211\n"
"v_mfma_f32_16x16x32_bf16 v[212:215], v[80:83], a[36:39], v[212:215]\n"
"s_waitcnt lgkmcnt(4)\n"
"v_rcp_f32 v101, v101\n"
"v_mfma_f32_16x16x32_bf16 v[216:219], v[48:51], a[224:227], v[216:219]\n"
"ds_read_b128 v[80:83], v0 offset:37888\n"
"v_mfma_f32_16x16x32_bf16 v[216:219], v[52:55], a[232:235], v[216:219]\n"
"ds_read_b128 v[84:87], v0 offset:38912\n"
"v_mfma_f32_16x16x32_bf16 v[216:219], v[56:59], a[240:243], v[216:219]\n"
"ds_read_b128 v[88:91], v0 offset:39936\n"
"v_cvt_pk_bf16_f32 v210, v212, v213\n"
"v_mfma_f32_16x16x32_bf16 v[216:219], v[60:63], a[248:251], v[216:219]\n"
"ds_read_b128 v[92:95], v0 offset:40960\n"
"v_cvt_pk_bf16_f32 v211, v214, v215\n"
"v_mfma_f32_16x16x32_bf16 v[220:223], v[60:63], a[252:255], v[220:223]\n"
"buffer_store_dwordx4 v[208:211], v0, s[8:11], s88 offen offset:1024 sc1\n"
"v_mfma_f32_16x16x32_bf16 v[220:223], v[56:59], a[244:247], v[220:223]\n"
"v_rcp_f32 v102, v102\n"
"v_mfma_f32_16x16x32_bf16 v[220:223], v[52:55], a[236:239], v[220:223]\n"
"v_rcp_f32 v103, v103\n"
"v_mfma_f32_16x16x32_bf16 v[220:223], v[48:51], a[228:231], v[220:223]\n"
"s_waitcnt lgkmcnt(4)\n"
"v_mul_f32 v144, v144, v96\n"
"v_mul_f32 v145, v145, v97\n"
"v_mfma_f32_16x16x32_bf16 v[216:219], v[64:67], a[192:195], v[216:219]\n"
"ds_read_b128 v[48:51], v0 offset:41984\n"
"v_mul_f32 v146, v146, v98\n"
"v_mfma_f32_16x16x32_bf16 v[216:219], v[68:71], a[200:203], v[216:219]\n"
"ds_read_b128 v[52:55], v0 offset:43008\n"
"v_mul_f32 v147, v147, v99\n"
"v_mfma_f32_16x16x32_bf16 v[216:219], v[72:75], a[208:211], v[216:219]\n"
"ds_read_b128 v[56:59], v0 offset:44032\n"
"v_mul_f32 v148, v148, v100\n"
"v_mfma_f32_16x16x32_bf16 v[216:219], v[76:79], a[216:219], v[216:219]\n"
"ds_read_b128 v[60:63], v0 offset:45056\n"
"v_mul_f32 v149, v149, v101\n"
"v_mfma_f32_16x16x32_bf16 v[220:223], v[76:79], a[220:223], v[220:223]\n"
"v_mul_f32 v150, v150, v102\n"
"v_mul_f32 v151, v151, v103\n"
"v_mfma_f32_16x16x32_bf16 v[220:223], v[72:75], a[212:215], v[220:223]\n"
"v_lshlrev_b32 v112, 16, v24\n"
"v_and_b32 v113, 0xffff0000, v24\n"
"v_mfma_f32_16x16x32_bf16 v[220:223], v[68:71], a[204:207], v[220:223]\n"
"v_lshlrev_b32 v114, 16, v25\n"
"v_and_b32 v115, 0xffff0000, v25\n"
"v_mfma_f32_16x16x32_bf16 v[220:223], v[64:67], a[196:199], v[220:223]\n"
"s_waitcnt lgkmcnt(4)\n"
"v_lshlrev_b32 v116, 16, v248\n"
"v_and_b32 v117, 0xffff0000, v248\n"
"v_mfma_f32_16x16x32_bf16 v[216:219], v[80:83], a[0:3], v[216:219]\n"
"ds_read_b128 v[64:67], v0 offset:46080\n"
"v_lshlrev_b32 v118, 16, v249\n"
"v_mfma_f32_16x16x32_bf16 v[216:219], v[84:87], a[8:11], v[216:219]\n"
"ds_read_b128 v[68:71], v0 offset:47104\n"
"v_and_b32 v119, 0xffff0000, v249\n"
"v_mfma_f32_16x16x32_bf16 v[216:219], v[88:91], a[16:19], v[216:219]\n"
"ds_read_b128 v[72:75], v0 offset:48128\n"
"v_lshlrev_b32 v120, 16, v26\n"
"v_mfma_f32_16x16x32_bf16 v[216:219], v[92:95], a[24:27], v[216:219]\n"
"ds_read_b128 v[76:79], v0 offset:49152\n"
"v_and_b32 v121, 0xffff0000, v26\n"
"v_mfma_f32_16x16x32_bf16 v[220:223], v[92:95], a[28:31], v[220:223]\n"
"v_lshlrev_b32 v122, 16, v27\n"
"v_and_b32 v123, 0xffff0000, v27\n"
"v_mfma_f32_16x16x32_bf16 v[220:223], v[88:91], a[20:23], v[220:223]\n"
"v_lshlrev_b32 v124, 16, v250\n"
"v_and_b32 v125, 0xffff0000, v250\n"
"v_mfma_f32_16x16x32_bf16 v[220:223], v[84:87], a[12:15], v[220:223]\n"
"v_lshlrev_b32 v126, 16, v251\n"
"v_and_b32 v127, 0xffff0000, v251\n"
"v_mfma_f32_16x16x32_bf16 v[220:223], v[80:83], a[4:7], v[220:223]\n"
"s_waitcnt lgkmcnt(4)\n"
"v_lshlrev_b32 v128, 16, v28\n"
"v_and_b32 v129, 0xffff0000, v28\n"
"v_mfma_f32_16x16x32_bf16 v[216:219], v[48:51], a[128:131], v[216:219]\n"
"ds_read_b128 v[80:83], v0 offset:50176\n"
"v_lshlrev_b32 v130, 16, v29\n"
"v_mfma_f32_16x16x32_bf16 v[216:219], v[52:55], a[136:139], v[216:219]\n"
"ds_read_b128 v[84:87], v0 offset:51200\n"
"v_and_b32 v131, 0xffff0000, v29\n"
"v_mfma_f32_16x16x32_bf16 v[216:219], v[56:59], a[144:147], v[216:219]\n"
"ds_read_b128 v[88:91], v0 offset:52224\n"
"v_lshlrev_b32 v132, 16, v252\n"
"v_mfma_f32_16x16x32_bf16 v[216:219], v[60:63], a[152:155], v[216:219]\n"
"ds_read_b128 v[92:95], v0 offset:53248\n"
"v_and_b32 v133, 0xffff0000, v252\n"
"v_mfma_f32_16x16x32_bf16 v[220:223], v[60:63], a[156:159], v[220:223]\n"
"v_lshlrev_b32 v134, 16, v253\n"
"v_and_b32 v135, 0xffff0000, v253\n"
"v_mfma_f32_16x16x32_bf16 v[220:223], v[56:59], a[148:151], v[220:223]\n"
"s_waitcnt vmcnt(5)\n"
"v_max3_u32 v158, v232, v233, v234\n"
"v_max3_u32 v158, v158, v235, v236\n"
"v_mfma_f32_16x16x32_bf16 v[220:223], v[52:55], a[140:143], v[220:223]\n"
"v_max3_u32 v158, v158, v237, v238\n"
"v_max3_u32 v158, v158, v239, v240\n"
"v_mfma_f32_16x16x32_bf16 v[220:223], v[48:51], a[132:135], v[220:223]\n"
"s_waitcnt lgkmcnt(4)\n"
"v_max3_u32 v158, v158, v241, v242\n"
"v_max3_u32 v158, v158, v243, v244\n"
"v_mfma_f32_16x16x32_bf16 v[216:219], v[64:67], a[32:35], v[216:219]\n"
"ds_read_b128 v[48:51], v0 offset:54272\n"
"v_max3_u32 v158, v158, v245, v246\n"
"v_mfma_f32_16x16x32_bf16 v[216:219], v[68:71], a[40:43], v[216:219]\n"
"ds_read_b128 v[52:55], v0 offset:55296\n"
"v_max_u32 v158, v158, v247\n"
"v_mfma_f32_16x16x32_bf16 v[216:219], v[72:75], a[48:51], v[216:219]\n"
"ds_read_b128 v[56:59], v0 offset:56320\n"
"v_cmp_eq_u32 vcc, -1, v158\n"
"v_mfma_f32_16x16x32_bf16 v[216:219], v[76:79], a[56:59], v[216:219]\n"
"ds_read_b128 v[60:63], v0 offset:57344\n"
"buffer_load_dwordx4 v[40:43], v0, s[16:19], s61 offen offset:0 sc1\n"
"v_mfma_f32_16x16x32_bf16 v[220:223], v[76:79], a[60:63], v[220:223]\n"
"s_cbranch_vccnz L_sr0\n"
"L_sr0x:\n"
"v_mfma_f32_16x16x32_bf16 v[160:163], v[154:157], v[232:235], 0\n"
"v_mfma_f32_16x16x32_bf16 v[220:223], v[72:75], a[52:55], v[220:223]\n"
"buffer_load_dwordx4 v[44:47], v0, s[16:19], s61 offen offset:1024 sc1\n"
"v_mfma_f32_16x16x32_bf16 v[164:167], v[152:155], v[232:235], 0\n"
"v_mfma_f32_16x16x32_bf16 v[220:223], v[68:71], a[44:47], v[220:223]\n"
"v_mfma_f32_16x16x32_bf16 v[160:163], v[154:157], v[236:239], v[160:163]\n"
"v_mfma_f32_16x16x32_bf16 v[220:223], v[64:67], a[36:39], v[220:223]\n"
"s_waitcnt lgkmcnt(4)\n"
"v_mfma_f32_16x16x32_bf16 v[164:167], v[152:155], v[236:239], v[164:167]\n"
"v_mfma_f32_16x16x32_bf16 v[224:227], v[80:83], a[224:227], v[224:227]\n"
"ds_read_b128 v[64:67], v0 offset:58368\n"
"v_mfma_f32_16x16x32_bf16 v[160:163], v[154:157], v[240:243], v[160:163]\n"
"v_mfma_f32_16x16x32_bf16 v[224:227], v[84:87], a[232:235], v[224:227]\n"
"ds_read_b128 v[68:71], v0 offset:59392\n"
"v_mfma_f32_16x16x32_bf16 v[164:167], v[152:155], v[240:243], v[164:167]\n"
"v_mfma_f32_16x16x32_bf16 v[224:227], v[88:91], a[240:243], v[224:227]\n"
"ds_read_b128 v[72:75], v0 offset:60416\n"
"v_mfma_f32_16x16x32_bf16 v[160:163], v[154:157], v[244:247], v[160:163]\n"
"v_mfma_f32_16x16x32_bf16 v[224:227], v[92:95], a[248:251], v[224:227]\n"
"ds_read_b128 v[76:79], v0 offset:61440\n"
"v_mfma_f32_16x16x32_bf16 v[164:167], v[152:155], v[244:247], v[164:167]\n"
"v_mfma_f32_16x16x32_bf16 v[228:231], v[92:95], a[252:255], v[228:231]\n"
"v_mfma_f32_16x16x32_bf16 v[228:231], v[88:91], a[244:247], v[228:231]\n"
"v_mfma_f32_16x16x32_bf16 v[228:231], v[84:87], a[236:239], v[228:231]\n"
"v_fma_f32 v128, v160, v144, v128\n"
"v_fma_f32 v129, v161, v145, v129\n"
"v_mfma_f32_16x16x32_bf16 v[228:231], v[80:83], a[228:231], v[228:231]\n"
"s_waitcnt lgkmcnt(4)\n"
"v_fma_f32 v130, v162, v146, v130\n"
"v_fma_f32 v131, v163, v147, v131\n"
"v_mfma_f32_16x16x32_bf16 v[224:227], v[48:51], a[192:195], v[224:227]\n"
"ds_read_b128 v[80:83], v0 offset:62464\n"
"ds_read_b128 v[84:87], v0 offset:63488\n"
"v_mfma_f32_16x16x32_bf16 v[224:227], v[52:55], a[200:203], v[224:227]\n"
"ds_read_b128 v[88:91], v0 offset:64512\n"
"ds_read_b128 v[92:95], v1 offset:0\n"
"v_mfma_f32_16x16x32_bf16 v[224:227], v[56:59], a[208:211], v[224:227]\n"
"v_fma_f32 v132, v164, v148, v132\n"
"v_fma_f32 v133, v165, v149, v133\n"
"v_mfma_f32_16x16x32_bf16 v[224:227], v[60:63], a[216:219], v[224:227]\n"
"v_fma_f32 v134, v166, v150, v134\n"
"v_fma_f32 v135, v167, v151, v135\n"
"v_mfma_f32_16x16x32_bf16 v[228:231], v[60:63], a[220:223], v[228:231]\n"
"v_cvt_pk_bf16_f32 v128, v128, v129\n"
"v_cvt_pk_bf16_f32 v129, v130, v131\n"
"v_mfma_f32_16x16x32_bf16 v[228:231], v[56:59], a[212:215], v[228:231]\n"
"buffer_store_dwordx2 v[128:129], v3, s[20:23], s62 offen offset:0\n"
"v_cvt_pk_bf16_f32 v132, v132, v133\n"
"v_mfma_f32_16x16x32_bf16 v[228:231], v[52:55], a[204:207], v[228:231]\n"
"v_cvt_pk_bf16_f32 v133, v134, v135\n"
"v_mfma_f32_16x16x32_bf16 v[228:231], v[48:51], a[196:199], v[228:231]\n"
"s_waitcnt vmcnt(13) lgkmcnt(0)\n"
"buffer_store_dwordx2 v[132:133], v3, s[20:23], s65 offen offset:0\n"
"v_mfma_f32_16x16x32_bf16 v[224:227], v[64:67], a[0:3], v[224:227]\n"
"s_barrier\n"
"ds_read_b128 v[48:51], v2 offset:10240\n"
"v_mfma_f32_16x16x32_bf16 v[224:227], v[68:71], a[8:11], v[224:227]\n"
"s_waitcnt vmcnt(5)\n"
"ds_read_b128 v[52:55], v2 offset:11264\n"
"v_max3_u32 v158, v32, v33, v34\n"
"v_mfma_f32_16x16x32_bf16 v[224:227], v[72:75], a[16:19], v[224:227]\n"
"ds_read_b128 v[56:59], v2 offset:12288\n"
"v_max3_u32 v158, v158, v35, v36\n"
"v_mfma_f32_16x16x32_bf16 v[224:227], v[76:79], a[24:27], v[224:227]\n"
"ds_read_b128 v[60:63], v2 offset:13312\n"
"v_max3_u32 v158, v158, v37, v38\n"
"v_mfma_f32_16x16x32_bf16 v[228:231], v[76:79], a[28:31], v[228:231]\n"
"v_max_u32 v158, v158, v39\n"
"v_cmp_eq_u32 vcc, -1, v158\n"
"v_mfma_f32_16x16x32_bf16 v[228:231], v[72:75], a[20:23], v[228:231]\n"
"v_mfma_f32_16x16x32_bf16 v[228:231], v[68:71], a[12:15], v[228:231]\n"
"s_cbranch_vccnz L_sr1\n"
"L_sr1x:\n"
"v_mfma_f32_16x16x32_bf16 v[96:99], v[154:157], v[32:35], 0\n"
"v_mfma_f32_16x16x32_bf16 v[228:231], v[64:67], a[4:7], v[228:231]\n"
"s_waitcnt lgkmcnt(4)\n"
"v_mfma_f32_16x16x32_bf16 v[100:103], v[152:155], v[32:35], 0\n"
"v_mfma_f32_16x16x32_bf16 v[224:227], v[80:83], a[128:131], v[224:227]\n"
"v_mfma_f32_16x16x32_bf16 v[104:107], v[154:157], v[36:39], 0\n"
"v_mfma_f32_16x16x32_bf16 v[224:227], v[84:87], a[136:139], v[224:227]\n"
"v_mfma_f32_16x16x32_bf16 v[108:111], v[152:155], v[36:39], 0\n"
"v_mfma_f32_16x16x32_bf16 v[224:227], v[88:91], a[144:147], v[224:227]\n"
"v_mfma_f32_16x16x32_bf16 v[224:227], v[92:95], a[152:155], v[224:227]\n"
"v_mfma_f32_16x16x32_bf16 v[228:231], v[92:95], a[156:159], v[228:231]\n"
"v_mfma_f32_16x16x32_bf16 v[228:231], v[88:91], a[148:151], v[228:231]\n"
"buffer_load_dwordx4 v[136:139], v0, s[16:19], s61 offen offset:2048 sc1\n"
"v_mfma_f32_16x16x32_bf16 v[228:231], v[84:87], a[140:143], v[228:231]\n"
"v_mfma_f32_16x16x32_bf16 v[228:231], v[80:83], a[132:135], v[228:231]\n"
"s_waitcnt lgkmcnt(0)\n"
"buffer_load_dwordx4 v[140:143], v0, s[16:19], s61 offen offset:3072 sc1\n"
"v_mfma_f32_16x16x32_bf16 v[224:227], v[48:51], a[32:35], v[224:227]\n"
"v_mfma_f32_16x16x32_bf16 v[224:227], v[52:55], a[40:43], v[224:227]\n"
"v_mfma_f32_16x16x32_bf16 v[224:227], v[56:59], a[48:51], v[224:227]\n"
"v_mfma_f32_16x16x32_bf16 v[224:227], v[60:63], a[56:59], v[224:227]\n"
"v_mfma_f32_16x16x32_bf16 v[228:231], v[60:63], a[60:63], v[228:231]\n"
"v_mfma_f32_16x16x32_bf16 v[228:231], v[56:59], a[52:55], v[228:231]\n"
"v_mfma_f32_16x16x32_bf16 v[228:231], v[52:55], a[44:47], v[228:231]\n"
"v_mfma_f32_16x16x32_bf16 v[228:231], v[48:51], a[36:39], v[228:231]\n"
"s_waitcnt vmcnt(4)\n"
"v_max3_u32 v158, v40, v41, v42\n"
"v_max3_u32 v158, v158, v43, v44\n"
"v_max3_u32 v158, v158, v45, v46\n"
"v_max_u32 v158, v158, v47\n"
"v_cmp_eq_u32 vcc, -1, v158\n"
"s_nop 1\n"
"s_cbranch_vccnz L_sr2\n"
"L_sr2x:\n"
"v_mfma_f32_16x16x32_bf16 v[96:99], v[154:157], v[40:43], v[96:99]\n"
"v_mfma_f32_16x16x32_bf16 v[100:103], v[152:155], v[40:43], v[100:103]\n"
"v_mfma_f32_16x16x32_bf16 v[104:107], v[154:157], v[44:47], v[104:107]\n"
"v_mfma_f32_16x16x32_bf16 v[108:111], v[152:155], v[44:47], v[108:111]\n"
"v_fma_f32 v112, v216, v8, v112\n"
"v_fma_f32 v113, v217, v9, v113\n"
"v_fma_f32 v114, v218, v10, v114\n"
"v_fma_f32 v115, v219, v11, v115\n"
"v_fma_f32 v116, v220, v12, v116\n"
"v_fma_f32 v117, v221, v13, v117\n"
"v_fma_f32 v118, v222, v14, v118\n"
"v_fma_f32 v119, v223, v15, v119\n"
"v_fma_f32 v120, v224, v16, v120\n"
"v_fma_f32 v121, v225, v17, v121\n"
"v_fma_f32 v122, v226, v18, v122\n"
"v_fma_f32 v123, v227, v19, v123\n"
"v_fma_f32 v124, v228, v20, v124\n"
"v_fma_f32 v125, v229, v21, v125\n"
"v_fma_f32 v126, v230, v22, v126\n"
"v_fma_f32 v127, v231, v23, v127\n"
"v_fma_f32 v112, v96, v8, v112\n"
"v_fma_f32 v113, v97, v9, v113\n"
"v_fma_f32 v114, v98, v10, v114\n"
"v_fma_f32 v115, v99, v11, v115\n"
"v_fma_f32 v116, v100, v12, v116\n"
"v_fma_f32 v117, v101, v13, v117\n"
"v_fma_f32 v118, v102, v14, v118\n"
"v_fma_f32 v119, v103, v15, v119\n"
"v_fma_f32 v120, v104, v16, v120\n"
"v_fma_f32 v121, v105, v17, v121\n"
"v_fma_f32 v122, v106, v18, v122\n"
"v_fma_f32 v123, v107, v19, v123\n"
"v_fma_f32 v124, v108, v20, v124\n"
"v_fma_f32 v125, v109, v21, v125\n"
"v_fma_f32 v126, v110, v22, v126\n"
"v_fma_f32 v127, v111, v23, v127\n"
"s_waitcnt vmcnt(0)\n"
"v_max3_u32 v158, v136, v137, v138\n"
"v_max3_u32 v158, v158, v139, v140\n"
"v_max3_u32 v158, v158, v141, v142\n"
"v_max_u32 v158, v158, v143\n"
"v_cmp_eq_u32 vcc, -1, v158\n"
"s_nop 1\n"
"s_cbranch_vccnz L_sr3\n"
"L_sr3x:\n"
"v_lshlrev_b32 v192, 16, v136\n"
"v_and_b32 v193, 0xffff0000, v136\n"
"v_lshlrev_b32 v194, 16, v137\n"
"v_and_b32 v195, 0xffff0000, v137\n"
"v_fma_f32 v112, v192, v8, v112\n"
"v_fma_f32 v113, v193, v9, v113\n"
"v_fma_f32 v114, v194, v10, v114\n"
"v_fma_f32 v115, v195, v11, v115\n"
"v_cvt_pk_bf16_f32 v208, v112, v113\n"
"v_cvt_pk_bf16_f32 v209, v114, v115\n"
"v_lshlrev_b32 v200, 16, v140\n"
"v_and_b32 v201, 0xffff0000, v140\n"
"v_lshlrev_b32 v202, 16, v141\n"
"v_and_b32 v203, 0xffff0000, v141\n"
"v_fma_f32 v120, v200, v16, v120\n"
"v_fma_f32 v121, v201, v17, v121\n"
"v_fma_f32 v122, v202, v18, v122\n"
"v_fma_f32 v123, v203, v19, v123\n"
"v_cvt_pk_bf16_f32 v210, v120, v121\n"
"v_cvt_pk_bf16_f32 v211, v122, v123\n"
"v_lshlrev_b32 v196, 16, v138\n"
"v_and_b32 v197, 0xffff0000, v138\n"
"v_lshlrev_b32 v198, 16, v139\n"
"v_and_b32 v199, 0xffff0000, v139\n"
"v_permlane16_swap_b32 v208, v210\n"
"v_permlane16_swap_b32 v209, v211\n"
"buffer_store_dwordx4 v[208:211], v254, s[20:23], s98 offen\n"
"v_fma_f32 v116, v196, v12, v116\n"
"v_fma_f32 v117, v197, v13, v117\n"
"v_fma_f32 v118, v198, v14, v118\n"
"v_fma_f32 v119, v199, v15, v119\n"
"v_cvt_pk_bf16_f32 v212, v116, v117\n"
"v_cvt_pk_bf16_f32 v213, v118, v119\n"
"v_lshlrev_b32 v204, 16, v142\n"
"v_and_b32 v205, 0xffff0000, v142\n"
"v_lshlrev_b32 v206, 16, v143\n"
"v_and_b32 v207, 0xffff0000, v143\n"
"v_fma_f32 v124, v204, v20, v124\n"
"v_fma_f32 v125, v205, v21, v125\n"
"v_fma_f32 v126, v206, v22, v126\n"
"v_fma_f32 v127, v207, v23, v127\n"
"v_cvt_pk_bf16_f32 v214, v124, v125\n"
"v_cvt_pk_bf16_f32 v215, v126, v127\n"
"s_nop 1\n"
"v_permlane16_swap_b32 v212, v214\n"
"v_permlane16_swap_b32 v213, v215\n"
"buffer_store_dwordx4 v[212:215], v254, s[20:23], s99 offen\n"
"s_endpgm\n"
"L_hook:\n"
"s_cmp_eq_u32 s62, 13\n"
"s_cbranch_scc1 L_h13\n"
"s_cmp_eq_u32 s62, 14\n"
"s_cbranch_scc1 L_h14\n"
"s_waitcnt vmcnt(0)\n"
"s_barrier\n"
"s_cmp_eq_u32 s52, 0\n"
"s_cbranch_scc0 L_hret\n"
"s_lshl_b32 s34, s6, 3\n"
"s_add_u32 s34, s34, 4096\n"
"v_mov_b32 v30, s100\n"
"v_mov_b32 v31, s101\n"
"s_mov_b64 exec, 1\n"
"buffer_store_dwordx2 v[30:31], v0, s[8:11], s34 offen sc1\n"
"s_mov_b64 exec, -1\n"
"s_branch L_hret\n"
"L_h13:\n"
"s_barrier\n"
"v_lshrrev_b32 v30, 1, v0\n"
"s_lshl_b32 s34, s52, 9\n"
"s_add_u32 s34, s34, 4096\n"
"buffer_load_dwordx2 v[30:31], v30, s[8:11], s34 offen sc1\n"
"s_branch L_hret\n"
"L_h14:\n"
"s_mov_b32 s35, 1048576\n"
"L_h14a:\n"
"v_cmp_ne_u64 vcc, s[100:101], v[30:31]\n"
"s_nop 1\n"
"s_cbranch_vccz L_h14b\n"
"s_sleep 1\n"
"v_lshrrev_b32 v30, 1, v0\n"
"s_lshl_b32 s34, s52, 9\n"
"s_add_u32 s34, s34, 4096\n"
"buffer_load_dwordx2 v[30:31], v30, s[8:11], s34 offen sc1\n"
"s_sub_u32 s35, s35, 1\n"
"s_waitcnt vmcnt(0)\n"
"s_cbranch_scc0 L_h14a\n"
"L_h14b:\n"
"s_barrier\n"
"s_cmp_eq_u32 s52, 0\n"
"s_cbranch_scc0 L_hr2\n"
"s_cmp_eq_u32 s6, 0\n"
"s_cbranch_scc0 L_hr2\n"
"v_mov_b32 v30, s100\n"
"v_mov_b32 v31, s101\n"
"s_mov_b64 exec, 1\n"
"buffer_store_dwordx2 v[30:31], v0, s[8:11], 0 offen sc1\n"
"s_mov_b64 exec, -1\n"
"s_branch L_hr2\n"
"L_os:\n"
"s_mov_b32 s35, 1048576\n"
"L_osa:\n"
"v_cmp_ne_u64 vcc, s[100:101], v[30:31]\n"
"s_nop 1\n"
"s_cbranch_vccz L_osb\n"
"s_sleep 1\n"
"v_bfe_u32 v30, v0, 4, 2\n"
"v_lshlrev_b32 v30, 3, v30\n"
"buffer_load_dwordx2 v[30:31], v30, s[8:11], s64 offen offset:0 sc1\n"
"s_sub_u32 s35, s35, 1\n"
"s_waitcnt vmcnt(0)\n"
"s_cbranch_scc0 L_osa\n"
"L_osb:\n"
"s_branch L_osr\n"
"L_ot:\n"
"s_mov_b32 s35, 1048576\n"
"L_ota:\n"
"v_cmp_ne_u64 vcc, s[100:101], v[30:31]\n"
"s_nop 1\n"
"s_cbranch_vccz L_otb\n"
"s_sleep 1\n"
"v_bfe_u32 v30, v0, 4, 2\n"
"v_lshlrev_b32 v30, 3, v30\n"
"buffer_load_dwordx2 v[30:31], v30, s[8:11], s64 offen offset:32 sc1\n"
"s_sub_u32 s35, s35, 1\n"
"s_waitcnt vmcnt(0)\n"
"s_cbranch_scc0 L_ota\n"
"L_otb:\n"
"s_branch L_otr\n"
"L_po:\n"
"s_lshr_b32 s34, s6, 5\n"
"s_lshl_b32 s34, s34, 5\n"
"s_add_u32 s34, s34, s53\n"
"s_lshl_b32 s34, s34, 3\n"
"s_add_u32 s34, s34, 8192\n"
"v_mov_b32 v30, s100\n"
"v_mov_b32 v31, s101\n"
"s_mov_b64 exec, 1\n"
"buffer_store_dwordx2 v[30:31], v0, s[8:11], s34 offen sc1\n"
"s_mov_b64 exec, -1\n"
"s_branch L_por\n"
"L_gr:\n"
"s_mov_b32 s35, 1048576\n"
"L_grl:\n"
"buffer_load_dwordx4 v[96:99], v0, s[16:19], s80 offen offset:0 sc1\n"
"buffer_load_dwordx4 v[100:103], v0, s[16:19], s80 offen offset:1024 sc1\n"
"buffer_load_dwordx4 v[104:107], v0, s[16:19], s80 offen offset:2048 sc1\n"
"buffer_load_dwordx4 v[108:111], v0, s[16:19], s80 offen offset:3072 sc1\n"
"buffer_load_dwordx4 v[112:115], v0, s[16:19], 0 offen offset:0 sc1\n"
"buffer_load_dwordx4 v[116:119], v0, s[16:19], 0 offen offset:1024 sc1\n"
"buffer_load_dwordx4 v[120:123], v0, s[16:19], 0 offen offset:2048 sc1\n"
"buffer_load_dwordx4 v[124:127], v0, s[16:19], 0 offen offset:3072 sc1\n"
"buffer_load_dwordx4 v[128:131], v0, s[16:19], s60 offen offset:0 sc1\n"
"buffer_load_dwordx4 v[132:135], v0, s[16:19], s60 offen offset:1024 sc1\n"
"s_sub_u32 s35, s35, 1\n"
"s_waitcnt vmcnt(0)\n"
"s_cbranch_scc1 L_grx\n"
"v_max3_u32 v158, v96, v97, v98\n"
"v_max3_u32 v158, v158, v99, v100\n"
"v_max3_u32 v158, v158, v101, v102\n"
"v_max3_u32 v158, v158, v103, v104\n"
"v_max3_u32 v158, v158, v105, v106\n"
"v_max3_u32 v158, v158, v107, v108\n"
"v_max3_u32 v158, v158, v109, v110\n"
"v_max3_u32 v158, v158, v111, v112\n"
"v_max3_u32 v158, v158, v113, v114\n"
"v_max3_u32 v158, v158, v115, v116\n"
"v_max3_u32 v158, v158, v117, v118\n"
"v_max3_u32 v158, v158, v119, v120\n"
"v_max3_u32 v158, v158, v121, v122\n"
"v_max3_u32 v158, v158, v123, v124\n"
"v_max3_u32 v158, v158, v125, v126\n"
"v_max3_u32 v158, v158, v127, v128\n"
"v_max3_u32 v158, v158, v129, v130\n"
"v_max3_u32 v158, v158, v131, v132\n"
"v_max3_u32 v158, v158, v133, v134\n"
"v_max_u32 v158, v158, v135\n"
"v_cmp_eq_u32 vcc, -1, v158\n"
"s_nop 1\n"
"s_cbranch_vccnz L_grl\n"
"s_branch L_grx\n"
"L_sr0:\n"
"s_mov_b32 s35, 1048576\n"
"L_sr0l:\n"
"buffer_load_dwordx4 v[232:235], v0, s[16:19], s81 offen offset:0 sc1\n"
"buffer_load_dwordx4 v[236:239], v0, s[16:19], s81 offen offset:1024 sc1\n"
"buffer_load_dwordx4 v[240:243], v0, s[16:19], s81 offen offset:2048 sc1\n"
"buffer_load_dwordx4 v[244:247], v0, s[16:19], s81 offen offset:3072 sc1\n"
"s_sub_u32 s35, s35, 1\n"
"s_waitcnt vmcnt(0)\n"
"s_cbranch_scc1 L_sr0x\n"
"v_max3_u32 v158, v232, v233, v234\n"
"v_max3_u32 v158, v158, v235, v236\n"
"v_max3_u32 v158, v158, v237, v238\n"
"v_max3_u32 v158, v158, v239, v240\n"
"v_max3_u32 v158, v158, v241, v242\n"
"v_max3_u32 v158, v158, v243, v244\n"
"v_max3_u32 v158, v158, v245, v246\n"
"v_max_u32 v158, v158, v247\n"
"v_cmp_eq_u32 vcc, -1, v158\n"
"s_nop 1\n"
"s_cbranch_vccnz L_sr0l\n"
"s_branch L_sr0x\n"
"L_sr1:\n"
"s_mov_b32 s35, 1048576\n"
"L_sr1l:\n"
"buffer_load_dwordx4 v[32:35], v0, s[16:19], s60 offen offset:2048 sc1\n"
"buffer_load_dwordx4 v[36:39], v0, s[16:19], s60 offen offset:3072 sc1\n"
"s_sub_u32 s35, s35, 1\n"
"s_waitcnt vmcnt(0)\n"
"s_cbranch_scc1 L_sr1x\n"
"v_max3_u32 v158, v32, v33, v34\n"
"v_max3_u32 v158, v158, v35, v36\n"
"v_max3_u32 v158, v158, v37, v38\n"
"v_max_u32 v158, v158, v39\n"
"v_cmp_eq_u32 vcc, -1, v158\n"
"s_nop 1\n"
"s_cbranch_vccnz L_sr1l\n"
"s_branch L_sr1x\n"
"L_sr2:\n"
"s_mov_b32 s35, 1048576\n"
"L_sr2l:\n"
"buffer_load_dwordx4 v[40:43], v0, s[16:19], s61 offen offset:0 sc1\n"
"buffer_load_dwordx4 v[44:47], v0, s[16:19], s61 offen offset:1024 sc1\n"
"s_sub_u32 s35, s35, 1\n"
"s_waitcnt vmcnt(0)\n"
"s_cbranch_scc1 L_sr2x\n"
"v_max3_u32 v158, v40, v41, v42\n"
"v_max3_u32 v158, v158, v43, v44\n"
"v_max3_u32 v158, v158, v45, v46\n"
"v_max_u32 v158, v158, v47\n"
"v_cmp_eq_u32 vcc, -1, v158\n"
"s_nop 1\n"
"s_cbranch_vccnz L_sr2l\n"
"s_branch L_sr2x\n"
"L_sr3:\n"
"s_mov_b32 s35, 1048576\n"
"L_sr3l:\n"
"buffer_load_dwordx4 v[136:139], v0, s[16:19], s61 offen offset:2048 sc1\n"
"buffer_load_dwordx4 v[140:143], v0, s[16:19], s61 offen offset:3072 sc1\n"
"s_sub_u32 s35, s35, 1\n"
"s_waitcnt vmcnt(0)\n"
"s_cbranch_scc1 L_sr3x\n"
"v_max3_u32 v158, v136, v137, v138\n"
"v_max3_u32 v158, v158, v139, v140\n"
"v_max3_u32 v158, v158, v141, v142\n"
"v_max_u32 v158, v158, v143\n"
"v_cmp_eq_u32 vcc, -1, v158\n"
"s_nop 1\n"
"s_cbranch_vccnz L_sr3l\n"
"s_branch L_sr3x\n"
"L_fb:\n"
"s_mov_b32 s85, 1\n"
"s_mov_b32 s38, 0\n"
"s_mov_b32 s34, 0\n"
"s_waitcnt vmcnt(0) lgkmcnt(0)\n"
"s_barrier\n"
"s_nop 7\n"
"v_lshrrev_b32 v30, 4, v0\n"
"v_and_b32 v31, 15, v30\n"
"v_and_b32 v30, 48, v30\n"
"v_lshl_add_u32 v1, v31, 8, v30\n"
"s_mov_b32 s36, s70\n"
"s_and_b32 s37, s71, 0xffff\n"
"s_mov_b32 s38, 0x480000\n"
"s_mov_b32 s39, 0x20000\n"
"v_lshrrev_b32 v80, 8, v0\n"
"v_lshlrev_b32 v80, 15, v80\n"
"v_bfe_u32 v81, v0, 6, 2\n"
"v_lshl_add_u32 v80, v81, 4, v80\n"
"v_bfe_u32 v81, v0, 4, 2\n"
"v_lshl_add_u32 v80, v81, 1, v80\n"
"v_add_u32 v81, 8192, v80\n"
"v_add_u32 v82, 16384, v80\n"
"v_add_u32 v83, 24576, v80\n"
"v_add_u32 v84, 131072, v80\n"
"v_add_u32 v85, 139264, v80\n"
"v_add_u32 v86, 147456, v80\n"
"v_add_u32 v87, 155648, v80\n"
"v_mov_b32 v96, 0\n"
"v_mov_b32 v97, 0\n"
"v_mov_b32 v98, 0\n"
"v_mov_b32 v99, 0\n"
"v_mov_b32 v100, 0\n"
"v_mov_b32 v101, 0\n"
"v_mov_b32 v102, 0\n"
"v_mov_b32 v103, 0\n"
"v_mov_b32 v104, 0\n"
"v_mov_b32 v105, 0\n"
"v_mov_b32 v106, 0\n"
"v_mov_b32 v107, 0\n"
"v_mov_b32 v108, 0\n"
"v_mov_b32 v109, 0\n"
"v_mov_b32 v110, 0\n"
"v_mov_b32 v111, 0\n"
"v_mov_b32 v112, 0\n"
"v_mov_b32 v113, 0\n"
"v_mov_b32 v114, 0\n"
"v_mov_b32 v115, 0\n"
"v_mov_b32 v116, 0\n"
"v_mov_b32 v117, 0\n"
"v_mov_b32 v118, 0\n"
"v_mov_b32 v119, 0\n"
"v_mov_b32 v120, 0\n"
"v_mov_b32 v121, 0\n"
"v_mov_b32 v122, 0\n"
"v_mov_b32 v123, 0\n"
"v_mov_b32 v124, 0\n"
"v_mov_b32 v125, 0\n"
"v_mov_b32 v126, 0\n"
"v_mov_b32 v127, 0\n"
"v_mov_b32 v128, 0\n"
"v_mov_b32 v129, 0\n"
"v_mov_b32 v130, 0\n"
"v_mov_b32 v131, 0\n"
"v_mov_b32 v132, 0\n"
"v_mov_b32 v133, 0\n"
"v_mov_b32 v134, 0\n"
"v_mov_b32 v135, 0\n"
"v_mov_b32 v136, 0\n"
"v_mov_b32 v137, 0\n"
"v_mov_b32 v138, 0\n"
"v_mov_b32 v139, 0\n"
"v_mov_b32 v140, 0\n"
"v_mov_b32 v141, 0\n"
"v_mov_b32 v142, 0\n"
"v_mov_b32 v143, 0\n"
"v_mov_b32 v144, 0\n"
"v_mov_b32 v145, 0\n"
"v_mov_b32 v146, 0\n"
"v_mov_b32 v147, 0\n"
"v_mov_b32 v148, 0\n"
"v_mov_b32 v149, 0\n"
"v_mov_b32 v150, 0\n"
"v_mov_b32 v151, 0\n"
"v_mov_b32 v152, 0\n"
"v_mov_b32 v153, 0\n"
"v_mov_b32 v154, 0\n"
"v_mov_b32 v155, 0\n"
"v_mov_b32 v156, 0\n"
"v_mov_b32 v157, 0\n"
"v_mov_b32 v158, 0\n"
"v_mov_b32 v159, 0\n"
"v_mov_b32 v160, 0\n"
"v_mov_b32 v161, 0\n"
"v_mov_b32 v162, 0\n"
"v_mov_b32 v163, 0\n"
"v_mov_b32 v164, 0\n"
"v_mov_b32 v165, 0\n"
"v_mov_b32 v166, 0\n"
"v_mov_b32 v167, 0\n"
"v_mov_b32 v168, 0\n"
"v_mov_b32 v169, 0\n"
"v_mov_b32 v170, 0\n"
"v_mov_b32 v171, 0\n"
"v_mov_b32 v172, 0\n"
"v_mov_b32 v173, 0\n"
"v_mov_b32 v174, 0\n"
"v_mov_b32 v175, 0\n"
"v_mov_b32 v176, 0\n"
"v_mov_b32 v177, 0\n"
"v_mov_b32 v178, 0\n"
"v_mov_b32 v179, 0\n"
"v_mov_b32 v180, 0\n"
"v_mov_b32 v181, 0\n"
"v_mov_b32 v182, 0\n"
"v_mov_b32 v183, 0\n"
"v_mov_b32 v184, 0\n"
"v_mov_b32 v185, 0\n"
"v_mov_b32 v186, 0\n"
"v_mov_b32 v187, 0\n"
"v_mov_b32 v188, 0\n"
"v_mov_b32 v189, 0\n"
"v_mov_b32 v190, 0\n"
"v_mov_b32 v191, 0\n"
"v_mov_b32 v192, 0\n"
"v_mov_b32 v193, 0\n"
"v_mov_b32 v194, 0\n"
"v_mov_b32 v195, 0\n"
"v_mov_b32 v196, 0\n"
"v_mov_b32 v197, 0\n"
"v_mov_b32 v198, 0\n"
"v_mov_b32 v199, 0\n"
"v_mov_b32 v200, 0\n"
"v_mov_b32 v201, 0\n"
"v_mov_b32 v202, 0\n"
"v_mov_b32 v203, 0\n"
"v_mov_b32 v204, 0\n"
"v_mov_b32 v205, 0\n"
"v_mov_b32 v206, 0\n"
"v_mov_b32 v207, 0\n"
"v_mov_b32 v208, 0\n"
"v_mov_b32 v209, 0\n"
"v_mov_b32 v210, 0\n"
"v_mov_b32 v211, 0\n"
"v_mov_b32 v212, 0\n"
"v_mov_b32 v213, 0\n"
"v_mov_b32 v214, 0\n"
"v_mov_b32 v215, 0\n"
"v_mov_b32 v216, 0\n"
"v_mov_b32 v217, 0\n"
"v_mov_b32 v218, 0\n"
"v_mov_b32 v219, 0\n"
"v_mov_b32 v220, 0\n"
"v_mov_b32 v221, 0\n"
"v_mov_b32 v222, 0\n"
"v_mov_b32 v223, 0\n"
"s_xor_b32 s34, s54, 0\n"
"s_mul_i32 s55, s34, 18432\n"
"s_add_u32 s55, s55, 0x10000\n"
"s_xor_b32 s34, s54, 1\n"
"s_mul_i32 s56, s34, 18432\n"
"s_add_u32 s56, s56, 0x10000\n"
"s_xor_b32 s34, s54, 2\n"
"s_mul_i32 s57, s34, 18432\n"
"s_add_u32 s57, s57, 0x10000\n"
"s_xor_b32 s34, s54, 3\n"
"s_mul_i32 s58, s34, 18432\n"
"s_add_u32 s58, s58, 0x10000\n"
"s_lshl_b32 s34, s53, 8\n"
"L_fbq:\n"
"buffer_load_dwordx4 v[64:67], v0, s[8:11], s55 offen sc1\n"
"buffer_load_dwordx4 v[68:71], v0, s[8:11], s56 offen sc1\n"
"buffer_load_dwordx4 v[72:75], v0, s[8:11], s57 offen sc1\n"
"buffer_load_dwordx4 v[76:79], v0, s[8:11], s58 offen sc1\n"
"buffer_load_ushort v224, v80, s[36:39], s34 offen offset:0\n"
"buffer_load_ushort v225, v81, s[36:39], s34 offen offset:0\n"
"buffer_load_ushort v226, v82, s[36:39], s34 offen offset:0\n"
"buffer_load_ushort v227, v83, s[36:39], s34 offen offset:0\n"
"buffer_load_ushort v228, v84, s[36:39], s34 offen offset:0\n"
"buffer_load_ushort v229, v85, s[36:39], s34 offen offset:0\n"
"buffer_load_ushort v230, v86, s[36:39], s34 offen offset:0\n"
"buffer_load_ushort v231, v87, s[36:39], s34 offen offset:0\n"
"buffer_load_ushort v232, v80, s[36:39], s34 offen offset:8\n"
"buffer_load_ushort v233, v81, s[36:39], s34 offen offset:8\n"
"buffer_load_ushort v234, v82, s[36:39], s34 offen offset:8\n"
"buffer_load_ushort v235, v83, s[36:39], s34 offen offset:8\n"
"buffer_load_ushort v236, v84, s[36:39], s34 offen offset:8\n"
"buffer_load_ushort v237, v85, s[36:39], s34 offen offset:8\n"
"buffer_load_ushort v238, v86, s[36:39], s34 offen offset:8\n"
"buffer_load_ushort v239, v87, s[36:39], s34 offen offset:8\n"
"s_waitcnt vmcnt(0)\n"
"v_lshl_or_b32 v240, v225, 16, v224\n"
"v_lshl_or_b32 v241, v227, 16, v226\n"
"v_lshl_or_b32 v242, v229, 16, v228\n"
"v_lshl_or_b32 v243, v231, 16, v230\n"
"v_lshl_or_b32 v244, v233, 16, v232\n"
"v_lshl_or_b32 v245, v235, 16, v234\n"
"v_lshl_or_b32 v246, v237, 16, v236\n"
"v_lshl_or_b32 v247, v239, 16, v238\n"
"s_nop 1\n"
"v_mfma_f32_16x16x32_bf16 v[160:163], v[240:243], v[64:67], v[160:163]\n"
"v_mfma_f32_16x16x32_bf16 v[164:167], v[240:243], v[68:71], v[164:167]\n"
"v_mfma_f32_16x16x32_bf16 v[168:171], v[240:243], v[72:75], v[168:171]\n"
"v_mfma_f32_16x16x32_bf16 v[172:175], v[240:243], v[76:79], v[172:175]\n"
"v_mfma_f32_16x16x32_bf16 v[96:99], v[244:247], v[64:67], v[96:99]\n"
"v_mfma_f32_16x16x32_bf16 v[100:103], v[244:247], v[68:71], v[100:103]\n"
"v_mfma_f32_16x16x32_bf16 v[104:107], v[244:247], v[72:75], v[104:107]\n"
"v_mfma_f32_16x16x32_bf16 v[108:111], v[244:247], v[76:79], v[108:111]\n"
"buffer_load_ushort v224, v80, s[36:39], s34 offen offset:64\n"
"buffer_load_ushort v225, v81, s[36:39], s34 offen offset:64\n"
"buffer_load_ushort v226, v82, s[36:39], s34 offen offset:64\n"
"buffer_load_ushort v227, v83, s[36:39], s34 offen offset:64\n"
"buffer_load_ushort v228, v84, s[36:39], s34 offen offset:64\n"
"buffer_load_ushort v229, v85, s[36:39], s34 offen offset:64\n"
"buffer_load_ushort v230, v86, s[36:39], s34 offen offset:64\n"
"buffer_load_ushort v231, v87, s[36:39], s34 offen offset:64\n"
"buffer_load_ushort v232, v80, s[36:39], s34 offen offset:72\n"
"buffer_load_ushort v233, v81, s[36:39], s34 offen offset:72\n"
"buffer_load_ushort v234, v82, s[36:39], s34 offen offset:72\n"
"buffer_load_ushort v235, v83, s[36:39], s34 offen offset:72\n"
"buffer_load_ushort v236, v84, s[36:39], s34 offen offset:72\n"
"buffer_load_ushort v237, v85, s[36:39], s34 offen offset:72\n"
"buffer_load_ushort v238, v86, s[36:39], s34 offen offset:72\n"
"buffer_load_ushort v239, v87, s[36:39], s34 offen offset:72\n"
"s_waitcnt vmcnt(0)\n"
"v_lshl_or_b32 v240, v225, 16, v224\n"
"v_lshl_or_b32 v241, v227, 16, v226\n"
"v_lshl_or_b32 v242, v229, 16, v228\n"
"v_lshl_or_b32 v243, v231, 16, v230\n"
"v_lshl_or_b32 v244, v233, 16, v232\n"
"v_lshl_or_b32 v245, v235, 16, v234\n"
"v_lshl_or_b32 v246, v237, 16, v236\n"
"v_lshl_or_b32 v247, v239, 16, v238\n"
"s_nop 1\n"
"v_mfma_f32_16x16x32_bf16 v[176:179], v[240:243], v[64:67], v[176:179]\n"
"v_mfma_f32_16x16x32_bf16 v[180:183], v[240:243], v[68:71], v[180:183]\n"
"v_mfma_f32_16x16x32_bf16 v[184:187], v[240:243], v[72:75], v[184:187]\n"
"v_mfma_f32_16x16x32_bf16 v[188:191], v[240:243], v[76:79], v[188:191]\n"
"v_mfma_f32_16x16x32_bf16 v[112:115], v[244:247], v[64:67], v[112:115]\n"
"v_mfma_f32_16x16x32_bf16 v[116:119], v[244:247], v[68:71], v[116:119]\n"
"v_mfma_f32_16x16x32_bf16 v[120:123], v[244:247], v[72:75], v[120:123]\n"
"v_mfma_f32_16x16x32_bf16 v[124:127], v[244:247], v[76:79], v[124:127]\n"
"buffer_load_ushort v224, v80, s[36:39], s34 offen offset:128\n"
"buffer_load_ushort v225, v81, s[36:39], s34 offen offset:128\n"
"buffer_load_ushort v226, v82, s[36:39], s34 offen offset:128\n"
"buffer_load_ushort v227, v83, s[36:39], s34 offen offset:128\n"
"buffer_load_ushort v228, v84, s[36:39], s34 offen offset:128\n"
"buffer_load_ushort v229, v85, s[36:39], s34 offen offset:128\n"
"buffer_load_ushort v230, v86, s[36:39], s34 offen offset:128\n"
"buffer_load_ushort v231, v87, s[36:39], s34 offen offset:128\n"
"buffer_load_ushort v232, v80, s[36:39], s34 offen offset:136\n"
"buffer_load_ushort v233, v81, s[36:39], s34 offen offset:136\n"
"buffer_load_ushort v234, v82, s[36:39], s34 offen offset:136\n"
"buffer_load_ushort v235, v83, s[36:39], s34 offen offset:136\n"
"buffer_load_ushort v236, v84, s[36:39], s34 offen offset:136\n"
"buffer_load_ushort v237, v85, s[36:39], s34 offen offset:136\n"
"buffer_load_ushort v238, v86, s[36:39], s34 offen offset:136\n"
"buffer_load_ushort v239, v87, s[36:39], s34 offen offset:136\n"
"s_waitcnt vmcnt(0)\n"
"v_lshl_or_b32 v240, v225, 16, v224\n"
"v_lshl_or_b32 v241, v227, 16, v226\n"
"v_lshl_or_b32 v242, v229, 16, v228\n"
"v_lshl_or_b32 v243, v231, 16, v230\n"
"v_lshl_or_b32 v244, v233, 16, v232\n"
"v_lshl_or_b32 v245, v235, 16, v234\n"
"v_lshl_or_b32 v246, v237, 16, v236\n"
"v_lshl_or_b32 v247, v239, 16, v238\n"
"s_nop 1\n"
"v_mfma_f32_16x16x32_bf16 v[192:195], v[240:243], v[64:67], v[192:195]\n"
"v_mfma_f32_16x16x32_bf16 v[196:199], v[240:243], v[68:71], v[196:199]\n"
"v_mfma_f32_16x16x32_bf16 v[200:203], v[240:243], v[72:75], v[200:203]\n"
"v_mfma_f32_16x16x32_bf16 v[204:207], v[240:243], v[76:79], v[204:207]\n"
"v_mfma_f32_16x16x32_bf16 v[128:131], v[244:247], v[64:67], v[128:131]\n"
"v_mfma_f32_16x16x32_bf16 v[132:135], v[244:247], v[68:71], v[132:135]\n"
"v_mfma_f32_16x16x32_bf16 v[136:139], v[244:247], v[72:75], v[136:139]\n"
"v_mfma_f32_16x16x32_bf16 v[140:143], v[244:247], v[76:79], v[140:143]\n"
"buffer_load_ushort v224, v80, s[36:39], s34 offen offset:192\n"
"buffer_load_ushort v225, v81, s[36:39], s34 offen offset:192\n"
"buffer_load_ushort v226, v82, s[36:39], s34 offen offset:192\n"
"buffer_load_ushort v227, v83, s[36:39], s34 offen offset:192\n"
"buffer_load_ushort v228, v84, s[36:39], s34 offen offset:192\n"
"buffer_load_ushort v229, v85, s[36:39], s34 offen offset:192\n"
"buffer_load_ushort v230, v86, s[36:39], s34 offen offset:192\n"
"buffer_load_ushort v231, v87, s[36:39], s34 offen offset:192\n"
"buffer_load_ushort v232, v80, s[36:39], s34 offen offset:200\n"
"buffer_load_ushort v233, v81, s[36:39], s34 offen offset:200\n"
"buffer_load_ushort v234, v82, s[36:39], s34 offen offset:200\n"
"buffer_load_ushort v235, v83, s[36:39], s34 offen offset:200\n"
"buffer_load_ushort v236, v84, s[36:39], s34 offen offset:200\n"
"buffer_load_ushort v237, v85, s[36:39], s34 offen offset:200\n"
"buffer_load_ushort v238, v86, s[36:39], s34 offen offset:200\n"
"buffer_load_ushort v239, v87, s[36:39], s34 offen offset:200\n"
"s_waitcnt vmcnt(0)\n"
"v_lshl_or_b32 v240, v225, 16, v224\n"
"v_lshl_or_b32 v241, v227, 16, v226\n"
"v_lshl_or_b32 v242, v229, 16, v228\n"
"v_lshl_or_b32 v243, v231, 16, v230\n"
"v_lshl_or_b32 v244, v233, 16, v232\n"
"v_lshl_or_b32 v245, v235, 16, v234\n"
"v_lshl_or_b32 v246, v237, 16, v236\n"
"v_lshl_or_b32 v247, v239, 16, v238\n"
"s_nop 1\n"
"v_mfma_f32_16x16x32_bf16 v[208:211], v[240:243], v[64:67], v[208:211]\n"
"v_mfma_f32_16x16x32_bf16 v[212:215], v[240:243], v[68:71], v[212:215]\n"
"v_mfma_f32_16x16x32_bf16 v[216:219], v[240:243], v[72:75], v[216:219]\n"
"v_mfma_f32_16x16x32_bf16 v[220:223], v[240:243], v[76:79], v[220:223]\n"
"v_mfma_f32_16x16x32_bf16 v[144:147], v[244:247], v[64:67], v[144:147]\n"
"v_mfma_f32_16x16x32_bf16 v[148:151], v[244:247], v[68:71], v[148:151]\n"
"v_mfma_f32_16x16x32_bf16 v[152:155], v[244:247], v[72:75], v[152:155]\n"
"v_mfma_f32_16x16x32_bf16 v[156:159], v[244:247], v[76:79], v[156:159]\n"
"s_add_u32 s55, s55, 1024\n"
"s_add_u32 s56, s56, 1024\n"
"s_add_u32 s57, s57, 1024\n"
"s_add_u32 s58, s58, 1024\n"
"s_add_u32 s34, s34, 262144\n"
"s_cmp_lt_u32 s34, 4718592\n"
"s_cbranch_scc1 L_fbq\n"
"s_nop 7\n"
"v_cvt_pk_bf16_f32 v160, v160, v161\n"
"v_cvt_pk_bf16_f32 v161, v162, v163\n"
"v_cvt_pk_bf16_f32 v164, v164, v165\n"
"v_cvt_pk_bf16_f32 v165, v166, v167\n"
"v_cvt_pk_bf16_f32 v168, v168, v169\n"
"v_cvt_pk_bf16_f32 v169, v170, v171\n"
"v_cvt_pk_bf16_f32 v172, v172, v173\n"
"v_cvt_pk_bf16_f32 v173, v174, v175\n"
"v_cvt_pk_bf16_f32 v162, v96, v97\n"
"v_cvt_pk_bf16_f32 v163, v98, v99\n"
"v_cvt_pk_bf16_f32 v166, v100, v101\n"
"v_cvt_pk_bf16_f32 v167, v102, v103\n"
"v_cvt_pk_bf16_f32 v170, v104, v105\n"
"v_cvt_pk_bf16_f32 v171, v106, v107\n"
"v_cvt_pk_bf16_f32 v174, v108, v109\n"
"v_cvt_pk_bf16_f32 v175, v110, v111\n"
"v_cvt_pk_bf16_f32 v176, v176, v177\n"
"v_cvt_pk_bf16_f32 v177, v178, v179\n"
"v_cvt_pk_bf16_f32 v180, v180, v181\n"
"v_cvt_pk_bf16_f32 v181, v182, v183\n"
"v_cvt_pk_bf16_f32 v184, v184, v185\n"
"v_cvt_pk_bf16_f32 v185, v186, v187\n"
"v_cvt_pk_bf16_f32 v188, v188, v189\n"
"v_cvt_pk_bf16_f32 v189, v190, v191\n"
"v_cvt_pk_bf16_f32 v178, v112, v113\n"
"v_cvt_pk_bf16_f32 v179, v114, v115\n"
"v_cvt_pk_bf16_f32 v182, v116, v117\n"
"v_cvt_pk_bf16_f32 v183, v118, v119\n"
"v_cvt_pk_bf16_f32 v186, v120, v121\n"
"v_cvt_pk_bf16_f32 v187, v122, v123\n"
"v_cvt_pk_bf16_f32 v190, v124, v125\n"
"v_cvt_pk_bf16_f32 v191, v126, v127\n"
"v_cvt_pk_bf16_f32 v192, v192, v193\n"
"v_cvt_pk_bf16_f32 v193, v194, v195\n"
"v_cvt_pk_bf16_f32 v196, v196, v197\n"
"v_cvt_pk_bf16_f32 v197, v198, v199\n"
"v_cvt_pk_bf16_f32 v200, v200, v201\n"
"v_cvt_pk_bf16_f32 v201, v202, v203\n"
"v_cvt_pk_bf16_f32 v204, v204, v205\n"
"v_cvt_pk_bf16_f32 v205, v206, v207\n"
"v_cvt_pk_bf16_f32 v194, v128, v129\n"
"v_cvt_pk_bf16_f32 v195, v130, v131\n"
"v_cvt_pk_bf16_f32 v198, v132, v133\n"
"v_cvt_pk_bf16_f32 v199, v134, v135\n"
"v_cvt_pk_bf16_f32 v202, v136, v137\n"
"v_cvt_pk_bf16_f32 v203, v138, v139\n"
"v_cvt_pk_bf16_f32 v206, v140, v141\n"
"v_cvt_pk_bf16_f32 v207, v142, v143\n"
"v_cvt_pk_bf16_f32 v208, v208, v209\n"
"v_cvt_pk_bf16_f32 v209, v210, v211\n"
"v_cvt_pk_bf16_f32 v212, v212, v213\n"
"v_cvt_pk_bf16_f32 v213, v214, v215\n"
"v_cvt_pk_bf16_f32 v216, v216, v217\n"
"v_cvt_pk_bf16_f32 v217, v218, v219\n"
"v_cvt_pk_bf16_f32 v220, v220, v221\n"
"v_cvt_pk_bf16_f32 v221, v222, v223\n"
"v_cvt_pk_bf16_f32 v210, v144, v145\n"
"v_cvt_pk_bf16_f32 v211, v146, v147\n"
"v_cvt_pk_bf16_f32 v214, v148, v149\n"
"v_cvt_pk_bf16_f32 v215, v150, v151\n"
"v_cvt_pk_bf16_f32 v218, v152, v153\n"
"v_cvt_pk_bf16_f32 v219, v154, v155\n"
"v_cvt_pk_bf16_f32 v222, v156, v157\n"
"v_cvt_pk_bf16_f32 v223, v158, v159\n"
"s_mov_b32 s38, 0\n"
"s_mov_b32 s34, 0\n"
"v_mov_b32 v8, 0xff800000\n"
"v_mov_b32 v12, 0xff800000\n"
"v_mov_b32 v16, 0xff800000\n"
"v_mov_b32 v20, 0xff800000\n"
"L_fbl:\n"
"buffer_load_dwordx4 a[0:3], v1, s[24:27], s34 offen offset:0\n"
"buffer_load_dwordx4 a[4:7], v1, s[24:27], s34 offen offset:64\n"
"buffer_load_dwordx4 a[8:11], v1, s[24:27], s34 offen offset:128\n"
"buffer_load_dwordx4 a[12:15], v1, s[24:27], s34 offen offset:192\n"
"s_waitcnt vmcnt(0)\n"
"v_mfma_f32_16x16x32_bf16 v[128:131], a[0:3], v[160:163], 0\n"
"v_mfma_f32_16x16x32_bf16 v[128:131], a[4:7], v[176:179], v[128:131]\n"
"v_mfma_f32_16x16x32_bf16 v[128:131], a[8:11], v[192:195], v[128:131]\n"
"v_mfma_f32_16x16x32_bf16 v[128:131], a[12:15], v[208:211], v[128:131]\n"
"v_mfma_f32_16x16x32_bf16 v[132:135], a[0:3], v[164:167], 0\n"
"v_mfma_f32_16x16x32_bf16 v[132:135], a[4:7], v[180:183], v[132:135]\n"
"v_mfma_f32_16x16x32_bf16 v[132:135], a[8:11], v[196:199], v[132:135]\n"
"v_mfma_f32_16x16x32_bf16 v[132:135], a[12:15], v[212:215], v[132:135]\n"
"v_mfma_f32_16x16x32_bf16 v[136:139], a[0:3], v[168:171], 0\n"
"v_mfma_f32_16x16x32_bf16 v[136:139], a[4:7], v[184:187], v[136:139]\n"
"v_mfma_f32_16x16x32_bf16 v[136:139], a[8:11], v[200:203], v[136:139]\n"
"v_mfma_f32_16x16x32_bf16 v[136:139], a[12:15], v[216:219], v[136:139]\n"
"v_mfma_f32_16x16x32_bf16 v[140:143], a[0:3], v[172:175], 0\n"
"v_mfma_f32_16x16x32_bf16 v[140:143], a[4:7], v[188:191], v[140:143]\n"
"v_mfma_f32_16x16x32_bf16 v[140:143], a[8:11], v[204:207], v[140:143]\n"
"v_mfma_f32_16x16x32_bf16 v[140:143], a[12:15], v[220:223], v[140:143]\n"
"s_nop 7\n"
"v_max3_f32 v8, v8, v128, v129\n"
"v_max3_f32 v8, v8, v130, v131\n"
"v_max3_f32 v12, v12, v132, v133\n"
"v_max3_f32 v12, v12, v134, v135\n"
"v_max3_f32 v16, v16, v136, v137\n"
"v_max3_f32 v16, v16, v138, v139\n"
"v_max3_f32 v20, v20, v140, v141\n"
"v_max3_f32 v20, v20, v142, v143\n"
"s_add_u32 s34, s34, 4096\n"
"s_cmp_lt_u32 s34, 524288\n"
"s_cbranch_scc1 L_fbl\n"
"v_mov_b32 v30, v8\n"
"s_nop 1\n"
"v_permlane32_swap_b32 v8, v30\n"
"v_max_f32 v8, v8, v30\n"
"v_mov_b32 v30, v8\n"
"s_nop 1\n"
"v_permlane16_swap_b32 v8, v30\n"
"v_min_f32 v9, -v8, -v30\n"
"v_min_f32 v10, -v8, -v30\n"
"v_min_f32 v11, -v8, -v30\n"
"v_min_f32 v8, -v8, -v30\n"
"v_mov_b32 v31, v12\n"
"s_nop 1\n"
"v_permlane32_swap_b32 v12, v31\n"
"v_max_f32 v12, v12, v31\n"
"v_mov_b32 v31, v12\n"
"s_nop 1\n"
"v_permlane16_swap_b32 v12, v31\n"
"v_min_f32 v13, -v12, -v31\n"
"v_min_f32 v14, -v12, -v31\n"
"v_min_f32 v15, -v12, -v31\n"
"v_min_f32 v12, -v12, -v31\n"
"v_mov_b32 v30, v16\n"
"s_nop 1\n"
"v_permlane32_swap_b32 v16, v30\n"
"v_max_f32 v16, v16, v30\n"
"v_mov_b32 v30, v16\n"
"s_nop 1\n"
"v_permlane16_swap_b32 v16, v30\n"
"v_min_f32 v17, -v16, -v30\n"
"v_min_f32 v18, -v16, -v30\n"
"v_min_f32 v19, -v16, -v30\n"
"v_min_f32 v16, -v16, -v30\n"
"v_mov_b32 v31, v20\n"
"s_nop 1\n"
"v_permlane32_swap_b32 v20, v31\n"
"v_max_f32 v20, v20, v31\n"
"v_mov_b32 v31, v20\n"
"s_nop 1\n"
"v_permlane16_swap_b32 v20, v31\n"
"v_min_f32 v21, -v20, -v31\n"
"v_min_f32 v22, -v20, -v31\n"
"v_min_f32 v23, -v20, -v31\n"
"v_min_f32 v20, -v20, -v31\n"
"s_lshl_b32 s80, s52, 12\n"
"s_add_u32 m0, s92, 141312\n"
"s_nop 0\n"
"buffer_load_dwordx4 v0, s[24:27], s80 offen lds\n"
"s_add_u32 m0, m0, 32\n"
"s_nop 0\n"
"buffer_load_dwordx4 v0, s[24:27], s80 offen offset:1024 lds\n"
"s_add_u32 m0, m0, 32\n"
"s_nop 0\n"
"buffer_load_dwordx4 v0, s[24:27], s80 offen offset:2048 lds\n"
"s_add_u32 m0, m0, 32\n"
"s_nop 0\n"
"buffer_load_dwordx4 v0, s[24:27], s80 offen offset:3072 lds\n"
"s_lshl_b32 s81, s52, 12\n"
"s_add_u32 m0, s92, 73728\n"
"s_nop 0\n"
"buffer_load_dwordx4 v0, s[28:31], s81 offen lds\n"
"s_add_u32 m0, m0, 32\n"
"s_nop 0\n"
"buffer_load_dwordx4 v0, s[28:31], s81 offen offset:1024 lds\n"
"s_add_u32 m0, m0, 32\n"
"s_nop 0\n"
"buffer_load_dwordx4 v0, s[28:31], s81 offen offset:2048 lds\n"
"s_add_u32 m0, m0, 32\n"
"s_nop 0\n"
"buffer_load_dwordx4 v0, s[28:31], s81 offen offset:3072 lds\n"
"s_add_u32 s81, s81, 16384\n"
"s_add_u32 m0, s92, 90624\n"
"s_nop 0\n"
"buffer_load_dwordx4 v0, s[28:31], s81 offen lds\n"
"s_add_u32 m0, m0, 32\n"
"s_nop 0\n"
"buffer_load_dwordx4 v0, s[28:31], s81 offen offset:1024 lds\n"
"s_add_u32 m0, m0, 32\n"
"s_nop 0\n"
"buffer_load_dwordx4 v0, s[28:31], s81 offen offset:2048 lds\n"
"s_add_u32 m0, m0, 32\n"
"s_nop 0\n"
"buffer_load_dwordx4 v0, s[28:31], s81 offen offset:3072 lds\n"
"s_add_u32 s80, s80, 32768\n"
"s_add_u32 m0, s92, 107520\n"
"s_nop 0\n"
"buffer_load_dwordx4 v0, s[24:27], s80 offen lds\n"
"s_add_u32 m0, m0, 32\n"
"s_nop 0\n"
"buffer_load_dwordx4 v0, s[24:27], s80 offen offset:1024 lds\n"
"s_add_u32 m0, m0, 32\n"
"s_nop 0\n"
"buffer_load_dwordx4 v0, s[24:27], s80 offen offset:2048 lds\n"
"s_add_u32 m0, m0, 32\n"
"s_nop 0\n"
"buffer_load_dwordx4 v0, s[24:27], s80 offen offset:3072 lds\n"
"s_add_u32 s80, s80, 16384\n"
"s_add_u32 m0, s92, 124416\n"
"s_nop 0\n"
"buffer_load_dwordx4 v0, s[24:27], s80 offen lds\n"
"s_add_u32 m0, m0, 32\n"
"s_nop 0\n"
"buffer_load_dwordx4 v0, s[24:27], s80 offen offset:1024 lds\n"
"s_add_u32 m0, m0, 32\n"
"s_nop 0\n"
"buffer_load_dwordx4 v0, s[24:27], s80 offen offset:2048 lds\n"
"s_add_u32 m0, m0, 32\n"
"s_nop 0\n"
"buffer_load_dwordx4 v0, s[24:27], s80 offen offset:3072 lds\n"
"s_sub_u32 s59, s80, 32768\n"
"s_add_u32 m0, s92, 0\n"
"s_nop 0\n"
"buffer_load_dwordx4 v0, s[24:27], s59 offen lds\n"
"s_add_u32 m0, m0, 32\n"
"s_nop 0\n"
"buffer_load_dwordx4 v0, s[24:27], s59 offen offset:1024 lds\n"
"s_add_u32 m0, m0, 32\n"
"s_nop 0\n"
"buffer_load_dwordx4 v0, s[24:27], s59 offen offset:2048 lds\n"
"s_add_u32 m0, m0, 32\n"
"s_nop 0\n"
"buffer_load_dwordx4 v0, s[24:27], s59 offen offset:3072 lds\n"
"v_add_u32 v24, 73728, v24\n"
"v_add_u32 v25, 73728, v25\n"
"s_waitcnt vmcnt(0)\n"
"s_barrier\n"
"ds_read_b128 a[0:3], v26 offset:0\n"
"ds_read_b128 a[4:7], v26 offset:64\n"
"ds_read_b128 a[8:11], v26 offset:128\n"
"ds_read_b128 a[12:15], v26 offset:192\n"
"ds_read_b128 a[16:19], v26 offset:256\n"
"ds_read_b128 a[20:23], v26 offset:320\n"
"ds_read_b128 a[24:27], v26 offset:384\n"
"ds_read_b128 a[28:31], v26 offset:448\n"
"ds_read_b128 a[32:35], v26 offset:512\n"
"ds_read_b128 a[36:39], v26 offset:576\n"
"ds_read_b128 a[40:43], v26 offset:640\n"
"ds_read_b128 a[44:47], v26 offset:704\n"
"ds_read_b128 a[48:51], v26 offset:768\n"
"ds_read_b128 a[52:55], v26 offset:832\n"
"ds_read_b128 a[56:59], v26 offset:896\n"
"ds_read_b128 a[60:63], v26 offset:960\n"
"v_bfe_u32 v2, v0, 4, 1\n"
"v_mul_u32_u24 v2, 0xaaaa, v2\n"
"v_add_u32 v2, 0x4444, v2\n"
"s_waitcnt lgkmcnt(0)\n"
"s_branch L_a0\n"
"L_end:\n"
 :: "{s[4:5]}"(__builtin_amdgcn_kernarg_segment_ptr()), "{s6}"(blockIdx.x), "{v0}"(threadIdx.x), "s"((unsigned)(size_t)lds) : "v1","v2","v3","v4","v5","v6","v7","v8","v9","v10","v11","v12","v13","v14","v15","v16","v17","v18","v19","v20","v21","v22","v23","v24","v25","v26","v27","v28","v29","v30","v31","v32","v33","v34","v35","v36","v37","v38","v39","v40","v41","v42","v43","v44","v45","v46","v47","v48","v49","v50","v51","v52","v53","v54","v55","v56","v57","v58","v59","v60","v61","v62","v63","v64","v65","v66","v67","v68","v69","v70","v71","v72","v73","v74","v75","v76","v77","v78","v79","v80","v81","v82","v83","v84","v85","v86","v87","v88","v89","v90","v91","v92","v93","v94","v95","v96","v97","v98","v99","v100","v101","v102","v103","v104","v105","v106","v107","v108","v109","v110","v111","v112","v113","v114","v115","v116","v117","v118","v119","v120","v121","v122","v123","v124","v125","v126","v127","v128","v129","v130","v131","v132","v133","v134","v135","v136","v137","v138","v139","v140","v141","v142","v143","v144","v145","v146","v147","v148","v149","v150","v151","v152","v153","v154","v155","v156","v157","v158","v159","v160","v161","v162","v163","v164","v165","v166","v167","v168","v169","v170","v171","v172","v173","v174","v175","v176","v177","v178","v179","v180","v181","v182","v183","v184","v185","v186","v187","v188","v189","v190","v191","v192","v193","v194","v195","v196","v197","v198","v199","v200","v201","v202","v203","v204","v205","v206","v207","v208","v209","v210","v211","v212","v213","v214","v215","v216","v217","v218","v219","v220","v221","v222","v223","v224","v225","v226","v227","v228","v229","v230","v231","v232","v233","v234","v235","v236","v237","v238","v239","v240","v241","v242","v243","v244","v245","v246","v247","v248","v249","v250","v251","v252","v253","v254","v255","a0","a1","a2","a3","a4","a5","a6","a7","a8","a9","a10","a11","a12","a13","a14","a15","a16","a17","a18","a19","a20","a21","a22","a23","a24","a25","a26","a27","a28","a29","a30","a31","a32","a33","a34","a35","a36","a37","a38","a39","a40","a41","a42","a43","a44","a45","a46","a47","a48","a49","a50","a51","a52","a53","a54","a55","a56","a57","a58","a59","a60","a61","a62","a63","a64","a65","a66","a67","a68","a69","a70","a71","a72","a73","a74","a75","a76","a77","a78","a79","a80","a81","a82","a83","a84","a85","a86","a87","a88","a89","a90","a91","a92","a93","a94","a95","a96","a97","a98","a99","a100","a101","a102","a103","a104","a105","a106","a107","a108","a109","a110","a111","a112","a113","a114","a115","a116","a117","a118","a119","a120","a121","a122","a123","a124","a125","a126","a127","a128","a129","a130","a131","a132","a133","a134","a135","a136","a137","a138","a139","a140","a141","a142","a143","a144","a145","a146","a147","a148","a149","a150","a151","a152","a153","a154","a155","a156","a157","a158","a159","a160","a161","a162","a163","a164","a165","a166","a167","a168","a169","a170","a171","a172","a173","a174","a175","a176","a177","a178","a179","a180","a181","a182","a183","a184","a185","a186","a187","a188","a189","a190","a191","a192","a193","a194","a195","a196","a197","a198","a199","a200","a201","a202","a203","a204","a205","a206","a207","a208","a209","a210","a211","a212","a213","a214","a215","a216","a217","a218","a219","a220","a221","a222","a223","a224","a225","a226","a227","a228","a229","a230","a231","a232","a233","a234","a235","a236","a237","a238","a239","a240","a241","a242","a243","a244","a245","a246","a247","a248","a249","a250","a251","a252","a253","a254","a255","s7","s8","s9","s10","s11","s12","s13","s14","s15","s16","s17","s18","s19","s20","s21","s22","s23","s24","s25","s26","s27","s28","s29","s30","s31","s33","s34","s35","s36","s37","s38","s39","s40","s41","s42","s43","s44","s45","s46","s47","s48","s49","s50","s51","s52","s53","s54","s55","s56","s57","s58","s59","s60","s61","s62","s63","s64","s65","s66","s67","s68","s69","s70","s71","s72","s73","s74","s75","s76","s77","s78","s79","s80","s81","s82","s83","s84","s85","s86","s87","s88","s89","s90","s91","s92","s93","s94","s95","s96","s97","s98","s99","s100","s101","vcc","scc","memory");
}

extern "C" void kernel_launch(void* const* d_in,const int* in_sizes,int n_in,void* d_out,int out_size,void* d_ws,size_t ws_size,hipStream_t stream){
  kf<<<256,256,0,stream>>>(d_ws,d_in[1],d_in[2],d_in[3],d_in[6],d_in[7],d_in[4],d_in[8],d_in[0],d_out);
}
```
